```python
import torch

def _softmax(x, axis=-1):
    return torch.softmax(x, dim=axis)
def _gelu(x):
    return torch.nn.functional.gelu(x, approximate="tanh")
def _silu(x):
    return torch.nn.functional.silu(x)
def _sigmoid(x):
    return torch.sigmoid(x)
def _layernorm(x, axis=-1, eps=1e-5):
    m = x.mean(dim=axis, keepdim=True)
    v = x.var(dim=axis, keepdim=True, unbiased=False)
    return (x - m) / torch.sqrt(v + eps)
def _scatter_add(tgt, idx, src, axis=0):
    return tgt.clone().index_add_(axis, idx, src)
def _segment_sum(src, seg, n):
    out = torch.zeros((n,) + src.shape[1:], dtype=src.dtype)
    return out.index_add_(0, seg, src)
def _pad(x, pw):
    flat = tuple(v for pair in reversed(pw) for v in pair)
    return torch.nn.functional.pad(x, flat)

kernel_name = 'parallel_sum__ssm_conv_s4__moe_arch_mod__prompt'

def setup_inputs():

    _w = (lambda a: (lambda t: t.to(torch.bfloat16) if t.is_floating_point() else t)(torch.as_tensor(a)))
    def _A_setup():
        B, L, D, N = 1, 2048, 512, 128
        A_re = -(lambda _x: torch.exp(torch.as_tensor(_x)))((lambda *s: torch.randn(*s, dtype=torch.bfloat16) * 3.2)(D, N))
        A_im = torch.pi * (lambda *s: torch.randn(*s, dtype=torch.bfloat16) * 0.1)(D, N)
        B_re = (lambda *s: torch.randn(*s, dtype=torch.bfloat16) * 0.4)(D, N)
        B_im = (lambda *s: torch.randn(*s, dtype=torch.bfloat16) * 0.4)(D, N)
        C_re = (lambda *s: torch.randn(*s, dtype=torch.bfloat16) * 0.4)(D, N)
        C_im = (lambda *s: torch.randn(*s, dtype=torch.bfloat16) * 0.4)(D, N)
        log_dt = (lambda *s: torch.randn(*s, dtype=torch.bfloat16) * 0.1)(D) * 0.5 - 4.0
        Dskip = (lambda *s: torch.randn(*s, dtype=torch.bfloat16) * 0.1)(D)
        return {
            "u": _w((lambda *s: torch.randn(*s, dtype=torch.bfloat16) * 0.05)(B, L, D)),
            "A_re": _w(A_re),
            "A_im": _w(A_im),
            "B_re": _w(B_re),
            "B_im": _w(B_im),
            "C_re": _w(C_re),
            "C_im": _w(C_im),
            "log_dt": _w(log_dt),
            "Dskip": _w(Dskip),
        }
    _A_in = _A_setup()

    def _B_setup():
        B, S, D, F, C = 1, 2048, 512, 1792, 2
        return {
            "x": _w((lambda *s: torch.randn(*s, dtype=torch.bfloat16) * 0.05)(B, S, D)),
            "w_r": _w((lambda *s: torch.randn(*s, dtype=torch.bfloat16) * 0.05)(D)),
            "w_gate": _w((lambda *s: torch.randn(*s, dtype=torch.bfloat16) * 0.05)(D, F)),
            "w_up": _w((lambda *s: torch.randn(*s, dtype=torch.bfloat16) * 0.05)(D, F)),
            "w_down": _w((lambda *s: torch.randn(*s, dtype=torch.bfloat16) * 0.05)(F, D)),
        }
    _B_in = _B_setup()

    return {
        "A_u": _A_in["u"],
        "A_A_re": _A_in["A_re"],
        "A_A_im": _A_in["A_im"],
        "A_B_re": _A_in["B_re"],
        "A_B_im": _A_in["B_im"],
        "A_C_re": _A_in["C_re"],
        "A_C_im": _A_in["C_im"],
        "A_log_dt": _A_in["log_dt"],
        "A_Dskip": _A_in["Dskip"],
        "B_x": _B_in["x"],
        "B_w_r": _B_in["w_r"],
        "B_w_gate": _B_in["w_gate"],
        "B_w_up": _B_in["w_up"],
        "B_w_down": _B_in["w_down"],
    }

def reference(A_u, A_A_re, A_A_im, A_B_re, A_B_im, A_C_re, A_C_im, A_log_dt, A_Dskip, B_x, B_w_r, B_w_gate, B_w_up, B_w_down):
    def _A_ref(u, A_re, A_im, B_re, B_im, C_re, C_im, log_dt, Dskip):
        Bsz, L, D = u.shape
        dt = torch.exp(log_dt)[:, None]
        A = A_re + 1j * A_im
        Bc = B_re + 1j * B_im
        Cc = C_re + 1j * C_im
        logdA = dt * A
        dB = dt * Bc
        l = (lambda x, dt: x.to(dt))(torch.arange(L), A_re.dtype)
        P = torch.exp(logdA[:, :, None] * l[None, None, :])
        K = torch.einsum("dn,dnl,dn->dl", Cc, P, dB).real
        L2 = 2 * L
        Kp = _pad(K, ((0, 0), (0, L)))
        up = (lambda x, *d: x.permute(*d))(u, 0, 2, 1)
        up = _pad(up, ((0, 0), (0, 0), (0, L)))
        Kf = (lambda x, axes=(-2,-1): torch.fft.rfft2(x.float(), dim=axes))(Kp[:, None, :], axes=(-2, -1))
        Uf = (lambda x, axes=(-2,-1): torch.fft.rfft2(x.float(), dim=axes))(up[:, :, None, :], axes=(-2, -1))
        Yf = Uf * Kf[None, :, :, :]
        y = (lambda x, s, axes=(-2,-1): torch.fft.irfft2(x, s=s, dim=axes).to(torch.bfloat16))(Yf, (1, L2), axes=(-2, -1))[:, :, 0, :L]
        y = (lambda x, *d: x.permute(*d))(y, 0, 2, 1) + u * Dskip[None, None, :]
        return y

    def _B_ref(x, w_r, w_gate, w_up, w_down):
        B, S, D = x.shape
        C = 2
        r = x @ w_r
        idx = torch.argsort(-r, axis=-1)[:, :C]
        idx_e = (lambda x, n, axis: torch.repeat_interleave(x, n, dim=axis))(idx.reshape(B, C, 1), D, axis=2)
        x_sel = (lambda x, i, axis: torch.take_along_dim(x, i, dim=axis))(x, idx_e, axis=1)
        g = _silu(x_sel @ w_gate)
        u = x_sel @ w_up
        y = (g * u) @ w_down
        r_sel = _sigmoid((lambda x, i, axis: torch.take_along_dim(x, i, dim=axis))(r, idx, axis=1))
        y = y * r_sel.reshape(B, C, 1)
        b_off = (torch.arange(B) * S).reshape(B, 1)
        flat_idx = (idx + b_off).reshape(B * C)
        zero = (x * 0.0).reshape(B * S, D)
        upd = _scatter_add(zero, flat_idx, y.reshape(B * C, D), axis=0)
        return x + upd.reshape(B, S, D)

    x = A_u
    xn = _layernorm(x, axis=-1)
    return x + _A_ref(xn, A_A_re, A_A_im, A_B_re, A_B_im, A_C_re, A_C_im, A_log_dt, A_Dskip) + _B_ref(xn, B_w_r, B_w_gate, B_w_up, B_w_down)
```

```cpp
#include <hip/hip_runtime.h>
#include <hip/hip_bf16.h>
#include <stdint.h>

typedef unsigned short u16;
#define T_LEN 2048
#define D_DIM 512
#define N_ST 128
#define F_DIM 1792

__device__ __forceinline__ float bf2f(u16 v) { return __uint_as_float(((unsigned)v) << 16); }
__device__ __forceinline__ u16 f2bf(float f) {
  unsigned u = __float_as_uint(f);
  u += 0x7FFF + ((u >> 16) & 1);
  return (u16)(u >> 16);
}
__device__ __forceinline__ float rbf(float f) { return bf2f(f2bf(f)); }

__global__ void k_ln(const u16* x, const u16* wr, u16* xn, float* r) {
  int t = blockIdx.x; int lane = threadIdx.x;
  float v[8]; float s = 0;
  for (int j = 0; j < 8; j++) { v[j] = bf2f(x[t * D_DIM + lane * 8 + j]); s += v[j]; }
  for (int o = 32; o; o >>= 1) s += __shfl_xor(s, o);
  float mean = s / D_DIM;
  float q = 0;
  for (int j = 0; j < 8; j++) { float d = v[j] - mean; q += d * d; }
  for (int o = 32; o; o >>= 1) q += __shfl_xor(q, o);
  float var = rbf(q / D_DIM);
  float mb = rbf(mean);
  float sd = rbf(sqrtf(rbf(var + 1e-5f)));
  float acc = 0;
  for (int j = 0; j < 8; j++) {
    float c = rbf(v[j] - mb);
    float n = rbf(c / sd);
    xn[t * D_DIM + lane * 8 + j] = f2bf(n);
    acc += n * bf2f(wr[lane * 8 + j]);
  }
  for (int o = 32; o; o >>= 1) acc += __shfl_xor(acc, o);
  if (lane == 0) r[t] = rbf(acc);
}

__global__ void k_gen(const u16* Are, const u16* Aim, const u16* Bre, const u16* Bim, const u16* Cre,
                      const u16* Cim, const u16* logdt, float* K) {
  int d = blockIdx.x;
  __shared__ float sa[N_ST], sw[N_ST], cr[N_ST], ci[N_ST];
  float dt = rbf(expf(bf2f(logdt[d])));
  if (threadIdx.x < N_ST) {
    int n = threadIdx.x; int i = d * N_ST + n;
    sa[n] = dt * bf2f(Are[i]); sw[n] = dt * bf2f(Aim[i]);
    float br = dt * bf2f(Bre[i]), bi = dt * bf2f(Bim[i]);
    float c_r = bf2f(Cre[i]), c_i = bf2f(Cim[i]);
    cr[n] = c_r * br - c_i * bi; ci[n] = c_r * bi + c_i * br;
  }
  __syncthreads();
  for (int l = threadIdx.x; l < T_LEN; l += blockDim.x) {
    float lf = rbf((float)l);
    float acc = 0;
    for (int n = 0; n < N_ST; n++) {
      float e = expf(sa[n] * lf);
      float s, c; sincosf(sw[n] * lf, &s, &c);
      acc += e * (cr[n] * c - ci[n] * s);
    }
    K[d * T_LEN + l] = acc;
  }
}

__global__ void k_top_ffn1(const float* r, const u16* xn, const u16* wg, const u16* wu, float* h, int* sel) {
  __shared__ int s_idx[2];
  if (threadIdx.x == 0) {
    float b0 = -INFINITY, b1 = -INFINITY; int i0 = 0, i1 = 1;
    for (int t = 0; t < T_LEN; t++) {
      float v = r[t];
      if (v > b0) { b1 = b0; i1 = i0; b0 = v; i0 = t; }
      else if (v > b1) { b1 = v; i1 = t; }
    }
    s_idx[0] = i0; s_idx[1] = i1;
    if (blockIdx.x == 0) { sel[0] = i0; sel[1] = i1; }
  }
  __syncthreads();
  int f = blockIdx.x * blockDim.x + threadIdx.x;
  if (f >= F_DIM) return;
  for (int c = 0; c < 2; c++) {
    float g = 0, u = 0;
    for (int d = 0; d < D_DIM; d++) {
      float xv = bf2f(xn[s_idx[c] * D_DIM + d]);
      g += xv * bf2f(wg[d * F_DIM + f]);
      u += xv * bf2f(wu[d * F_DIM + f]);
    }
    g = rbf(g); u = rbf(u);
    float sg = rbf(g / (1.f + expf(-g)));
    h[c * F_DIM + f] = rbf(sg * u);
  }
}

__global__ void k_ffn2(const float* h, const u16* wd, const float* r, const int* sel, float* upd) {
  int d = blockIdx.x * blockDim.x + threadIdx.x;
  if (d >= D_DIM) return;
  for (int c = 0; c < 2; c++) {
    float y = 0;
    for (int f = 0; f < F_DIM; f++) y += h[c * F_DIM + f] * bf2f(wd[f * D_DIM + d]);
    y = rbf(y);
    float rs = rbf(1.f / (1.f + expf(-r[sel[c]])));
    upd[c * D_DIM + d] = rbf(y * rs);
  }
}

__global__ void k_conv(const u16* x, const u16* xn, const float* K, const u16* dskip, const int* sel,
                       const float* upd, u16* out) {
  int d = blockIdx.x;
  __shared__ float su[T_LEN], sk[T_LEN];
  for (int t = threadIdx.x; t < T_LEN; t += blockDim.x) { su[t] = bf2f(xn[t * D_DIM + d]); sk[t] = K[d * T_LEN + t]; }
  __syncthreads();
  float ds = bf2f(dskip[d]);
  int s0 = sel[0], s1 = sel[1];
  for (int t = threadIdx.x; t < T_LEN; t += blockDim.x) {
    float acc = 0;
    for (int l = 0; l <= t; l++) acc += sk[l] * su[t - l];
    float y = rbf(acc);
    float a = rbf(y + rbf(su[t] * ds));
    float b = su[t];
    if (t == s0) b = rbf(b + upd[d]);
    if (t == s1) b = rbf(b + upd[D_DIM + d]);
    float o = rbf(rbf(bf2f(x[t * D_DIM + d]) + a) + b);
    out[t * D_DIM + d] = f2bf(o);
  }
}

extern "C" void kernel_launch(void* const* d_in, const int* in_sizes, int n_in, void* d_out, int out_size,
                              void* d_ws, size_t ws_size, hipStream_t stream) {
  const u16* x = (const u16*)d_in[0];
  char* ws = (char*)d_ws;
  u16* xn = (u16*)ws;
  float* K = (float*)(ws + (2 << 20));
  float* r = (float*)(ws + (6 << 20));
  float* h = (float*)(ws + (6 << 20) + 8192);
  float* upd = (float*)(ws + (6 << 20) + 8192 + 16384);
  int* sel = (int*)(ws + (6 << 20) + 8192 + 16384 + 4096);
  k_ln<<<T_LEN, 64, 0, stream>>>(x, (const u16*)d_in[10], xn, r);
  k_gen<<<D_DIM, 256, 0, stream>>>((const u16*)d_in[1], (const u16*)d_in[2], (const u16*)d_in[3], (const u16*)d_in[4],
                                   (const u16*)d_in[5], (const u16*)d_in[6], (const u16*)d_in[7], K);
  k_top_ffn1<<<F_DIM / 64, 64, 0, stream>>>(r, xn, (const u16*)d_in[11], (const u16*)d_in[12], h, sel);
  k_ffn2<<<D_DIM / 64, 64, 0, stream>>>(h, (const u16*)d_in[13], r, sel, upd);
  k_conv<<<D_DIM, 256, 0, stream>>>(x, xn, K, (const u16*)d_in[8], sel, upd, (u16*)d_out);
}
```

```cpp
#include <hip/hip_runtime.h>

typedef unsigned short u16;
typedef unsigned u32;
typedef unsigned long long u64;
typedef u32 v4u __attribute__((ext_vector_type(4)));
typedef u32 v2u __attribute__((ext_vector_type(2)));
typedef float v4f __attribute__((ext_vector_type(4)));
typedef float v2f __attribute__((ext_vector_type(2)));
typedef __bf16 v8bf __attribute__((ext_vector_type(8)));
typedef __bf16 v2bf __attribute__((ext_vector_type(2)));
typedef short v4s __attribute__((ext_vector_type(4)));
#define LDS __attribute__((address_space(3)))
#define GLB __attribute__((address_space(1)))
#define DEVI __device__ __forceinline__

#define WS_XT 0
#define WS_KT (4u << 20)
#define WS_XROW (6u << 20)
#define WS_CAND (8u << 20)
#define WS_PART ((8u << 20) + 65536)
#define NFFN 112
#define SENT 0xFFFFFFFFu

#define STAMP(k, i)

DEVI u32 pk(float a, float b) { return __builtin_bit_cast(u32, __builtin_convertvector((v2f){a, b}, v2bf)); }
DEVI float rbf(float a) { return __uint_as_float(pk(0.f, a)); }
DEVI float lo2f(u32 v) { return __uint_as_float(v << 16); }
DEVI float hi2f(u32 v) { return __uint_as_float(v & 0xffff0000u); }
template <int CTRL, int RM> DEVI float dppf(float v) {
  return __int_as_float(__builtin_amdgcn_update_dpp(0, __float_as_int(v), CTRL, RM, 0xf, false)); }
template <int CTRL> DEVI u32 dppu(u32 v) { return (u32)__builtin_amdgcn_update_dpp(0, (int)v, CTRL, 0xf, 0xf, false); }
DEVI float wave_sum(float v) {
  v += dppf<0xB1, 0xf>(v); v += dppf<0x4E, 0xf>(v); v += dppf<0x141, 0xf>(v); v += dppf<0x140, 0xf>(v);
  v += dppf<0x142, 0xa>(v); v += dppf<0x143, 0xc>(v);
  return __int_as_float(__builtin_amdgcn_readlane(__float_as_int(v), 63));
}
DEVI void bar() { asm volatile("s_waitcnt lgkmcnt(0)\n\ts_barrier" ::: "memory"); }
DEVI v4s trd(LDS char* p) { return __builtin_amdgcn_ds_read_tr16_b64_v4i16((LDS v4s*)p); }
DEVI v8bf cat(v4s a, v4s b) { return __builtin_bit_cast(v8bf, __builtin_shufflevector(a, b, 0, 1, 2, 3, 4, 5, 6, 7)); }
DEVI void lds_wait(LDS char* p, u32 n) {
  while (*(volatile LDS u32*)p < n) __builtin_amdgcn_s_sleep(1);
  asm volatile("" ::: "memory");
}
DEVI void lds_sig(LDS char* p, int lane) {
  asm volatile("s_waitcnt lgkmcnt(0)" ::: "memory");
  if (lane == 0) __hip_atomic_fetch_add((LDS u32*)p, 1u, __ATOMIC_RELAXED, __HIP_MEMORY_SCOPE_WORKGROUP);
}

struct cf { float r, i; };
DEVI cf cmul(cf a, cf b) { return {a.r * b.r - a.i * b.i, a.r * b.i + a.i * b.r}; }
DEVI cf cpow(float er, float ph, float s) {
  float e = __builtin_amdgcn_exp2f(er * s), f = __builtin_amdgcn_fractf(ph * s);
  return {e * __builtin_amdgcn_cosf(f), e * __builtin_amdgcn_sinf(f)};
}
#define GROW 528
#define GCH (63 * GROW)
template <int N> DEVI void chain(LDS char* p, int row, cf x, cf m) {
#pragma unroll
  for (int j = 0; j < N; j++) { *(LDS u32*)(p + (row + j) * GROW) = pk(x.r, x.i); if (j + 1 < N) x = cmul(x, m); }
}

#define K1_LDS (2 * GCH)
__global__ void __launch_bounds__(512) k1(const u16* __restrict__ x, const u16* __restrict__ wr, const u16* __restrict__ Are,
    const u16* __restrict__ Aim, const u16* __restrict__ Bre, const u16* __restrict__ Bim, const u16* __restrict__ Cre,
    const u16* __restrict__ Cim, const u16* __restrict__ logdt, char* __restrict__ ws) {
  __shared__ __attribute__((aligned(16))) char smem[K1_LDS];
  LDS char* L = (LDS char*)smem;
  const int lane = threadIdx.x & 63, w = __builtin_amdgcn_readfirstlane(threadIdx.x >> 6);
  const int b = blockIdx.x & 255, sw = (b & 7) * 32 + (b >> 3);
  STAMP(0, 0);
  if (blockIdx.x < 256) {
    const int t = sw * 8 + w;
    v4u xr = *(const v4u*)(x + t * 512 + lane * 8);
    v4u wv = *(const v4u*)(wr + lane * 8);
    if (w < 2 && threadIdx.x < NFFN) *(v4u*)(ws + WS_PART + (b * NFFN + threadIdx.x) * 16) = (v4u){SENT, SENT, SENT, SENT};
    float v[8];
#pragma unroll
    for (int j = 0; j < 4; j++) { v[2 * j] = lo2f(xr[j]); v[2 * j + 1] = hi2f(xr[j]); }
    float s = ((v[0] + v[1]) + (v[2] + v[3])) + ((v[4] + v[5]) + (v[6] + v[7]));
    STAMP(0, 1);
    float mean = wave_sum(s) * (1.f / 512.f);
    float q = 0.f;
#pragma unroll
    for (int j = 0; j < 8; j++) { float d = v[j] - mean; q += d * d; }
    float var = rbf(wave_sum(q) * (1.f / 512.f));
    float mb = rbf(mean);
    float rs = __builtin_amdgcn_rcpf(rbf(__builtin_amdgcn_sqrtf(rbf(var + 1e-5f))));
    v4u xn; float acc = 0.f;
#pragma unroll
    for (int j = 0; j < 4; j++) {
      u32 xj = pk(rbf(v[2 * j] - mb) * rs, rbf(v[2 * j + 1] - mb) * rs), wj = wv[j];
      xn[j] = xj;
      acc += lo2f(xj) * lo2f(wj) + hi2f(xj) * hi2f(wj);
    }
    *(LDS v4u*)(L + w * 2112 + lane * 32) = (v4u){xn[0], xr[0], xn[1], xr[1]};
    *(LDS v4u*)(L + w * 2112 + lane * 32 + 16) = (v4u){xn[2], xr[2], xn[3], xr[3]};
    u32 rb = __float_as_uint(rbf(wave_sum(acc)));
    u32 key = ((rb & 0x80000000u) ? (~rb & 0xffff0000u) : (rb | 0x80000000u)) | (u32)(2047 - t);
    if (lane == 0) *(LDS u32*)(L + 17408 + w * 4) = key;
    STAMP(0, 2);
    bar();
    {
      LDS char* p = L + ((lane >> 2) & 3) * 2112 + w * 256 + (lane >> 4) * 32 + (lane & 3) * 8;
      v4s a0 = trd(p), a1 = trd(p + 4 * 2112), b0 = trd(p + 128), b1 = trd(p + 4 * 2112 + 128);
      char* dst = ws + WS_XT + (size_t)(w * 32 + (lane >> 2)) * 16384 + sw * 64 + (lane & 3) * 16;
      *(v8bf*)dst = cat(a0, a1);
      *(v8bf*)(dst + 16 * 16384) = cat(b0, b1);
    }
    v4u k0 = *(LDS v4u*)(L + 17408), k1v = *(LDS v4u*)(L + 17424);
    int rank = 0;
#pragma unroll
    for (int j = 0; j < 4; j++) rank += (k0[j] > key) + (k1v[j] > key);
    if (rank < 2) {
      *(v4u*)(ws + WS_XROW + t * 1024 + lane * 16) = xn;
      if (lane == 0) *(u32*)(ws + WS_CAND + b * 8 + rank * 4) = key;
    }
    STAMP(0, 3);
  } else {
    const int ch = w >> 2, d = sw * 2 + ch;
    {
      const int n = ((w >> 1) & 1) * 64 + lane, idx = d * 128 + n;
      float are = lo2f(Are[idx]), aim = lo2f(Aim[idx]), bre = lo2f(Bre[idx]), bim = lo2f(Bim[idx]), cre = lo2f(Cre[idx]), cim = lo2f(Cim[idx]);
      float dt = rbf(expf(lo2f(logdt[d])));
      float er = dt * are * 1.44269504089f, ph = dt * aim * 0.15915494309f;
      STAMP(0, 1);
      cf cb = cmul({cre, cim}, {dt * bre, dt * bim});
      cf E = cpow(er, ph, 256.f);
      LDS char* p = L + ch * GCH + n * 4;
      if (w & 1) {
        cf z4 = cpow(er, ph, 4.f), z8 = cmul(z4, z4), z16 = cmul(z8, z8);
        cf xx = cmul(cb, cpow(er, ph, 768.f));
        chain<5>(p, 30, xx, z4);
#pragma unroll
        for (int a = 0; a < 4; a++) { xx = cmul(xx, E); chain<3>(p, 35 + 3 * a, xx, z8); }
        z16.i = -z16.i;
        *(LDS u32*)(p + 47 * GROW) = 0x3f80u;
        chain<15>(p, 48, z16, z16);
      } else {
        cf z = cpow(er, ph, 1.f), z2 = cmul(z, z), z4 = cmul(z2, z2);
        chain<16>(p, 0, cb, z);
        cf xx = cmul(cb, E);
        chain<9>(p, 16, xx, z2);
        chain<5>(p, 25, cmul(xx, E), z4);
      }
    }
    STAMP(0, 2);
    bar();
    const int T = w & 3, r = lane & 15, kg = lane >> 4, i2 = min(r & 3, 2);
    int row = T == 0 ? r : T == 1 ? 16 + 2 * (r >> 2) + max((r & 3) - 1, 0) : T == 2 ? 25 + 5 * (r >> 3) + 2 * ((r >> 2) & 1) + i2 : 35 + 3 * (r >> 2) + i2;
    LDS char* pa = L + ch * GCH + row * GROW + kg * 16;
    LDS char* pb = L + ch * GCH + (47 + r) * GROW + kg * 16;
    v4f acc = {0.f, 0.f, 0.f, 0.f};
#pragma unroll
    for (int ks = 0; ks < 8; ks++)
      acc = __builtin_amdgcn_mfma_f32_16x16x32_bf16(*(LDS v8bf*)(pa + ks * 64), *(LDS v8bf*)(pb + ks * 64), acc, 0, 0, 0);
    char* dst = ws + WS_KT + d * 4096 + r * 32;
    if (T < 2) {
      *(v2u*)(dst + T * 512 + kg * 8) = (v2u){pk(acc[0], acc[1]), pk(acc[2], acc[3])};
    } else {
      u32 p00 = pk(acc[0], acc[0]), p01 = pk(acc[0], acc[1]), p11 = pk(acc[1], acc[1]), p22 = pk(acc[2], acc[2]);
      if (T == 2) *(v4u*)(dst + 1024 + (kg >> 1) * 512 + (kg & 1) * 16) = (v4u){p00, p01, p11, p22};
      else { *(v4u*)(dst + 2048 + kg * 512) = (v4u){p00, p00, p01, p11}; *(v4u*)(dst + 2048 + kg * 512 + 16) = (v4u){p11, p11, p22, p22}; }
    }
    STAMP(0, 3);
  }
}

#define U_CP 4160
#define U_CH 16704
#define O_U 0
#define O_K (2 * U_CH)
#define O_P (O_K + 2 * 4608)
#define O_F (O_P + 32768)
#define O_W (O_F + 2048)
#define K2_LDS (O_W + 49152)

DEVI void merge2(u32& a0, u32& a1, u32 b0, u32 b1) { u32 m = min(a0, b0); a0 = max(a0, b0); a1 = max(m, max(a1, b1)); }

__global__ void __launch_bounds__(512) k2(const u16* __restrict__ dskip, const u16* __restrict__ wg, const u16* __restrict__ wu,
    const u16* __restrict__ wd, char* __restrict__ ws, u16* __restrict__ out) {
  __shared__ __attribute__((aligned(16))) char smem[K2_LDS];
  LDS char* L = (LDS char*)smem;
  const int tid = threadIdx.x, lane = tid & 63, w = __builtin_amdgcn_readfirstlane(tid >> 6);
  const int b = blockIdx.x, pair = (b & 7) * 32 + (b >> 3);
  const int slice = (w + 4) & 7;
  STAMP(1, 0);
  v4u c0 = *(const v4u*)(ws + WS_CAND + lane * 32), c1 = *(const v4u*)(ws + WS_CAND + lane * 32 + 16);
  const int tb8 = slice * 32 + (lane >> 1), ch = lane & 1;
  const char* xt = ws + WS_XT + pair * 16384 + tb8 * 64 + ch * 16;
  v4u un = *(const v4u*)xt;
  v2u pv = *(const v2u*)(xt + (tb8 ? -56 : 0));
  v4u kv = *(const v4u*)(ws + WS_KT + pair * 8192 + tid * 16);
  v4u xr = *(const v4u*)(xt + 32);
  u32 dsk = *(const u32*)(dskip + pair * 2);
  const bool ffn = (w >= 4) && ((b >> 3) < 14);
  const int fs = (b & 7) * 14 + (b >> 3), fw = w & 3;
  if (ffn) {
    const int rho = lane >> 1, kk = 8 * ((rho >> 2) & 3) + 4 * (rho >> 4) + (rho & 3);
    const size_t go = (size_t)(fw * 128 + kk) * 3584 + fs * 32 + (lane & 1) * 16;
    LDS char* wl = L + O_W + fw * 12288;
#pragma unroll
    for (int ks = 0; ks < 4; ks++) {
      __builtin_amdgcn_global_load_lds((const GLB void*)((const char*)wg + go + ks * 32 * 3584), (LDS void*)(wl + ks * 1024), 16, 0, 0);
      __builtin_amdgcn_global_load_lds((const GLB void*)((const char*)wu + go + ks * 32 * 3584), (LDS void*)(wl + 4096 + ks * 1024), 16, 0, 0);
    }
    const size_t dofs = (size_t)(fs * 16 + (lane >> 2)) * 1024 + fw * 256 + (lane & 3) * 16;
#pragma unroll
    for (int j = 0; j < 4; j++)
      __builtin_amdgcn_global_load_lds((const GLB void*)((const char*)wd + dofs + j * 64), (LDS void*)(wl + 8192 + j * 1024), 16, 0, 0);
  }
  if (w == 0) *(LDS v4u*)(L + O_K + (lane >> 5) * 4608 + (lane & 31) * 16) = (v4u){0, 0, 0, 0};
  if (w == 1 && lane < 16) *(LDS v4u*)(L + O_U + (lane >> 3) * U_CH + ((lane >> 1) & 3) * U_CP + (lane & 1) * 16) = (v4u){0, 0, 0, 0};
  if (w == 2 && lane < 4) *(LDS v4u*)(L + O_F + lane * 16) = (v4u){0, 0, 0, 0};
  u32 k0 = c0[0], k1v = c0[1];
  merge2(k0, k1v, c0[2], c0[3]); merge2(k0, k1v, c1[0], c1[1]); merge2(k0, k1v, c1[2], c1[3]);
  merge2(k0, k1v, dppu<0xB1>(k0), dppu<0xB1>(k1v));
  merge2(k0, k1v, dppu<0x4E>(k0), dppu<0x4E>(k1v));
  merge2(k0, k1v, dppu<0x141>(k0), dppu<0x141>(k1v));
  merge2(k0, k1v, dppu<0x140>(k0), dppu<0x140>(k1v));
  u32 s0 = __builtin_amdgcn_readlane(k0, 0), s1 = __builtin_amdgcn_readlane(k1v, 0);
  merge2(s0, s1, __builtin_amdgcn_readlane(k0, 16), __builtin_amdgcn_readlane(k1v, 16));
  merge2(s0, s1, __builtin_amdgcn_readlane(k0, 32), __builtin_amdgcn_readlane(k1v, 32));
  merge2(s0, s1, __builtin_amdgcn_readlane(k0, 48), __builtin_amdgcn_readlane(k1v, 48));
  const int ts0 = 2047 - (s0 & 2047), ts1 = 2047 - (s1 & 2047);
  STAMP(1, 1);
  v4u xf[4];
  if (ffn) {
    const char* xp = ws + WS_XROW + ((lane & 1) ? ts1 : ts0) * 1024 + fw * 256 + (lane >> 4) * 16;
#pragma unroll
    for (int ks = 0; ks < 4; ks++) xf[ks] = *(const v4u*)(xp + ks * 64);
  }
  {
    if (tb8 == 0) pv = (v2u){0, 0};
    LDS char* p = L + O_U + ch * U_CH + 32 + tb8 * 16;
    u32 e0 = __builtin_amdgcn_alignbit(pv[1], pv[0], 16), e1 = __builtin_amdgcn_alignbit(un[0], pv[1], 16),
        e2 = __builtin_amdgcn_alignbit(un[1], un[0], 16), e3 = __builtin_amdgcn_alignbit(un[2], un[1], 16),
        e4 = __builtin_amdgcn_alignbit(un[3], un[2], 16);
    *(LDS v4u*)p = un;
    *(LDS v4u*)(p + U_CP) = (v4u){e1, e2, e3, e4};
    *(LDS v4u*)(p + 2 * U_CP) = (v4u){pv[1], un[0], un[1], un[2]};
    *(LDS v4u*)(p + 3 * U_CP) = (v4u){e0, e1, e2, e3};
    *(LDS v4u*)(L + O_K + (w >> 2) * 4608 + 512 + (tid & 255) * 16) = kv;
  }
  STAMP(1, 2);
  bar();
  STAMP(1, 3);
  float base[2][4];
  {
    u32 sn0 = ch ? un[0] : un[2], sn1 = ch ? un[1] : un[3], sx0 = ch ? xr[0] : xr[2], sx1 = ch ? xr[1] : xr[3];
    u32 kn0 = ch ? un[2] : un[0], kn1 = ch ? un[3] : un[1], kx0 = ch ? xr[2] : xr[0], kx1 = ch ? xr[3] : xr[1];
    u32 rn0 = dppu<0xB1>(sn0), rn1 = dppu<0xB1>(sn1), rx0 = dppu<0xB1>(sx0), rx1 = dppu<0xB1>(sx1);
    float dk = 1.f + (ch ? hi2f(dsk) : lo2f(dsk)), dr = 1.f + (ch ? lo2f(dsk) : hi2f(dsk));
    float bk[4] = {lo2f(kx0) + lo2f(kn0) * dk, hi2f(kx0) + hi2f(kn0) * dk, lo2f(kx1) + lo2f(kn1) * dk, hi2f(kx1) + hi2f(kn1) * dk};
    float br[4] = {lo2f(rx0) + lo2f(rn0) * dr, hi2f(rx0) + hi2f(rn0) * dr, lo2f(rx1) + lo2f(rn1) * dr, hi2f(rx1) + hi2f(rn1) * dr};
#pragma unroll
    for (int i = 0; i < 4; i++) { base[0][i] = ch ? br[i] : bk[i]; base[1][i] = ch ? bk[i] : br[i]; }
  }
  const int c16 = lane & 15, kg = lane >> 4;
  if (w < 4) {
    const int mc = w & 1, kh = w >> 1;
    LDS char* pa = L + O_U + mc * U_CH + ((lane >> 2) & 3) * U_CP + 32 - 16 - 8 + kg * 16 + (lane & 3) * 8 + kh * 256;
    LDS char* pb = L + O_K + mc * 4608 + 528 + c16 * 32 - kg * 16 - kh * 256 - 192;
    LDS char* pw = L + O_P + w * 8192 + c16 * 64 + 16 * (kg ^ ((c16 >> 1) & 3));
    v4f acc[4];
#pragma unroll
    for (int s = 0; s < 4; s++) acc[s] = (v4f){0.f, 0.f, 0.f, 0.f};
#pragma unroll
    for (int js = 0; js < 4; js++) {
      v8bf A[4], B[4];
#pragma unroll
      for (int a = 0; a < 4; a++) {
        A[a] = cat(trd(pa + a * 512 + js * 64 + 8), trd(pa + a * 512 + js * 64));
        B[a] = *(LDS v8bf*)(pb + a * 512 + (3 - js) * 64);
      }
#pragma unroll
      for (int s = 0; s < 4; s++)
#pragma unroll
        for (int a = 0; a <= s; a++) acc[s] = __builtin_amdgcn_mfma_f32_16x16x32_bf16(A[a], B[s - a], acc[s], 0, 0, 0);
    }
#pragma unroll
    for (int s = 0; s < 4; s++) { *(LDS v4f*)(pw + s * 1024) = acc[s]; acc[s] = (v4f){0.f, 0.f, 0.f, 0.f}; }
    lds_sig(L + O_F, lane);
    STAMP(1, 4);
#pragma unroll
    for (int js = 0; js < 4; js++) {
      v8bf A[8], B[8];
#pragma unroll
      for (int a = 0; a < 8; a++) {
        A[a] = cat(trd(pa + a * 512 + js * 64 + 8), trd(pa + a * 512 + js * 64));
        B[a] = *(LDS v8bf*)(pb + a * 512 + (3 - js) * 64);
      }
#pragma unroll
      for (int s = 0; s < 4; s++)
#pragma unroll
        for (int a = 0; a <= s + 4; a++) acc[s] = __builtin_amdgcn_mfma_f32_16x16x32_bf16(A[a], B[s + 4 - a], acc[s], 0, 0, 0);
    }
#pragma unroll
    for (int s = 0; s < 4; s++) *(LDS v4f*)(pw + 4096 + s * 1024) = acc[s];
    lds_sig(L + O_F + 4, lane);
    STAMP(1, 5);
    lds_wait(L + O_F + 4, 4);
  } else {
    if (ffn) {
      LDS char* wl = L + O_W + fw * 12288 + lane * 8;
      v4f g = {0.f, 0.f, 0.f, 0.f}, u = {0.f, 0.f, 0.f, 0.f};
#pragma unroll
      for (int ks = 0; ks < 4; ks++) {
        v8bf xb = __builtin_bit_cast(v8bf, xf[ks]);
        g = __builtin_amdgcn_mfma_f32_16x16x32_bf16(cat(trd(wl + ks * 1024), trd(wl + ks * 1024 + 512)), xb, g, 0, 0, 0);
        u = __builtin_amdgcn_mfma_f32_16x16x32_bf16(cat(trd(wl + 4096 + ks * 1024), trd(wl + 4096 + ks * 1024 + 512)), xb, u, 0, 0, 0);
      }
      v4s wdf[8];
      LDS char* dl = L + O_W + fw * 12288 + 8192 + (lane >> 2) * 64 + (lane & 3) * 8;
#pragma unroll
      for (int j = 0; j < 8; j++) wdf[j] = trd(dl + (j >> 1) * 1024 + (j & 1) * 32);
      STAMP(1, 4);
      LDS char* ex = L + O_F + 1024 + (kg * 2 + (lane & 1)) * 32;
      if (c16 < 2) { *(LDS v4f*)(ex + fw * 256) = g; *(LDS v4f*)(ex + fw * 256 + 16) = u; }
      lds_sig(L + O_F + 8, lane);
      lds_wait(L + O_F + 8, 4);
      g = *(LDS v4f*)ex; u = *(LDS v4f*)(ex + 16);
#pragma unroll
      for (int j = 1; j < 4; j++) { g += *(LDS v4f*)(ex + j * 256); u += *(LDS v4f*)(ex + j * 256 + 16); }
      float h[4];
#pragma unroll
      for (int i = 0; i < 4; i++) {
        float gg = rbf(g[i]);
        float sg = rbf(gg * __builtin_amdgcn_rcpf(1.f + __builtin_amdgcn_exp2f(gg * -1.44269504089f)));
        h[i] = sg * rbf(u[i]);
      }
      v4s hf = __builtin_bit_cast(v4s, (v2u){pk(h[0], h[1]), pk(h[2], h[3])});
      v4f o[8];
#pragma unroll
      for (int j = 0; j < 8; j++) o[j] = __builtin_amdgcn_mfma_f32_16x16x16bf16_1k(hf, wdf[j], (v4f){0.f, 0.f, 0.f, 0.f}, 0, 0, 0);
      __amdgpu_buffer_rsrc_t rs = __builtin_amdgcn_make_buffer_rsrc(ws + WS_PART, 0, NFFN * 4096, 0x00020000);
#pragma unroll
      for (int hh = 0; hh < 2; hh++) {
        v4f e = kg == 0 ? o[4 * hh] : kg == 1 ? o[4 * hh + 1] : kg == 2 ? o[4 * hh + 2] : o[4 * hh + 3];
        u32 e0 = __float_as_uint(e[0]), e1 = __float_as_uint(e[1]);
        e0 = e0 == SENT ? 0x7fc00000u : e0; e1 = e1 == SENT ? 0x7fc00000u : e1;
        __builtin_amdgcn_raw_buffer_store_b64((v2u){e0, e1}, rs, fs * 4096 + fw * 1024 + hh * 512 + lane * 8, 0, 16);
      }
      STAMP(1, 5);
    }
    lds_wait(L + O_F, 4);
  }
  float sv[2][2] = {{0.f, 0.f}, {0.f, 0.f}};
  {
    LDS char* pr = L + O_P + slice * 1024 + (lane >> 2) * 64 + 16 * ((lane & 3) ^ ((lane >> 3) & 3));
    v4f y0 = *(LDS v4f*)pr + *(LDS v4f*)(pr + 16384), y1 = *(LDS v4f*)(pr + 8192) + *(LDS v4f*)(pr + 24576);
    const int t0 = slice * 256 + lane * 4;
    char* op = (char*)out + t0 * 1024 + pair * 4;
#pragma unroll
    for (int i = 0; i < 4; i++) {
      float o0 = base[0][i] + y0[i], o1 = base[1][i] + y1[i];
      if (t0 + i == ts0) { sv[0][0] = o0; sv[0][1] = o1; }
      else if (t0 + i == ts1) { sv[1][0] = o0; sv[1][1] = o1; }
      else *(u32*)(op + i * 1024) = pk(o0, o1);
    }
  }
  STAMP(1, 6);
  if (w == 7) {
    __amdgpu_buffer_rsrc_t rs = __builtin_amdgcn_make_buffer_rsrc(ws + WS_PART, 0, NFFN * 4096, 0x00020000);
    const int j2 = lane < NFFN - 64 ? lane + 64 : lane;
    v4u a, c;
    for (int spin = 0; spin < 20000; spin++) {
      asm volatile("" ::: "memory");
      a = __builtin_amdgcn_raw_buffer_load_b128(rs, lane * 4096 + pair * 16, 0, 16);
      c = __builtin_amdgcn_raw_buffer_load_b128(rs, j2 * 4096 + pair * 16, 0, 16);
      bool bad = a[0] == SENT || a[1] == SENT || a[2] == SENT || a[3] == SENT || c[0] == SENT || c[1] == SENT || c[2] == SENT || c[3] == SENT;
      if (!__any(bad)) break;
      __builtin_amdgcn_s_sleep(2);
    }
    float up[4];
#pragma unroll
    for (int i = 0; i < 4; i++) {
      float v = __uint_as_float(a[i]) + (lane < NFFN - 64 ? __uint_as_float(c[i]) : 0.f);
      u32 kk = (i & 1) ? s1 : s0;
      float r = __uint_as_float(((kk & 0x80000000u) ? (kk & 0x7fffffffu) : ~kk) & 0xffff0000u);
      float sg = rbf(__builtin_amdgcn_rcpf(1.f + __builtin_amdgcn_exp2f(r * -1.44269504089f)));
      up[i] = rbf(rbf(wave_sum(v)) * sg);
    }
    if (lane == 0) *(LDS v4f*)(L + O_F + 16) = (v4f){up[0], up[1], up[2], up[3]};
    lds_sig(L + O_F + 12, lane);
  }
  if (slice == (ts0 >> 8) || slice == (ts1 >> 8)) {
    lds_wait(L + O_F + 12, 1);
    v4f up = *(LDS v4f*)(L + O_F + 16);
    const int tq = slice * 64 + lane;
    if (tq == (ts0 >> 2)) *(u32*)((char*)out + ts0 * 1024 + pair * 4) = pk(sv[0][0] + up[0], sv[0][1] + up[2]);
    if (tq == (ts1 >> 2)) *(u32*)((char*)out + ts1 * 1024 + pair * 4) = pk(sv[1][0] + up[1], sv[1][1] + up[3]);
  }
  STAMP(1, 7);
}

extern "C" void kernel_launch(void* const* d_in, const int* in_sizes, int n_in, void* d_out, int out_size, void* d_ws,
                              size_t ws_size, hipStream_t stream) {
  k1<<<512, 512, 0, stream>>>((const u16*)d_in[0], (const u16*)d_in[10], (const u16*)d_in[1], (const u16*)d_in[2],
                              (const u16*)d_in[3], (const u16*)d_in[4], (const u16*)d_in[5], (const u16*)d_in[6],
                              (const u16*)d_in[7], (char*)d_ws);
  k2<<<256, 512, 0, stream>>>((const u16*)d_in[8], (const u16*)d_in[11], (const u16*)d_in[12], (const u16*)d_in[13],
                              (char*)d_ws, (u16*)d_out);
}
```

```cpp
#define DBG_STAMP
#define PB0 512
#define PW0 8
#define PK 0
#define PI 3
#define PMODE 0
#define PBO 0
#define PB 512
#define PWO 0
#define PW 8
#include <hip/hip_runtime.h>

typedef unsigned short u16;
typedef unsigned u32;
typedef unsigned long long u64;
typedef u32 v4u __attribute__((ext_vector_type(4)));
typedef u32 v2u __attribute__((ext_vector_type(2)));
typedef float v4f __attribute__((ext_vector_type(4)));
typedef float v2f __attribute__((ext_vector_type(2)));
typedef __bf16 v8bf __attribute__((ext_vector_type(8)));
typedef __bf16 v2bf __attribute__((ext_vector_type(2)));
typedef short v4s __attribute__((ext_vector_type(4)));
#define LDS __attribute__((address_space(3)))
#define GLB __attribute__((address_space(1)))
#define DEVI __device__ __forceinline__

#define WS_XT 0
#define WS_KT (4u << 20)
#define WS_XROW (6u << 20)
#define WS_CAND (8u << 20)
#define WS_PART ((8u << 20) + 65536)
#define NFFN 112
#define SENT 0xFFFFFFFFu

#define WS_STAMP (16u << 20)
#define STAMP(k, i) do { if (lane == 0) { u64 t_; asm volatile("s_memrealtime %0\n\ts_waitcnt lgkmcnt(0)" : "=s"(t_) :: "memory"); \
  ((u64*)(ws + WS_STAMP))[(((k) * 512 + blockIdx.x) * 16 + w) * 8 + (i)] = t_; } } while (0)

DEVI u32 pk(float a, float b) { return __builtin_bit_cast(u32, __builtin_convertvector((v2f){a, b}, v2bf)); }
DEVI float rbf(float a) { return __uint_as_float(pk(0.f, a)); }
DEVI float lo2f(u32 v) { return __uint_as_float(v << 16); }
DEVI float hi2f(u32 v) { return __uint_as_float(v & 0xffff0000u); }
template <int CTRL, int RM> DEVI float dppf(float v) {
  return __int_as_float(__builtin_amdgcn_update_dpp(0, __float_as_int(v), CTRL, RM, 0xf, false)); }
template <int CTRL> DEVI u32 dppu(u32 v) { return (u32)__builtin_amdgcn_update_dpp(0, (int)v, CTRL, 0xf, 0xf, false); }
DEVI float wave_sum(float v) {
  v += dppf<0xB1, 0xf>(v); v += dppf<0x4E, 0xf>(v); v += dppf<0x141, 0xf>(v); v += dppf<0x140, 0xf>(v);
  v += dppf<0x142, 0xa>(v); v += dppf<0x143, 0xc>(v);
  return __int_as_float(__builtin_amdgcn_readlane(__float_as_int(v), 63));
}
DEVI void bar() { asm volatile("s_waitcnt lgkmcnt(0)\n\ts_barrier" ::: "memory"); }
DEVI v4s trd(LDS char* p) { return __builtin_amdgcn_ds_read_tr16_b64_v4i16((LDS v4s*)p); }
DEVI v8bf cat(v4s a, v4s b) { return __builtin_bit_cast(v8bf, __builtin_shufflevector(a, b, 0, 1, 2, 3, 4, 5, 6, 7)); }
DEVI void lds_wait(LDS char* p, u32 n) {
  while (*(volatile LDS u32*)p < n) __builtin_amdgcn_s_sleep(1);
  asm volatile("" ::: "memory");
}
DEVI void lds_sig(LDS char* p, int lane) {
  asm volatile("s_waitcnt lgkmcnt(0)" ::: "memory");
  if (lane == 0) __hip_atomic_fetch_add((LDS u32*)p, 1u, __ATOMIC_RELAXED, __HIP_MEMORY_SCOPE_WORKGROUP);
}

struct cf { float r, i; };
DEVI cf cmul(cf a, cf b) { return {a.r * b.r - a.i * b.i, a.r * b.i + a.i * b.r}; }
DEVI cf cpow(float er, float ph, float s) {
  float e = __builtin_amdgcn_exp2f(er * s), f = __builtin_amdgcn_fractf(ph * s);
  return {e * __builtin_amdgcn_cosf(f), e * __builtin_amdgcn_sinf(f)};
}
#define GROW 528
#define GCH (63 * GROW)
template <int N> DEVI void chain(LDS char* p, int row, cf x, cf m) {
#pragma unroll
  for (int j = 0; j < N; j++) { *(LDS u32*)(p + (row + j) * GROW) = pk(x.r, x.i); if (j + 1 < N) x = cmul(x, m); }
}

#define K1_LDS (2 * GCH)
__global__ void __launch_bounds__(512) k1(const u16* __restrict__ x, const u16* __restrict__ wr, const u16* __restrict__ Are,
    const u16* __restrict__ Aim, const u16* __restrict__ Bre, const u16* __restrict__ Bim, const u16* __restrict__ Cre,
    const u16* __restrict__ Cim, const u16* __restrict__ logdt, char* __restrict__ ws) {
  __shared__ __attribute__((aligned(16))) char smem[K1_LDS];
  LDS char* L = (LDS char*)smem;
  const int lane = threadIdx.x & 63, w = __builtin_amdgcn_readfirstlane(threadIdx.x >> 6);
  const int b = blockIdx.x & 255, sw = (b & 7) * 32 + (b >> 3);
  STAMP(0, 0);
  if (blockIdx.x < 256) {
    const int t = sw * 8 + w;
    v4u xr = *(const v4u*)(x + t * 512 + lane * 8);
    v4u wv = *(const v4u*)(wr + lane * 8);
    if (w < 2 && threadIdx.x < NFFN) *(v4u*)(ws + WS_PART + (b * NFFN + threadIdx.x) * 16) = (v4u){SENT, SENT, SENT, SENT};
    float v[8];
#pragma unroll
    for (int j = 0; j < 4; j++) { v[2 * j] = lo2f(xr[j]); v[2 * j + 1] = hi2f(xr[j]); }
    float s = ((v[0] + v[1]) + (v[2] + v[3])) + ((v[4] + v[5]) + (v[6] + v[7]));
    STAMP(0, 1);
    float mean = wave_sum(s) * (1.f / 512.f);
    float q = 0.f;
#pragma unroll
    for (int j = 0; j < 8; j++) { float d = v[j] - mean; q += d * d; }
    float var = rbf(wave_sum(q) * (1.f / 512.f));
    float mb = rbf(mean);
    float rs = __builtin_amdgcn_rcpf(rbf(__builtin_amdgcn_sqrtf(rbf(var + 1e-5f))));
    v4u xn; float acc = 0.f;
#pragma unroll
    for (int j = 0; j < 4; j++) {
      u32 xj = pk(rbf(v[2 * j] - mb) * rs, rbf(v[2 * j + 1] - mb) * rs), wj = wv[j];
      xn[j] = xj;
      acc += lo2f(xj) * lo2f(wj) + hi2f(xj) * hi2f(wj);
    }
    *(LDS v4u*)(L + w * 2112 + lane * 32) = (v4u){xn[0], xr[0], xn[1], xr[1]};
    *(LDS v4u*)(L + w * 2112 + lane * 32 + 16) = (v4u){xn[2], xr[2], xn[3], xr[3]};
    u32 rb = __float_as_uint(rbf(wave_sum(acc)));
    u32 key = ((rb & 0x80000000u) ? (~rb & 0xffff0000u) : (rb | 0x80000000u)) | (u32)(2047 - t);
    if (lane == 0) *(LDS u32*)(L + 17408 + w * 4) = key;
    STAMP(0, 2);
    bar();
    {
      LDS char* p = L + ((lane >> 2) & 3) * 2112 + w * 256 + (lane >> 4) * 32 + (lane & 3) * 8;
      v4s a0 = trd(p), a1 = trd(p + 4 * 2112), b0 = trd(p + 128), b1 = trd(p + 4 * 2112 + 128);
      char* dst = ws + WS_XT + (size_t)(w * 32 + (lane >> 2)) * 16384 + sw * 64 + (lane & 3) * 16;
      *(v8bf*)dst = cat(a0, a1);
      *(v8bf*)(dst + 16 * 16384) = cat(b0, b1);
    }
    v4u k0 = *(LDS v4u*)(L + 17408), k1v = *(LDS v4u*)(L + 17424);
    int rank = 0;
#pragma unroll
    for (int j = 0; j < 4; j++) rank += (k0[j] > key) + (k1v[j] > key);
    if (rank < 2) {
      *(v4u*)(ws + WS_XROW + t * 1024 + lane * 16) = xn;
      if (lane == 0) *(u32*)(ws + WS_CAND + b * 8 + rank * 4) = key;
    }
    STAMP(0, 3);
  } else {
    const int ch = w >> 2, d = sw * 2 + ch;
    {
      const int n = ((w >> 1) & 1) * 64 + lane, idx = d * 128 + n;
      float are = lo2f(Are[idx]), aim = lo2f(Aim[idx]), bre = lo2f(Bre[idx]), bim = lo2f(Bim[idx]), cre = lo2f(Cre[idx]), cim = lo2f(Cim[idx]);
      float dt = rbf(expf(lo2f(logdt[d])));
      float er = dt * are * 1.44269504089f, ph = dt * aim * 0.15915494309f;
      STAMP(0, 1);
      cf cb = cmul({cre, cim}, {dt * bre, dt * bim});
      cf E = cpow(er, ph, 256.f);
      LDS char* p = L + ch * GCH + n * 4;
      if (w & 1) {
        cf z4 = cpow(er, ph, 4.f), z8 = cmul(z4, z4), z16 = cmul(z8, z8);
        cf xx = cmul(cb, cpow(er, ph, 768.f));
        chain<5>(p, 30, xx, z4);
#pragma unroll
        for (int a = 0; a < 4; a++) { xx = cmul(xx, E); chain<3>(p, 35 + 3 * a, xx, z8); }
        z16.i = -z16.i;
        *(LDS u32*)(p + 47 * GROW) = 0x3f80u;
        chain<15>(p, 48, z16, z16);
      } else {
        cf z = cpow(er, ph, 1.f), z2 = cmul(z, z), z4 = cmul(z2, z2);
        chain<16>(p, 0, cb, z);
        cf xx = cmul(cb, E);
        chain<9>(p, 16, xx, z2);
        chain<5>(p, 25, cmul(xx, E), z4);
      }
    }
    STAMP(0, 2);
    bar();
    const int T = w & 3, r = lane & 15, kg = lane >> 4, i2 = min(r & 3, 2);
    int row = T == 0 ? r : T == 1 ? 16 + 2 * (r >> 2) + max((r & 3) - 1, 0) : T == 2 ? 25 + 5 * (r >> 3) + 2 * ((r >> 2) & 1) + i2 : 35 + 3 * (r >> 2) + i2;
    LDS char* pa = L + ch * GCH + row * GROW + kg * 16;
    LDS char* pb = L + ch * GCH + (47 + r) * GROW + kg * 16;
    v4f acc = {0.f, 0.f, 0.f, 0.f};
#pragma unroll
    for (int ks = 0; ks < 8; ks++)
      acc = __builtin_amdgcn_mfma_f32_16x16x32_bf16(*(LDS v8bf*)(pa + ks * 64), *(LDS v8bf*)(pb + ks * 64), acc, 0, 0, 0);
    char* dst = ws + WS_KT + d * 4096 + r * 32;
    if (T < 2) {
      *(v2u*)(dst + T * 512 + kg * 8) = (v2u){pk(acc[0], acc[1]), pk(acc[2], acc[3])};
    } else {
      u32 p00 = pk(acc[0], acc[0]), p01 = pk(acc[0], acc[1]), p11 = pk(acc[1], acc[1]), p22 = pk(acc[2], acc[2]);
      if (T == 2) *(v4u*)(dst + 1024 + (kg >> 1) * 512 + (kg & 1) * 16) = (v4u){p00, p01, p11, p22};
      else { *(v4u*)(dst + 2048 + kg * 512) = (v4u){p00, p00, p01, p11}; *(v4u*)(dst + 2048 + kg * 512 + 16) = (v4u){p11, p11, p22, p22}; }
    }
    STAMP(0, 3);
  }
}

#define U_CP 4160
#define U_CH 16704
#define O_U 0
#define O_K (2 * U_CH)
#define O_P (O_K + 2 * 4608)
#define O_F (O_P + 32768)
#define O_W (O_F + 2048)
#define K2_LDS (O_W + 49152)

DEVI void merge2(u32& a0, u32& a1, u32 b0, u32 b1) { u32 m = min(a0, b0); a0 = max(a0, b0); a1 = max(m, max(a1, b1)); }

__global__ void __launch_bounds__(512) k2(const u16* __restrict__ dskip, const u16* __restrict__ wg, const u16* __restrict__ wu,
    const u16* __restrict__ wd, char* __restrict__ ws, u16* __restrict__ out) {
  __shared__ __attribute__((aligned(16))) char smem[K2_LDS];
  LDS char* L = (LDS char*)smem;
  const int tid = threadIdx.x, lane = tid & 63, w = __builtin_amdgcn_readfirstlane(tid >> 6);
  const int b = blockIdx.x, pair = (b & 7) * 32 + (b >> 3);
  const int slice = (w + 4) & 7;
  STAMP(1, 0);
  v4u c0 = *(const v4u*)(ws + WS_CAND + lane * 32), c1 = *(const v4u*)(ws + WS_CAND + lane * 32 + 16);
  const int tb8 = slice * 32 + (lane >> 1), ch = lane & 1;
  const char* xt = ws + WS_XT + pair * 16384 + tb8 * 64 + ch * 16;
  v4u un = *(const v4u*)xt;
  v2u pv = *(const v2u*)(xt + (tb8 ? -56 : 0));
  v4u kv = *(const v4u*)(ws + WS_KT + pair * 8192 + tid * 16);
  v4u xr = *(const v4u*)(xt + 32);
  u32 dsk = *(const u32*)(dskip + pair * 2);
  const bool ffn = (w >= 4) && ((b >> 3) < 14);
  const int fs = (b & 7) * 14 + (b >> 3), fw = w & 3;
  if (ffn) {
    const int rho = lane >> 1, kk = 8 * ((rho >> 2) & 3) + 4 * (rho >> 4) + (rho & 3);
    const size_t go = (size_t)(fw * 128 + kk) * 3584 + fs * 32 + (lane & 1) * 16;
    LDS char* wl = L + O_W + fw * 12288;
#pragma unroll
    for (int ks = 0; ks < 4; ks++) {
      __builtin_amdgcn_global_load_lds((const GLB void*)((const char*)wg + go + ks * 32 * 3584), (LDS void*)(wl + ks * 1024), 16, 0, 0);
      __builtin_amdgcn_global_load_lds((const GLB void*)((const char*)wu + go + ks * 32 * 3584), (LDS void*)(wl + 4096 + ks * 1024), 16, 0, 0);
    }
    const size_t dofs = (size_t)(fs * 16 + (lane >> 2)) * 1024 + fw * 256 + (lane & 3) * 16;
#pragma unroll
    for (int j = 0; j < 4; j++)
      __builtin_amdgcn_global_load_lds((const GLB void*)((const char*)wd + dofs + j * 64), (LDS void*)(wl + 8192 + j * 1024), 16, 0, 0);
  }
  if (w == 0) *(LDS v4u*)(L + O_K + (lane >> 5) * 4608 + (lane & 31) * 16) = (v4u){0, 0, 0, 0};
  if (w == 1 && lane < 16) *(LDS v4u*)(L + O_U + (lane >> 3) * U_CH + ((lane >> 1) & 3) * U_CP + (lane & 1) * 16) = (v4u){0, 0, 0, 0};
  if (w == 2 && lane < 4) *(LDS v4u*)(L + O_F + lane * 16) = (v4u){0, 0, 0, 0};
  u32 k0 = c0[0], k1v = c0[1];
  merge2(k0, k1v, c0[2], c0[3]); merge2(k0, k1v, c1[0], c1[1]); merge2(k0, k1v, c1[2], c1[3]);
  merge2(k0, k1v, dppu<0xB1>(k0), dppu<0xB1>(k1v));
  merge2(k0, k1v, dppu<0x4E>(k0), dppu<0x4E>(k1v));
  merge2(k0, k1v, dppu<0x141>(k0), dppu<0x141>(k1v));
  merge2(k0, k1v, dppu<0x140>(k0), dppu<0x140>(k1v));
  u32 s0 = __builtin_amdgcn_readlane(k0, 0), s1 = __builtin_amdgcn_readlane(k1v, 0);
  merge2(s0, s1, __builtin_amdgcn_readlane(k0, 16), __builtin_amdgcn_readlane(k1v, 16));
  merge2(s0, s1, __builtin_amdgcn_readlane(k0, 32), __builtin_amdgcn_readlane(k1v, 32));
  merge2(s0, s1, __builtin_amdgcn_readlane(k0, 48), __builtin_amdgcn_readlane(k1v, 48));
  const int ts0 = 2047 - (s0 & 2047), ts1 = 2047 - (s1 & 2047);
  STAMP(1, 1);
  v4u xf[4];
  if (ffn) {
    const char* xp = ws + WS_XROW + ((lane & 1) ? ts1 : ts0) * 1024 + fw * 256 + (lane >> 4) * 16;
#pragma unroll
    for (int ks = 0; ks < 4; ks++) xf[ks] = *(const v4u*)(xp + ks * 64);
  }
  {
    if (tb8 == 0) pv = (v2u){0, 0};
    LDS char* p = L + O_U + ch * U_CH + 32 + tb8 * 16;
    u32 e0 = __builtin_amdgcn_alignbit(pv[1], pv[0], 16), e1 = __builtin_amdgcn_alignbit(un[0], pv[1], 16),
        e2 = __builtin_amdgcn_alignbit(un[1], un[0], 16), e3 = __builtin_amdgcn_alignbit(un[2], un[1], 16),
        e4 = __builtin_amdgcn_alignbit(un[3], un[2], 16);
    *(LDS v4u*)p = un;
    *(LDS v4u*)(p + U_CP) = (v4u){e1, e2, e3, e4};
    *(LDS v4u*)(p + 2 * U_CP) = (v4u){pv[1], un[0], un[1], un[2]};
    *(LDS v4u*)(p + 3 * U_CP) = (v4u){e0, e1, e2, e3};
    *(LDS v4u*)(L + O_K + (w >> 2) * 4608 + 512 + (tid & 255) * 16) = kv;
  }
  STAMP(1, 2);
  bar();
  STAMP(1, 3);
  float base[2][4];
  {
    u32 sn0 = ch ? un[0] : un[2], sn1 = ch ? un[1] : un[3], sx0 = ch ? xr[0] : xr[2], sx1 = ch ? xr[1] : xr[3];
    u32 kn0 = ch ? un[2] : un[0], kn1 = ch ? un[3] : un[1], kx0 = ch ? xr[2] : xr[0], kx1 = ch ? xr[3] : xr[1];
    u32 rn0 = dppu<0xB1>(sn0), rn1 = dppu<0xB1>(sn1), rx0 = dppu<0xB1>(sx0), rx1 = dppu<0xB1>(sx1);
    float dk = 1.f + (ch ? hi2f(dsk) : lo2f(dsk)), dr = 1.f + (ch ? lo2f(dsk) : hi2f(dsk));
    float bk[4] = {lo2f(kx0) + lo2f(kn0) * dk, hi2f(kx0) + hi2f(kn0) * dk, lo2f(kx1) + lo2f(kn1) * dk, hi2f(kx1) + hi2f(kn1) * dk};
    float br[4] = {lo2f(rx0) + lo2f(rn0) * dr, hi2f(rx0) + hi2f(rn0) * dr, lo2f(rx1) + lo2f(rn1) * dr, hi2f(rx1) + hi2f(rn1) * dr};
#pragma unroll
    for (int i = 0; i < 4; i++) { base[0][i] = ch ? br[i] : bk[i]; base[1][i] = ch ? bk[i] : br[i]; }
  }
  const int c16 = lane & 15, kg = lane >> 4;
  if (w < 4) {
    const int mc = w & 1, kh = w >> 1;
    LDS char* pa = L + O_U + mc * U_CH + ((lane >> 2) & 3) * U_CP + 32 - 16 - 8 + kg * 16 + (lane & 3) * 8 + kh * 256;
    LDS char* pb = L + O_K + mc * 4608 + 528 + c16 * 32 - kg * 16 - kh * 256 - 192;
    LDS char* pw = L + O_P + w * 8192 + c16 * 64 + 16 * (kg ^ ((c16 >> 1) & 3));
    v4f acc[4];
#pragma unroll
    for (int s = 0; s < 4; s++) acc[s] = (v4f){0.f, 0.f, 0.f, 0.f};
#pragma unroll
    for (int js = 0; js < 4; js++) {
      v8bf A[4], B[4];
#pragma unroll
      for (int a = 0; a < 4; a++) {
        A[a] = cat(trd(pa + a * 512 + js * 64 + 8), trd(pa + a * 512 + js * 64));
        B[a] = *(LDS v8bf*)(pb + a * 512 + (3 - js) * 64);
      }
#pragma unroll
      for (int s = 0; s < 4; s++)
#pragma unroll
        for (int a = 0; a <= s; a++) acc[s] = __builtin_amdgcn_mfma_f32_16x16x32_bf16(A[a], B[s - a], acc[s], 0, 0, 0);
    }
#pragma unroll
    for (int s = 0; s < 4; s++) { *(LDS v4f*)(pw + s * 1024) = acc[s]; acc[s] = (v4f){0.f, 0.f, 0.f, 0.f}; }
    lds_sig(L + O_F, lane);
    STAMP(1, 4);
#pragma unroll
    for (int js = 0; js < 4; js++) {
      v8bf A[8], B[8];
#pragma unroll
      for (int a = 0; a < 8; a++) {
        A[a] = cat(trd(pa + a * 512 + js * 64 + 8), trd(pa + a * 512 + js * 64));
        B[a] = *(LDS v8bf*)(pb + a * 512 + (3 - js) * 64);
      }
#pragma unroll
      for (int s = 0; s < 4; s++)
#pragma unroll
        for (int a = 0; a <= s + 4; a++) acc[s] = __builtin_amdgcn_mfma_f32_16x16x32_bf16(A[a], B[s + 4 - a], acc[s], 0, 0, 0);
    }
#pragma unroll
    for (int s = 0; s < 4; s++) *(LDS v4f*)(pw + 4096 + s * 1024) = acc[s];
    lds_sig(L + O_F + 4, lane);
    STAMP(1, 5);
    lds_wait(L + O_F + 4, 4);
  } else {
    if (ffn) {
      LDS char* wl = L + O_W + fw * 12288 + lane * 8;
      v4f g = {0.f, 0.f, 0.f, 0.f}, u = {0.f, 0.f, 0.f, 0.f};
#pragma unroll
      for (int ks = 0; ks < 4; ks++) {
        v8bf xb = __builtin_bit_cast(v8bf, xf[ks]);
        g = __builtin_amdgcn_mfma_f32_16x16x32_bf16(cat(trd(wl + ks * 1024), trd(wl + ks * 1024 + 512)), xb, g, 0, 0, 0);
        u = __builtin_amdgcn_mfma_f32_16x16x32_bf16(cat(trd(wl + 4096 + ks * 1024), trd(wl + 4096 + ks * 1024 + 512)), xb, u, 0, 0, 0);
      }
      v4s wdf[8];
      LDS char* dl = L + O_W + fw * 12288 + 8192 + (lane >> 2) * 64 + (lane & 3) * 8;
#pragma unroll
      for (int j = 0; j < 8; j++) wdf[j] = trd(dl + (j >> 1) * 1024 + (j & 1) * 32);
      STAMP(1, 4);
      LDS char* ex = L + O_F + 1024 + (kg * 2 + (lane & 1)) * 32;
      if (c16 < 2) { *(LDS v4f*)(ex + fw * 256) = g; *(LDS v4f*)(ex + fw * 256 + 16) = u; }
      lds_sig(L + O_F + 8, lane);
      lds_wait(L + O_F + 8, 4);
      g = *(LDS v4f*)ex; u = *(LDS v4f*)(ex + 16);
#pragma unroll
      for (int j = 1; j < 4; j++) { g += *(LDS v4f*)(ex + j * 256); u += *(LDS v4f*)(ex + j * 256 + 16); }
      float h[4];
#pragma unroll
      for (int i = 0; i < 4; i++) {
        float gg = rbf(g[i]);
        float sg = rbf(gg * __builtin_amdgcn_rcpf(1.f + __builtin_amdgcn_exp2f(gg * -1.44269504089f)));
        h[i] = sg * rbf(u[i]);
      }
      v4s hf = __builtin_bit_cast(v4s, (v2u){pk(h[0], h[1]), pk(h[2], h[3])});
      v4f o[8];
#pragma unroll
      for (int j = 0; j < 8; j++) o[j] = __builtin_amdgcn_mfma_f32_16x16x16bf16_1k(hf, wdf[j], (v4f){0.f, 0.f, 0.f, 0.f}, 0, 0, 0);
      __amdgpu_buffer_rsrc_t rs = __builtin_amdgcn_make_buffer_rsrc(ws + WS_PART, 0, NFFN * 4096, 0x00020000);
#pragma unroll
      for (int hh = 0; hh < 2; hh++) {
        v4f e = kg == 0 ? o[4 * hh] : kg == 1 ? o[4 * hh + 1] : kg == 2 ? o[4 * hh + 2] : o[4 * hh + 3];
        u32 e0 = __float_as_uint(e[0]), e1 = __float_as_uint(e[1]);
        e0 = e0 == SENT ? 0x7fc00000u : e0; e1 = e1 == SENT ? 0x7fc00000u : e1;
        __builtin_amdgcn_raw_buffer_store_b64((v2u){e0, e1}, rs, fs * 4096 + fw * 1024 + hh * 512 + lane * 8, 0, 16);
      }
      STAMP(1, 5);
    }
    lds_wait(L + O_F, 4);
  }
  float sv[2][2] = {{0.f, 0.f}, {0.f, 0.f}};
  {
    LDS char* pr = L + O_P + slice * 1024 + (lane >> 2) * 64 + 16 * ((lane & 3) ^ ((lane >> 3) & 3));
    v4f y0 = *(LDS v4f*)pr + *(LDS v4f*)(pr + 16384), y1 = *(LDS v4f*)(pr + 8192) + *(LDS v4f*)(pr + 24576);
    const int t0 = slice * 256 + lane * 4;
    char* op = (char*)out + t0 * 1024 + pair * 4;
#pragma unroll
    for (int i = 0; i < 4; i++) {
      float o0 = base[0][i] + y0[i], o1 = base[1][i] + y1[i];
      if (t0 + i == ts0) { sv[0][0] = o0; sv[0][1] = o1; }
      else if (t0 + i == ts1) { sv[1][0] = o0; sv[1][1] = o1; }
      else *(u32*)(op + i * 1024) = pk(o0, o1);
    }
  }
  STAMP(1, 6);
  if (w == 7) {
    __amdgpu_buffer_rsrc_t rs = __builtin_amdgcn_make_buffer_rsrc(ws + WS_PART, 0, NFFN * 4096, 0x00020000);
    const int j2 = lane < NFFN - 64 ? lane + 64 : lane;
    v4u a, c;
    for (int spin = 0; spin < 20000; spin++) {
      asm volatile("" ::: "memory");
      a = __builtin_amdgcn_raw_buffer_load_b128(rs, lane * 4096 + pair * 16, 0, 16);
      c = __builtin_amdgcn_raw_buffer_load_b128(rs, j2 * 4096 + pair * 16, 0, 16);
      bool bad = a[0] == SENT || a[1] == SENT || a[2] == SENT || a[3] == SENT || c[0] == SENT || c[1] == SENT || c[2] == SENT || c[3] == SENT;
      if (!__any(bad)) break;
      __builtin_amdgcn_s_sleep(2);
    }
    float up[4];
#pragma unroll
    for (int i = 0; i < 4; i++) {
      float v = __uint_as_float(a[i]) + (lane < NFFN - 64 ? __uint_as_float(c[i]) : 0.f);
      u32 kk = (i & 1) ? s1 : s0;
      float r = __uint_as_float(((kk & 0x80000000u) ? (kk & 0x7fffffffu) : ~kk) & 0xffff0000u);
      float sg = rbf(__builtin_amdgcn_rcpf(1.f + __builtin_amdgcn_exp2f(r * -1.44269504089f)));
      up[i] = rbf(rbf(wave_sum(v)) * sg);
    }
    if (lane == 0) *(LDS v4f*)(L + O_F + 16) = (v4f){up[0], up[1], up[2], up[3]};
    lds_sig(L + O_F + 12, lane);
  }
  if (slice == (ts0 >> 8) || slice == (ts1 >> 8)) {
    lds_wait(L + O_F + 12, 1);
    v4f up = *(LDS v4f*)(L + O_F + 16);
    const int tq = slice * 64 + lane;
    if (tq == (ts0 >> 2)) *(u32*)((char*)out + ts0 * 1024 + pair * 4) = pk(sv[0][0] + up[0], sv[0][1] + up[2]);
    if (tq == (ts1 >> 2)) *(u32*)((char*)out + ts1 * 1024 + pair * 4) = pk(sv[1][0] + up[1], sv[1][1] + up[3]);
  }
  STAMP(1, 7);
}

__global__ void __launch_bounds__(64) kprobe(char* ws) {
  u64 ts; asm volatile("s_memrealtime %0\n\ts_waitcnt lgkmcnt(0)" : "=s"(ts) :: "memory");
  const int lane = threadIdx.x;
  const u64* st = (const u64*)(ws + WS_STAMP);
  u64 t0 = ~0ull, mn = ~0ull, mx = 0;
  for (int j = lane; j < PB0 * PW0; j += 64) { u64 v = st[((0 * 512 + j / PW0) * 16 + j % PW0) * 8 + 0]; t0 = v < t0 ? v : t0; }
  for (int j = lane; j < PB * PW; j += 64) { u64 v = st[((PK * 512 + PBO + j / PW) * 16 + PWO + j % PW) * 8 + PI]; mn = v < mn ? v : mn; mx = v > mx ? v : mx; }
  for (int o = 32; o; o >>= 1) {
    u64 a = __shfl_xor(t0, o), b = __shfl_xor(mn, o), c = __shfl_xor(mx, o);
    t0 = a < t0 ? a : t0; mn = b < mn ? b : mn; mx = c > mx ? c : mx;
  }
  u64 x = (PMODE == 0 ? t0 : PMODE == 1 ? mn : PMODE == 2 ? mx : ts) - t0;
  if (x > 5000) x = 5000;
  u64 t1; asm volatile("s_memrealtime %0\n\ts_waitcnt lgkmcnt(0)" : "=s"(t1) :: "memory");
  u64 end = ts + 800 + 4 * x;
  for (int i = 0; i < 2000000; i++) {
    asm volatile("s_memrealtime %0\n\ts_waitcnt lgkmcnt(0)" : "=s"(t1) :: "memory");
    if (t1 >= end) break;
  }
}

extern "C" void kernel_launch(void* const* d_in, const int* in_sizes, int n_in, void* d_out, int out_size, void* d_ws,
                              size_t ws_size, hipStream_t stream) {
  k1<<<512, 512, 0, stream>>>((const u16*)d_in[0], (const u16*)d_in[10], (const u16*)d_in[1], (const u16*)d_in[2],
                              (const u16*)d_in[3], (const u16*)d_in[4], (const u16*)d_in[5], (const u16*)d_in[6],
                              (const u16*)d_in[7], (char*)d_ws);
  k2<<<256, 512, 0, stream>>>((const u16*)d_in[8], (const u16*)d_in[11], (const u16*)d_in[12], (const u16*)d_in[13],
                              (char*)d_ws, (u16*)d_out);
  kprobe<<<1, 64, 0, stream>>>((char*)d_ws);
}
```

```cpp
#define DBG_STAMP
#define PK0 2
#define PB0 1
#define PW0 1
#define PK 0
#define PI 3
#define PMODE 0
#define PBO 0
#define PB 512
#define PWO 0
#define PW 8
#include <hip/hip_runtime.h>

typedef unsigned short u16;
typedef unsigned u32;
typedef unsigned long long u64;
typedef u32 v4u __attribute__((ext_vector_type(4)));
typedef u32 v2u __attribute__((ext_vector_type(2)));
typedef float v4f __attribute__((ext_vector_type(4)));
typedef float v2f __attribute__((ext_vector_type(2)));
typedef __bf16 v8bf __attribute__((ext_vector_type(8)));
typedef __bf16 v2bf __attribute__((ext_vector_type(2)));
typedef short v4s __attribute__((ext_vector_type(4)));
#define LDS __attribute__((address_space(3)))
#define GLB __attribute__((address_space(1)))
#define DEVI __device__ __forceinline__

#define WS_XT 0
#define WS_KT (4u << 20)
#define WS_XROW (6u << 20)
#define WS_CAND (8u << 20)
#define WS_PART ((8u << 20) + 65536)
#define NFFN 112
#define SENT 0xFFFFFFFFu

#define WS_STAMP (16u << 20)
#define STAMP(k, i) do { if (lane == 0) { u64 t_; asm volatile("s_memrealtime %0\n\ts_waitcnt lgkmcnt(0)" : "=s"(t_) :: "memory"); \
  ((u64*)(ws + WS_STAMP))[(((k) * 512 + blockIdx.x) * 16 + w) * 8 + (i)] = t_; } } while (0)

DEVI u32 pk(float a, float b) { return __builtin_bit_cast(u32, __builtin_convertvector((v2f){a, b}, v2bf)); }
DEVI float rbf(float a) { return __uint_as_float(pk(0.f, a)); }
DEVI float lo2f(u32 v) { return __uint_as_float(v << 16); }
DEVI float hi2f(u32 v) { return __uint_as_float(v & 0xffff0000u); }
template <int CTRL, int RM> DEVI float dppf(float v) {
  return __int_as_float(__builtin_amdgcn_update_dpp(0, __float_as_int(v), CTRL, RM, 0xf, false)); }
template <int CTRL> DEVI u32 dppu(u32 v) { return (u32)__builtin_amdgcn_update_dpp(0, (int)v, CTRL, 0xf, 0xf, false); }
DEVI float wave_sum(float v) {
  v += dppf<0xB1, 0xf>(v); v += dppf<0x4E, 0xf>(v); v += dppf<0x141, 0xf>(v); v += dppf<0x140, 0xf>(v);
  v += dppf<0x142, 0xa>(v); v += dppf<0x143, 0xc>(v);
  return __int_as_float(__builtin_amdgcn_readlane(__float_as_int(v), 63));
}
DEVI void bar() { asm volatile("s_waitcnt lgkmcnt(0)\n\ts_barrier" ::: "memory"); }
DEVI v4s trd(LDS char* p) { return __builtin_amdgcn_ds_read_tr16_b64_v4i16((LDS v4s*)p); }
DEVI v8bf cat(v4s a, v4s b) { return __builtin_bit_cast(v8bf, __builtin_shufflevector(a, b, 0, 1, 2, 3, 4, 5, 6, 7)); }
DEVI void lds_wait(LDS char* p, u32 n) {
  while (*(volatile LDS u32*)p < n) __builtin_amdgcn_s_sleep(1);
  asm volatile("" ::: "memory");
}
DEVI void lds_sig(LDS char* p, int lane) {
  asm volatile("s_waitcnt lgkmcnt(0)" ::: "memory");
  if (lane == 0) __hip_atomic_fetch_add((LDS u32*)p, 1u, __ATOMIC_RELAXED, __HIP_MEMORY_SCOPE_WORKGROUP);
}

struct cf { float r, i; };
DEVI cf cmul(cf a, cf b) { return {a.r * b.r - a.i * b.i, a.r * b.i + a.i * b.r}; }
DEVI cf cpow(float er, float ph, float s) {
  float e = __builtin_amdgcn_exp2f(er * s), f = __builtin_amdgcn_fractf(ph * s);
  return {e * __builtin_amdgcn_cosf(f), e * __builtin_amdgcn_sinf(f)};
}
#define GROW 528
#define GCH (63 * GROW)
template <int N> DEVI void chain(LDS char* p, int row, cf x, cf m) {
#pragma unroll
  for (int j = 0; j < N; j++) { *(LDS u32*)(p + (row + j) * GROW) = pk(x.r, x.i); if (j + 1 < N) x = cmul(x, m); }
}

#define K1_LDS (2 * GCH)
__global__ void __launch_bounds__(512) k1(const u16* __restrict__ x, const u16* __restrict__ wr, const u16* __restrict__ Are,
    const u16* __restrict__ Aim, const u16* __restrict__ Bre, const u16* __restrict__ Bim, const u16* __restrict__ Cre,
    const u16* __restrict__ Cim, const u16* __restrict__ logdt, char* __restrict__ ws) {
  __shared__ __attribute__((aligned(16))) char smem[K1_LDS];
  LDS char* L = (LDS char*)smem;
  const int lane = threadIdx.x & 63, w = __builtin_amdgcn_readfirstlane(threadIdx.x >> 6);
  const int b = blockIdx.x & 255, sw = (b & 7) * 32 + (b >> 3);
  STAMP(0, 0);
  if (blockIdx.x < 256) {
    const int t = sw * 8 + w;
    v4u xr = *(const v4u*)(x + t * 512 + lane * 8);
    v4u wv = *(const v4u*)(wr + lane * 8);
    if (w < 2 && threadIdx.x < NFFN) *(v4u*)(ws + WS_PART + (b * NFFN + threadIdx.x) * 16) = (v4u){SENT, SENT, SENT, SENT};
    float v[8];
#pragma unroll
    for (int j = 0; j < 4; j++) { v[2 * j] = lo2f(xr[j]); v[2 * j + 1] = hi2f(xr[j]); }
    float s = ((v[0] + v[1]) + (v[2] + v[3])) + ((v[4] + v[5]) + (v[6] + v[7]));
    STAMP(0, 1);
    float mean = wave_sum(s) * (1.f / 512.f);
    float q = 0.f;
#pragma unroll
    for (int j = 0; j < 8; j++) { float d = v[j] - mean; q += d * d; }
    float var = rbf(wave_sum(q) * (1.f / 512.f));
    float mb = rbf(mean);
    float rs = __builtin_amdgcn_rcpf(rbf(__builtin_amdgcn_sqrtf(rbf(var + 1e-5f))));
    v4u xn; float acc = 0.f;
#pragma unroll
    for (int j = 0; j < 4; j++) {
      u32 xj = pk(rbf(v[2 * j] - mb) * rs, rbf(v[2 * j + 1] - mb) * rs), wj = wv[j];
      xn[j] = xj;
      acc += lo2f(xj) * lo2f(wj) + hi2f(xj) * hi2f(wj);
    }
    *(LDS v4u*)(L + w * 2112 + lane * 32) = (v4u){xn[0], xr[0], xn[1], xr[1]};
    *(LDS v4u*)(L + w * 2112 + lane * 32 + 16) = (v4u){xn[2], xr[2], xn[3], xr[3]};
    u32 rb = __float_as_uint(rbf(wave_sum(acc)));
    u32 key = ((rb & 0x80000000u) ? (~rb & 0xffff0000u) : (rb | 0x80000000u)) | (u32)(2047 - t);
    if (lane == 0) *(LDS u32*)(L + 17408 + w * 4) = key;
    STAMP(0, 2);
    bar();
    {
      LDS char* p = L + ((lane >> 2) & 3) * 2112 + w * 256 + (lane >> 4) * 32 + (lane & 3) * 8;
      v4s a0 = trd(p), a1 = trd(p + 4 * 2112), b0 = trd(p + 128), b1 = trd(p + 4 * 2112 + 128);
      char* dst = ws + WS_XT + (size_t)(w * 32 + (lane >> 2)) * 16384 + sw * 64 + (lane & 3) * 16;
      *(v8bf*)dst = cat(a0, a1);
      *(v8bf*)(dst + 16 * 16384) = cat(b0, b1);
    }
    v4u k0 = *(LDS v4u*)(L + 17408), k1v = *(LDS v4u*)(L + 17424);
    int rank = 0;
#pragma unroll
    for (int j = 0; j < 4; j++) rank += (k0[j] > key) + (k1v[j] > key);
    if (rank < 2) {
      *(v4u*)(ws + WS_XROW + t * 1024 + lane * 16) = xn;
      if (lane == 0) *(u32*)(ws + WS_CAND + b * 8 + rank * 4) = key;
    }
    STAMP(0, 3);
  } else {
    const int ch = w >> 2, d = sw * 2 + ch;
    {
      const int n = ((w >> 1) & 1) * 64 + lane, idx = d * 128 + n;
      float are = lo2f(Are[idx]), aim = lo2f(Aim[idx]), bre = lo2f(Bre[idx]), bim = lo2f(Bim[idx]), cre = lo2f(Cre[idx]), cim = lo2f(Cim[idx]);
      float dt = rbf(expf(lo2f(logdt[d])));
      float er = dt * are * 1.44269504089f, ph = dt * aim * 0.15915494309f;
      STAMP(0, 1);
      cf cb = cmul({cre, cim}, {dt * bre, dt * bim});
      cf E = cpow(er, ph, 256.f);
      LDS char* p = L + ch * GCH + n * 4;
      if (w & 1) {
        cf z4 = cpow(er, ph, 4.f), z8 = cmul(z4, z4), z16 = cmul(z8, z8);
        cf xx = cmul(cb, cpow(er, ph, 768.f));
        chain<5>(p, 30, xx, z4);
#pragma unroll
        for (int a = 0; a < 4; a++) { xx = cmul(xx, E); chain<3>(p, 35 + 3 * a, xx, z8); }
        z16.i = -z16.i;
        *(LDS u32*)(p + 47 * GROW) = 0x3f80u;
        chain<15>(p, 48, z16, z16);
      } else {
        cf z = cpow(er, ph, 1.f), z2 = cmul(z, z), z4 = cmul(z2, z2);
        chain<16>(p, 0, cb, z);
        cf xx = cmul(cb, E);
        chain<9>(p, 16, xx, z2);
        chain<5>(p, 25, cmul(xx, E), z4);
      }
    }
    STAMP(0, 2);
    bar();
    const int T = w & 3, r = lane & 15, kg = lane >> 4, i2 = min(r & 3, 2);
    int row = T == 0 ? r : T == 1 ? 16 + 2 * (r >> 2) + max((r & 3) - 1, 0) : T == 2 ? 25 + 5 * (r >> 3) + 2 * ((r >> 2) & 1) + i2 : 35 + 3 * (r >> 2) + i2;
    LDS char* pa = L + ch * GCH + row * GROW + kg * 16;
    LDS char* pb = L + ch * GCH + (47 + r) * GROW + kg * 16;
    v4f acc = {0.f, 0.f, 0.f, 0.f};
#pragma unroll
    for (int ks = 0; ks < 8; ks++)
      acc = __builtin_amdgcn_mfma_f32_16x16x32_bf16(*(LDS v8bf*)(pa + ks * 64), *(LDS v8bf*)(pb + ks * 64), acc, 0, 0, 0);
    char* dst = ws + WS_KT + d * 4096 + r * 32;
    if (T < 2) {
      *(v2u*)(dst + T * 512 + kg * 8) = (v2u){pk(acc[0], acc[1]), pk(acc[2], acc[3])};
    } else {
      u32 p00 = pk(acc[0], acc[0]), p01 = pk(acc[0], acc[1]), p11 = pk(acc[1], acc[1]), p22 = pk(acc[2], acc[2]);
      if (T == 2) *(v4u*)(dst + 1024 + (kg >> 1) * 512 + (kg & 1) * 16) = (v4u){p00, p01, p11, p22};
      else { *(v4u*)(dst + 2048 + kg * 512) = (v4u){p00, p00, p01, p11}; *(v4u*)(dst + 2048 + kg * 512 + 16) = (v4u){p11, p11, p22, p22}; }
    }
    STAMP(0, 3);
  }
}

#define U_CP 4160
#define U_CH 16704
#define O_U 0
#define O_K (2 * U_CH)
#define O_P (O_K + 2 * 4608)
#define O_F (O_P + 32768)
#define O_W (O_F + 2048)
#define K2_LDS (O_W + 49152)

DEVI void merge2(u32& a0, u32& a1, u32 b0, u32 b1) { u32 m = min(a0, b0); a0 = max(a0, b0); a1 = max(m, max(a1, b1)); }

__global__ void __launch_bounds__(512) k2(const u16* __restrict__ dskip, const u16* __restrict__ wg, const u16* __restrict__ wu,
    const u16* __restrict__ wd, char* __restrict__ ws, u16* __restrict__ out) {
  __shared__ __attribute__((aligned(16))) char smem[K2_LDS];
  LDS char* L = (LDS char*)smem;
  const int tid = threadIdx.x, lane = tid & 63, w = __builtin_amdgcn_readfirstlane(tid >> 6);
  const int b = blockIdx.x, pair = (b & 7) * 32 + (b >> 3);
  const int slice = (w + 4) & 7;
  STAMP(1, 0);
  v4u c0 = *(const v4u*)(ws + WS_CAND + lane * 32), c1 = *(const v4u*)(ws + WS_CAND + lane * 32 + 16);
  const int tb8 = slice * 32 + (lane >> 1), ch = lane & 1;
  const char* xt = ws + WS_XT + pair * 16384 + tb8 * 64 + ch * 16;
  v4u un = *(const v4u*)xt;
  v2u pv = *(const v2u*)(xt + (tb8 ? -56 : 0));
  v4u kv = *(const v4u*)(ws + WS_KT + pair * 8192 + tid * 16);
  v4u xr = *(const v4u*)(xt + 32);
  u32 dsk = *(const u32*)(dskip + pair * 2);
  const bool ffn = (w >= 4) && ((b >> 3) < 14);
  const int fs = (b & 7) * 14 + (b >> 3), fw = w & 3;
  if (ffn) {
    const int rho = lane >> 1, kk = 8 * ((rho >> 2) & 3) + 4 * (rho >> 4) + (rho & 3);
    const size_t go = (size_t)(fw * 128 + kk) * 3584 + fs * 32 + (lane & 1) * 16;
    LDS char* wl = L + O_W + fw * 12288;
#pragma unroll
    for (int ks = 0; ks < 4; ks++) {
      __builtin_amdgcn_global_load_lds((const GLB void*)((const char*)wg + go + ks * 32 * 3584), (LDS void*)(wl + ks * 1024), 16, 0, 0);
      __builtin_amdgcn_global_load_lds((const GLB void*)((const char*)wu + go + ks * 32 * 3584), (LDS void*)(wl + 4096 + ks * 1024), 16, 0, 0);
    }
    const size_t dofs = (size_t)(fs * 16 + (lane >> 2)) * 1024 + fw * 256 + (lane & 3) * 16;
#pragma unroll
    for (int j = 0; j < 4; j++)
      __builtin_amdgcn_global_load_lds((const GLB void*)((const char*)wd + dofs + j * 64), (LDS void*)(wl + 8192 + j * 1024), 16, 0, 0);
  }
  if (w == 0) *(LDS v4u*)(L + O_K + (lane >> 5) * 4608 + (lane & 31) * 16) = (v4u){0, 0, 0, 0};
  if (w == 1 && lane < 16) *(LDS v4u*)(L + O_U + (lane >> 3) * U_CH + ((lane >> 1) & 3) * U_CP + (lane & 1) * 16) = (v4u){0, 0, 0, 0};
  if (w == 2 && lane < 4) *(LDS v4u*)(L + O_F + lane * 16) = (v4u){0, 0, 0, 0};
  u32 k0 = c0[0], k1v = c0[1];
  merge2(k0, k1v, c0[2], c0[3]); merge2(k0, k1v, c1[0], c1[1]); merge2(k0, k1v, c1[2], c1[3]);
  merge2(k0, k1v, dppu<0xB1>(k0), dppu<0xB1>(k1v));
  merge2(k0, k1v, dppu<0x4E>(k0), dppu<0x4E>(k1v));
  merge2(k0, k1v, dppu<0x141>(k0), dppu<0x141>(k1v));
  merge2(k0, k1v, dppu<0x140>(k0), dppu<0x140>(k1v));
  u32 s0 = __builtin_amdgcn_readlane(k0, 0), s1 = __builtin_amdgcn_readlane(k1v, 0);
  merge2(s0, s1, __builtin_amdgcn_readlane(k0, 16), __builtin_amdgcn_readlane(k1v, 16));
  merge2(s0, s1, __builtin_amdgcn_readlane(k0, 32), __builtin_amdgcn_readlane(k1v, 32));
  merge2(s0, s1, __builtin_amdgcn_readlane(k0, 48), __builtin_amdgcn_readlane(k1v, 48));
  const int ts0 = 2047 - (s0 & 2047), ts1 = 2047 - (s1 & 2047);
  STAMP(1, 1);
  v4u xf[4];
  if (ffn) {
    const char* xp = ws + WS_XROW + ((lane & 1) ? ts1 : ts0) * 1024 + fw * 256 + (lane >> 4) * 16;
#pragma unroll
    for (int ks = 0; ks < 4; ks++) xf[ks] = *(const v4u*)(xp + ks * 64);
  }
  {
    if (tb8 == 0) pv = (v2u){0, 0};
    LDS char* p = L + O_U + ch * U_CH + 32 + tb8 * 16;
    u32 e0 = __builtin_amdgcn_alignbit(pv[1], pv[0], 16), e1 = __builtin_amdgcn_alignbit(un[0], pv[1], 16),
        e2 = __builtin_amdgcn_alignbit(un[1], un[0], 16), e3 = __builtin_amdgcn_alignbit(un[2], un[1], 16),
        e4 = __builtin_amdgcn_alignbit(un[3], un[2], 16);
    *(LDS v4u*)p = un;
    *(LDS v4u*)(p + U_CP) = (v4u){e1, e2, e3, e4};
    *(LDS v4u*)(p + 2 * U_CP) = (v4u){pv[1], un[0], un[1], un[2]};
    *(LDS v4u*)(p + 3 * U_CP) = (v4u){e0, e1, e2, e3};
    *(LDS v4u*)(L + O_K + (w >> 2) * 4608 + 512 + (tid & 255) * 16) = kv;
  }
  STAMP(1, 2);
  bar();
  STAMP(1, 3);
  float base[2][4];
  {
    u32 sn0 = ch ? un[0] : un[2], sn1 = ch ? un[1] : un[3], sx0 = ch ? xr[0] : xr[2], sx1 = ch ? xr[1] : xr[3];
    u32 kn0 = ch ? un[2] : un[0], kn1 = ch ? un[3] : un[1], kx0 = ch ? xr[2] : xr[0], kx1 = ch ? xr[3] : xr[1];
    u32 rn0 = dppu<0xB1>(sn0), rn1 = dppu<0xB1>(sn1), rx0 = dppu<0xB1>(sx0), rx1 = dppu<0xB1>(sx1);
    float dk = 1.f + (ch ? hi2f(dsk) : lo2f(dsk)), dr = 1.f + (ch ? lo2f(dsk) : hi2f(dsk));
    float bk[4] = {lo2f(kx0) + lo2f(kn0) * dk, hi2f(kx0) + hi2f(kn0) * dk, lo2f(kx1) + lo2f(kn1) * dk, hi2f(kx1) + hi2f(kn1) * dk};
    float br[4] = {lo2f(rx0) + lo2f(rn0) * dr, hi2f(rx0) + hi2f(rn0) * dr, lo2f(rx1) + lo2f(rn1) * dr, hi2f(rx1) + hi2f(rn1) * dr};
#pragma unroll
    for (int i = 0; i < 4; i++) { base[0][i] = ch ? br[i] : bk[i]; base[1][i] = ch ? bk[i] : br[i]; }
  }
  const int c16 = lane & 15, kg = lane >> 4;
  if (w < 4) {
    const int mc = w & 1, kh = w >> 1;
    LDS char* pa = L + O_U + mc * U_CH + ((lane >> 2) & 3) * U_CP + 32 - 16 - 8 + kg * 16 + (lane & 3) * 8 + kh * 256;
    LDS char* pb = L + O_K + mc * 4608 + 528 + c16 * 32 - kg * 16 - kh * 256 - 192;
    LDS char* pw = L + O_P + w * 8192 + c16 * 64 + 16 * (kg ^ ((c16 >> 1) & 3));
    v4f acc[4];
#pragma unroll
    for (int s = 0; s < 4; s++) acc[s] = (v4f){0.f, 0.f, 0.f, 0.f};
#pragma unroll
    for (int js = 0; js < 4; js++) {
      v8bf A[4], B[4];
#pragma unroll
      for (int a = 0; a < 4; a++) {
        A[a] = cat(trd(pa + a * 512 + js * 64 + 8), trd(pa + a * 512 + js * 64));
        B[a] = *(LDS v8bf*)(pb + a * 512 + (3 - js) * 64);
      }
#pragma unroll
      for (int s = 0; s < 4; s++)
#pragma unroll
        for (int a = 0; a <= s; a++) acc[s] = __builtin_amdgcn_mfma_f32_16x16x32_bf16(A[a], B[s - a], acc[s], 0, 0, 0);
    }
#pragma unroll
    for (int s = 0; s < 4; s++) { *(LDS v4f*)(pw + s * 1024) = acc[s]; acc[s] = (v4f){0.f, 0.f, 0.f, 0.f}; }
    lds_sig(L + O_F, lane);
    STAMP(1, 4);
#pragma unroll
    for (int js = 0; js < 4; js++) {
      v8bf A[8], B[8];
#pragma unroll
      for (int a = 0; a < 8; a++) {
        A[a] = cat(trd(pa + a * 512 + js * 64 + 8), trd(pa + a * 512 + js * 64));
        B[a] = *(LDS v8bf*)(pb + a * 512 + (3 - js) * 64);
      }
#pragma unroll
      for (int s = 0; s < 4; s++)
#pragma unroll
        for (int a = 0; a <= s + 4; a++) acc[s] = __builtin_amdgcn_mfma_f32_16x16x32_bf16(A[a], B[s + 4 - a], acc[s], 0, 0, 0);
    }
#pragma unroll
    for (int s = 0; s < 4; s++) *(LDS v4f*)(pw + 4096 + s * 1024) = acc[s];
    lds_sig(L + O_F + 4, lane);
    STAMP(1, 5);
    lds_wait(L + O_F + 4, 4);
  } else {
    if (ffn) {
      LDS char* wl = L + O_W + fw * 12288 + lane * 8;
      v4f g = {0.f, 0.f, 0.f, 0.f}, u = {0.f, 0.f, 0.f, 0.f};
#pragma unroll
      for (int ks = 0; ks < 4; ks++) {
        v8bf xb = __builtin_bit_cast(v8bf, xf[ks]);
        g = __builtin_amdgcn_mfma_f32_16x16x32_bf16(cat(trd(wl + ks * 1024), trd(wl + ks * 1024 + 512)), xb, g, 0, 0, 0);
        u = __builtin_amdgcn_mfma_f32_16x16x32_bf16(cat(trd(wl + 4096 + ks * 1024), trd(wl + 4096 + ks * 1024 + 512)), xb, u, 0, 0, 0);
      }
      v4s wdf[8];
      LDS char* dl = L + O_W + fw * 12288 + 8192 + (lane >> 2) * 64 + (lane & 3) * 8;
#pragma unroll
      for (int j = 0; j < 8; j++) wdf[j] = trd(dl + (j >> 1) * 1024 + (j & 1) * 32);
      STAMP(1, 4);
      LDS char* ex = L + O_F + 1024 + (kg * 2 + (lane & 1)) * 32;
      if (c16 < 2) { *(LDS v4f*)(ex + fw * 256) = g; *(LDS v4f*)(ex + fw * 256 + 16) = u; }
      lds_sig(L + O_F + 8, lane);
      lds_wait(L + O_F + 8, 4);
      g = *(LDS v4f*)ex; u = *(LDS v4f*)(ex + 16);
#pragma unroll
      for (int j = 1; j < 4; j++) { g += *(LDS v4f*)(ex + j * 256); u += *(LDS v4f*)(ex + j * 256 + 16); }
      float h[4];
#pragma unroll
      for (int i = 0; i < 4; i++) {
        float gg = rbf(g[i]);
        float sg = rbf(gg * __builtin_amdgcn_rcpf(1.f + __builtin_amdgcn_exp2f(gg * -1.44269504089f)));
        h[i] = sg * rbf(u[i]);
      }
      v4s hf = __builtin_bit_cast(v4s, (v2u){pk(h[0], h[1]), pk(h[2], h[3])});
      v4f o[8];
#pragma unroll
      for (int j = 0; j < 8; j++) o[j] = __builtin_amdgcn_mfma_f32_16x16x16bf16_1k(hf, wdf[j], (v4f){0.f, 0.f, 0.f, 0.f}, 0, 0, 0);
      __amdgpu_buffer_rsrc_t rs = __builtin_amdgcn_make_buffer_rsrc(ws + WS_PART, 0, NFFN * 4096, 0x00020000);
#pragma unroll
      for (int hh = 0; hh < 2; hh++) {
        v4f e = kg == 0 ? o[4 * hh] : kg == 1 ? o[4 * hh + 1] : kg == 2 ? o[4 * hh + 2] : o[4 * hh + 3];
        u32 e0 = __float_as_uint(e[0]), e1 = __float_as_uint(e[1]);
        e0 = e0 == SENT ? 0x7fc00000u : e0; e1 = e1 == SENT ? 0x7fc00000u : e1;
        __builtin_amdgcn_raw_buffer_store_b64((v2u){e0, e1}, rs, fs * 4096 + fw * 1024 + hh * 512 + lane * 8, 0, 16);
      }
      STAMP(1, 5);
    }
    lds_wait(L + O_F, 4);
  }
  float sv[2][2] = {{0.f, 0.f}, {0.f, 0.f}};
  {
    LDS char* pr = L + O_P + slice * 1024 + (lane >> 2) * 64 + 16 * ((lane & 3) ^ ((lane >> 3) & 3));
    v4f y0 = *(LDS v4f*)pr + *(LDS v4f*)(pr + 16384), y1 = *(LDS v4f*)(pr + 8192) + *(LDS v4f*)(pr + 24576);
    const int t0 = slice * 256 + lane * 4;
    char* op = (char*)out + t0 * 1024 + pair * 4;
#pragma unroll
    for (int i = 0; i < 4; i++) {
      float o0 = base[0][i] + y0[i], o1 = base[1][i] + y1[i];
      if (t0 + i == ts0) { sv[0][0] = o0; sv[0][1] = o1; }
      else if (t0 + i == ts1) { sv[1][0] = o0; sv[1][1] = o1; }
      else *(u32*)(op + i * 1024) = pk(o0, o1);
    }
  }
  STAMP(1, 6);
  if (w == 7) {
    __amdgpu_buffer_rsrc_t rs = __builtin_amdgcn_make_buffer_rsrc(ws + WS_PART, 0, NFFN * 4096, 0x00020000);
    const int j2 = lane < NFFN - 64 ? lane + 64 : lane;
    v4u a, c;
    for (int spin = 0; spin < 20000; spin++) {
      asm volatile("" ::: "memory");
      a = __builtin_amdgcn_raw_buffer_load_b128(rs, lane * 4096 + pair * 16, 0, 16);
      c = __builtin_amdgcn_raw_buffer_load_b128(rs, j2 * 4096 + pair * 16, 0, 16);
      bool bad = a[0] == SENT || a[1] == SENT || a[2] == SENT || a[3] == SENT || c[0] == SENT || c[1] == SENT || c[2] == SENT || c[3] == SENT;
      if (!__any(bad)) break;
      __builtin_amdgcn_s_sleep(2);
    }
    float up[4];
#pragma unroll
    for (int i = 0; i < 4; i++) {
      float v = __uint_as_float(a[i]) + (lane < NFFN - 64 ? __uint_as_float(c[i]) : 0.f);
      u32 kk = (i & 1) ? s1 : s0;
      float r = __uint_as_float(((kk & 0x80000000u) ? (kk & 0x7fffffffu) : ~kk) & 0xffff0000u);
      float sg = rbf(__builtin_amdgcn_rcpf(1.f + __builtin_amdgcn_exp2f(r * -1.44269504089f)));
      up[i] = rbf(rbf(wave_sum(v)) * sg);
    }
    if (lane == 0) *(LDS v4f*)(L + O_F + 16) = (v4f){up[0], up[1], up[2], up[3]};
    lds_sig(L + O_F + 12, lane);
  }
  if (slice == (ts0 >> 8) || slice == (ts1 >> 8)) {
    lds_wait(L + O_F + 12, 1);
    v4f up = *(LDS v4f*)(L + O_F + 16);
    const int tq = slice * 64 + lane;
    if (tq == (ts0 >> 2)) *(u32*)((char*)out + ts0 * 1024 + pair * 4) = pk(sv[0][0] + up[0], sv[0][1] + up[2]);
    if (tq == (ts1 >> 2)) *(u32*)((char*)out + ts1 * 1024 + pair * 4) = pk(sv[1][0] + up[1], sv[1][1] + up[3]);
  }
  STAMP(1, 7);
}

__global__ void __launch_bounds__(64) k0(char* ws) {
  const int lane = threadIdx.x, w = 0;
  STAMP(2, 0);
}

__global__ void __launch_bounds__(64) kprobe(char* ws) {
  u64 ts; asm volatile("s_memrealtime %0\n\ts_waitcnt lgkmcnt(0)" : "=s"(ts) :: "memory");
  const int lane = threadIdx.x;
  const u64* st = (const u64*)(ws + WS_STAMP);
  u64 t0 = ~0ull, mn = ~0ull, mx = 0;
  for (int j = lane; j < PB0 * PW0; j += 64) { u64 v = st[((PK0 * 512 + j / PW0) * 16 + j % PW0) * 8 + 0]; t0 = v < t0 ? v : t0; }
  for (int j = lane; j < PB * PW; j += 64) { u64 v = st[((PK * 512 + PBO + j / PW) * 16 + PWO + j % PW) * 8 + PI]; mn = v < mn ? v : mn; mx = v > mx ? v : mx; }
  for (int o = 32; o; o >>= 1) {
    u64 a = __shfl_xor(t0, o), b = __shfl_xor(mn, o), c = __shfl_xor(mx, o);
    t0 = a < t0 ? a : t0; mn = b < mn ? b : mn; mx = c > mx ? c : mx;
  }
  u64 x = (PMODE == 0 ? t0 : PMODE == 1 ? mn : PMODE == 2 ? mx : ts) - t0;
  if (x > 5000) x = 5000;
  u64 t1; asm volatile("s_memrealtime %0\n\ts_waitcnt lgkmcnt(0)" : "=s"(t1) :: "memory");
  u64 end = ts + 800 + 4 * x;
  for (int i = 0; i < 2000000; i++) {
    asm volatile("s_memrealtime %0\n\ts_waitcnt lgkmcnt(0)" : "=s"(t1) :: "memory");
    if (t1 >= end) break;
  }
}

extern "C" void kernel_launch(void* const* d_in, const int* in_sizes, int n_in, void* d_out, int out_size, void* d_ws,
                              size_t ws_size, hipStream_t stream) {
  k0<<<1, 64, 0, stream>>>((char*)d_ws);
  k1<<<512, 512, 0, stream>>>((const u16*)d_in[0], (const u16*)d_in[10], (const u16*)d_in[1], (const u16*)d_in[2],
                              (const u16*)d_in[3], (const u16*)d_in[4], (const u16*)d_in[5], (const u16*)d_in[6],
                              (const u16*)d_in[7], (char*)d_ws);
  k2<<<256, 512, 0, stream>>>((const u16*)d_in[8], (const u16*)d_in[11], (const u16*)d_in[12], (const u16*)d_in[13],
                              (char*)d_ws, (u16*)d_out);
  kprobe<<<1, 64, 0, stream>>>((char*)d_ws);
}
```

```cpp
#define DBG_STAMP
#define PK0 2
#define PB0 1
#define PW0 1
#define PK 0
#define PI 3
#define PMODE 0
#define PBO 0
#define PB 512
#define PWO 0
#define PW 8
#include <hip/hip_runtime.h>

typedef unsigned short u16;
typedef unsigned u32;
typedef unsigned long long u64;
typedef u32 v4u __attribute__((ext_vector_type(4)));
typedef u32 v2u __attribute__((ext_vector_type(2)));
typedef float v4f __attribute__((ext_vector_type(4)));
typedef float v2f __attribute__((ext_vector_type(2)));
typedef __bf16 v8bf __attribute__((ext_vector_type(8)));
typedef __bf16 v2bf __attribute__((ext_vector_type(2)));
typedef short v4s __attribute__((ext_vector_type(4)));
#define LDS __attribute__((address_space(3)))
#define GLB __attribute__((address_space(1)))
#define DEVI __device__ __forceinline__

#define WS_XT 0
#define WS_KT (4u << 20)
#define WS_XROW (6u << 20)
#define WS_CAND (8u << 20)
#define WS_PART ((8u << 20) + 65536)
#define NFFN 112
#define SENT 0xFFFFFFFFu

#define WS_STAMP (16u << 20)
#define STAMP(k, i) do { if (lane == 0) { u64 t_; asm volatile("s_memrealtime %0\n\ts_waitcnt lgkmcnt(0)" : "=s"(t_) :: "memory"); \
  ((u64*)(ws + WS_STAMP))[(((k) * 512 + blockIdx.x) * 16 + w) * 8 + (i)] = t_; } } while (0)
#define STAMPT u64 tt_; asm volatile("s_memrealtime %0" : "=s"(tt_) :: "memory");
#define STAMPW(k, i) do { if (lane == 0) { asm volatile("s_waitcnt lgkmcnt(0)" : "+s"(tt_) :: "memory"); \
  ((u64*)(ws + WS_STAMP))[(((k) * 512 + blockIdx.x) * 16 + w) * 8 + (i)] = tt_; } } while (0)

DEVI u32 pk(float a, float b) { return __builtin_bit_cast(u32, __builtin_convertvector((v2f){a, b}, v2bf)); }
DEVI float rbf(float a) { return __uint_as_float(pk(0.f, a)); }
DEVI float lo2f(u32 v) { return __uint_as_float(v << 16); }
DEVI float hi2f(u32 v) { return __uint_as_float(v & 0xffff0000u); }
template <int CTRL, int RM> DEVI float dppf(float v) {
  return __int_as_float(__builtin_amdgcn_update_dpp(0, __float_as_int(v), CTRL, RM, 0xf, false)); }
template <int CTRL> DEVI u32 dppu(u32 v) { return (u32)__builtin_amdgcn_update_dpp(0, (int)v, CTRL, 0xf, 0xf, false); }
DEVI float wave_sum(float v) {
  v += dppf<0xB1, 0xf>(v); v += dppf<0x4E, 0xf>(v); v += dppf<0x141, 0xf>(v); v += dppf<0x140, 0xf>(v);
  v += dppf<0x142, 0xa>(v); v += dppf<0x143, 0xc>(v);
  return __int_as_float(__builtin_amdgcn_readlane(__float_as_int(v), 63));
}
DEVI void bar() { asm volatile("s_waitcnt lgkmcnt(0)\n\ts_barrier" ::: "memory"); }
DEVI v4s trd(LDS char* p) { return __builtin_amdgcn_ds_read_tr16_b64_v4i16((LDS v4s*)p); }
DEVI v8bf cat(v4s a, v4s b) { return __builtin_bit_cast(v8bf, __builtin_shufflevector(a, b, 0, 1, 2, 3, 4, 5, 6, 7)); }
DEVI void lds_wait(LDS char* p, u32 n) {
  while (*(volatile LDS u32*)p < n) __builtin_amdgcn_s_sleep(1);
  asm volatile("" ::: "memory");
}
DEVI void lds_sig(LDS char* p, int lane) {
  asm volatile("s_waitcnt lgkmcnt(0)" ::: "memory");
  if (lane == 0) __hip_atomic_fetch_add((LDS u32*)p, 1u, __ATOMIC_RELAXED, __HIP_MEMORY_SCOPE_WORKGROUP);
}

struct cf { float r, i; };
DEVI cf cmul(cf a, cf b) { return {a.r * b.r - a.i * b.i, a.r * b.i + a.i * b.r}; }
DEVI cf cpow(float er, float ph, float s) {
  float e = __builtin_amdgcn_exp2f(er * s), f = __builtin_amdgcn_fractf(ph * s);
  return {e * __builtin_amdgcn_cosf(f), e * __builtin_amdgcn_sinf(f)};
}
#define GROW 528
#define GCH (63 * GROW)
template <int N> DEVI void chain(LDS char* p, int row, cf x, cf m) {
#pragma unroll
  for (int j = 0; j < N; j++) { *(LDS u32*)(p + (row + j) * GROW) = pk(x.r, x.i); if (j + 1 < N) x = cmul(x, m); }
}

#define K1_LDS (2 * GCH)
__global__ void __launch_bounds__(512) k1(const u16* __restrict__ x, const u16* __restrict__ wr, const u16* __restrict__ Are,
    const u16* __restrict__ Aim, const u16* __restrict__ Bre, const u16* __restrict__ Bim, const u16* __restrict__ Cre,
    const u16* __restrict__ Cim, const u16* __restrict__ logdt, char* __restrict__ ws) {
  STAMPT
  __shared__ __attribute__((aligned(16))) char smem[K1_LDS];
  LDS char* L = (LDS char*)smem;
  const int lane = threadIdx.x & 63, w = __builtin_amdgcn_readfirstlane(threadIdx.x >> 6);
  const int b = blockIdx.x & 255, sw = (b & 7) * 32 + (b >> 3);
  STAMP(0, 0);
  STAMPW(0, 4);
  if (blockIdx.x < 256) {
    const int t = sw * 8 + w;
    v4u xr = *(const v4u*)(x + t * 512 + lane * 8);
    v4u wv = *(const v4u*)(wr + lane * 8);
    if (w < 2 && threadIdx.x < NFFN) *(v4u*)(ws + WS_PART + (b * NFFN + threadIdx.x) * 16) = (v4u){SENT, SENT, SENT, SENT};
    float v[8];
#pragma unroll
    for (int j = 0; j < 4; j++) { v[2 * j] = lo2f(xr[j]); v[2 * j + 1] = hi2f(xr[j]); }
    float s = ((v[0] + v[1]) + (v[2] + v[3])) + ((v[4] + v[5]) + (v[6] + v[7]));
    STAMP(0, 1);
    float mean = wave_sum(s) * (1.f / 512.f);
    float q = 0.f;
#pragma unroll
    for (int j = 0; j < 8; j++) { float d = v[j] - mean; q += d * d; }
    float var = rbf(wave_sum(q) * (1.f / 512.f));
    float mb = rbf(mean);
    float rs = __builtin_amdgcn_rcpf(rbf(__builtin_amdgcn_sqrtf(rbf(var + 1e-5f))));
    v4u xn; float acc = 0.f;
#pragma unroll
    for (int j = 0; j < 4; j++) {
      u32 xj = pk(rbf(v[2 * j] - mb) * rs, rbf(v[2 * j + 1] - mb) * rs), wj = wv[j];
      xn[j] = xj;
      acc += lo2f(xj) * lo2f(wj) + hi2f(xj) * hi2f(wj);
    }
    *(LDS v4u*)(L + w * 2112 + lane * 32) = (v4u){xn[0], xr[0], xn[1], xr[1]};
    *(LDS v4u*)(L + w * 2112 + lane * 32 + 16) = (v4u){xn[2], xr[2], xn[3], xr[3]};
    u32 rb = __float_as_uint(rbf(wave_sum(acc)));
    u32 key = ((rb & 0x80000000u) ? (~rb & 0xffff0000u) : (rb | 0x80000000u)) | (u32)(2047 - t);
    if (lane == 0) *(LDS u32*)(L + 17408 + w * 4) = key;
    STAMP(0, 2);
    bar();
    {
      LDS char* p = L + ((lane >> 2) & 3) * 2112 + w * 256 + (lane >> 4) * 32 + (lane & 3) * 8;
      v4s a0 = trd(p), a1 = trd(p + 4 * 2112), b0 = trd(p + 128), b1 = trd(p + 4 * 2112 + 128);
      char* dst = ws + WS_XT + (size_t)(w * 32 + (lane >> 2)) * 16384 + sw * 64 + (lane & 3) * 16;
      *(v8bf*)dst = cat(a0, a1);
      *(v8bf*)(dst + 16 * 16384) = cat(b0, b1);
    }
    v4u k0 = *(LDS v4u*)(L + 17408), k1v = *(LDS v4u*)(L + 17424);
    int rank = 0;
#pragma unroll
    for (int j = 0; j < 4; j++) rank += (k0[j] > key) + (k1v[j] > key);
    if (rank < 2) {
      *(v4u*)(ws + WS_XROW + t * 1024 + lane * 16) = xn;
      if (lane == 0) *(u32*)(ws + WS_CAND + b * 8 + rank * 4) = key;
    }
    STAMP(0, 3);
  } else {
    const int ch = w >> 2, d = sw * 2 + ch;
    {
      const int n = ((w >> 1) & 1) * 64 + lane, idx = d * 128 + n;
      float are = lo2f(Are[idx]), aim = lo2f(Aim[idx]), bre = lo2f(Bre[idx]), bim = lo2f(Bim[idx]), cre = lo2f(Cre[idx]), cim = lo2f(Cim[idx]);
      float dt = rbf(expf(lo2f(logdt[d])));
      float er = dt * are * 1.44269504089f, ph = dt * aim * 0.15915494309f;
      STAMP(0, 1);
      cf cb = cmul({cre, cim}, {dt * bre, dt * bim});
      cf E = cpow(er, ph, 256.f);
      LDS char* p = L + ch * GCH + n * 4;
      if (w & 1) {
        cf z4 = cpow(er, ph, 4.f), z8 = cmul(z4, z4), z16 = cmul(z8, z8);
        cf xx = cmul(cb, cpow(er, ph, 768.f));
        chain<5>(p, 30, xx, z4);
#pragma unroll
        for (int a = 0; a < 4; a++) { xx = cmul(xx, E); chain<3>(p, 35 + 3 * a, xx, z8); }
        z16.i = -z16.i;
        *(LDS u32*)(p + 47 * GROW) = 0x3f80u;
        chain<15>(p, 48, z16, z16);
      } else {
        cf z = cpow(er, ph, 1.f), z2 = cmul(z, z), z4 = cmul(z2, z2);
        chain<16>(p, 0, cb, z);
        cf xx = cmul(cb, E);
        chain<9>(p, 16, xx, z2);
        chain<5>(p, 25, cmul(xx, E), z4);
      }
    }
    STAMP(0, 2);
    bar();
    const int T = w & 3, r = lane & 15, kg = lane >> 4, i2 = min(r & 3, 2);
    int row = T == 0 ? r : T == 1 ? 16 + 2 * (r >> 2) + max((r & 3) - 1, 0) : T == 2 ? 25 + 5 * (r >> 3) + 2 * ((r >> 2) & 1) + i2 : 35 + 3 * (r >> 2) + i2;
    LDS char* pa = L + ch * GCH + row * GROW + kg * 16;
    LDS char* pb = L + ch * GCH + (47 + r) * GROW + kg * 16;
    v4f acc = {0.f, 0.f, 0.f, 0.f};
#pragma unroll
    for (int ks = 0; ks < 8; ks++)
      acc = __builtin_amdgcn_mfma_f32_16x16x32_bf16(*(LDS v8bf*)(pa + ks * 64), *(LDS v8bf*)(pb + ks * 64), acc, 0, 0, 0);
    char* dst = ws + WS_KT + d * 4096 + r * 32;
    if (T < 2) {
      *(v2u*)(dst + T * 512 + kg * 8) = (v2u){pk(acc[0], acc[1]), pk(acc[2], acc[3])};
    } else {
      u32 p00 = pk(acc[0], acc[0]), p01 = pk(acc[0], acc[1]), p11 = pk(acc[1], acc[1]), p22 = pk(acc[2], acc[2]);
      if (T == 2) *(v4u*)(dst + 1024 + (kg >> 1) * 512 + (kg & 1) * 16) = (v4u){p00, p01, p11, p22};
      else { *(v4u*)(dst + 2048 + kg * 512) = (v4u){p00, p00, p01, p11}; *(v4u*)(dst + 2048 + kg * 512 + 16) = (v4u){p11, p11, p22, p22}; }
    }
    STAMP(0, 3);
  }
}

#define U_CP 4160
#define U_CH 16704
#define O_U 0
#define O_K (2 * U_CH)
#define O_P (O_K + 2 * 4608)
#define O_F (O_P + 32768)
#define O_W (O_F + 2048)
#define K2_LDS (O_W + 49152)

DEVI void merge2(u32& a0, u32& a1, u32 b0, u32 b1) { u32 m = min(a0, b0); a0 = max(a0, b0); a1 = max(m, max(a1, b1)); }

__global__ void __launch_bounds__(512) k2(const u16* __restrict__ dskip, const u16* __restrict__ wg, const u16* __restrict__ wu,
    const u16* __restrict__ wd, char* __restrict__ ws, u16* __restrict__ out) {
  STAMPT
  __shared__ __attribute__((aligned(16))) char smem[K2_LDS];
  LDS char* L = (LDS char*)smem;
  const int tid = threadIdx.x, lane = tid & 63, w = __builtin_amdgcn_readfirstlane(tid >> 6);
  const int b = blockIdx.x, pair = (b & 7) * 32 + (b >> 3);
  const int slice = (w + 4) & 7;
  STAMP(1, 0);
  STAMPW(1, 2);
  v4u c0 = *(const v4u*)(ws + WS_CAND + lane * 32), c1 = *(const v4u*)(ws + WS_CAND + lane * 32 + 16);
  const int tb8 = slice * 32 + (lane >> 1), ch = lane & 1;
  const char* xt = ws + WS_XT + pair * 16384 + tb8 * 64 + ch * 16;
  v4u un = *(const v4u*)xt;
  v2u pv = *(const v2u*)(xt + (tb8 ? -56 : 0));
  v4u kv = *(const v4u*)(ws + WS_KT + pair * 8192 + tid * 16);
  v4u xr = *(const v4u*)(xt + 32);
  u32 dsk = *(const u32*)(dskip + pair * 2);
  const bool ffn = (w >= 4) && ((b >> 3) < 14);
  const int fs = (b & 7) * 14 + (b >> 3), fw = w & 3;
  if (ffn) {
    const int rho = lane >> 1, kk = 8 * ((rho >> 2) & 3) + 4 * (rho >> 4) + (rho & 3);
    const size_t go = (size_t)(fw * 128 + kk) * 3584 + fs * 32 + (lane & 1) * 16;
    LDS char* wl = L + O_W + fw * 12288;
#pragma unroll
    for (int ks = 0; ks < 4; ks++) {
      __builtin_amdgcn_global_load_lds((const GLB void*)((const char*)wg + go + ks * 32 * 3584), (LDS void*)(wl + ks * 1024), 16, 0, 0);
      __builtin_amdgcn_global_load_lds((const GLB void*)((const char*)wu + go + ks * 32 * 3584), (LDS void*)(wl + 4096 + ks * 1024), 16, 0, 0);
    }
    const size_t dofs = (size_t)(fs * 16 + (lane >> 2)) * 1024 + fw * 256 + (lane & 3) * 16;
#pragma unroll
    for (int j = 0; j < 4; j++)
      __builtin_amdgcn_global_load_lds((const GLB void*)((const char*)wd + dofs + j * 64), (LDS void*)(wl + 8192 + j * 1024), 16, 0, 0);
  }
  if (w == 0) *(LDS v4u*)(L + O_K + (lane >> 5) * 4608 + (lane & 31) * 16) = (v4u){0, 0, 0, 0};
  if (w == 1 && lane < 16) *(LDS v4u*)(L + O_U + (lane >> 3) * U_CH + ((lane >> 1) & 3) * U_CP + (lane & 1) * 16) = (v4u){0, 0, 0, 0};
  if (w == 2 && lane < 4) *(LDS v4u*)(L + O_F + lane * 16) = (v4u){0, 0, 0, 0};
  u32 k0 = c0[0], k1v = c0[1];
  merge2(k0, k1v, c0[2], c0[3]); merge2(k0, k1v, c1[0], c1[1]); merge2(k0, k1v, c1[2], c1[3]);
  merge2(k0, k1v, dppu<0xB1>(k0), dppu<0xB1>(k1v));
  merge2(k0, k1v, dppu<0x4E>(k0), dppu<0x4E>(k1v));
  merge2(k0, k1v, dppu<0x141>(k0), dppu<0x141>(k1v));
  merge2(k0, k1v, dppu<0x140>(k0), dppu<0x140>(k1v));
  u32 s0 = __builtin_amdgcn_readlane(k0, 0), s1 = __builtin_amdgcn_readlane(k1v, 0);
  merge2(s0, s1, __builtin_amdgcn_readlane(k0, 16), __builtin_amdgcn_readlane(k1v, 16));
  merge2(s0, s1, __builtin_amdgcn_readlane(k0, 32), __builtin_amdgcn_readlane(k1v, 32));
  merge2(s0, s1, __builtin_amdgcn_readlane(k0, 48), __builtin_amdgcn_readlane(k1v, 48));
  const int ts0 = 2047 - (s0 & 2047), ts1 = 2047 - (s1 & 2047);
  STAMP(1, 1);
  v4u xf[4];
  if (ffn) {
    const char* xp = ws + WS_XROW + ((lane & 1) ? ts1 : ts0) * 1024 + fw * 256 + (lane >> 4) * 16;
#pragma unroll
    for (int ks = 0; ks < 4; ks++) xf[ks] = *(const v4u*)(xp + ks * 64);
  }
  {
    if (tb8 == 0) pv = (v2u){0, 0};
    LDS char* p = L + O_U + ch * U_CH + 32 + tb8 * 16;
    u32 e0 = __builtin_amdgcn_alignbit(pv[1], pv[0], 16), e1 = __builtin_amdgcn_alignbit(un[0], pv[1], 16),
        e2 = __builtin_amdgcn_alignbit(un[1], un[0], 16), e3 = __builtin_amdgcn_alignbit(un[2], un[1], 16),
        e4 = __builtin_amdgcn_alignbit(un[3], un[2], 16);
    *(LDS v4u*)p = un;
    *(LDS v4u*)(p + U_CP) = (v4u){e1, e2, e3, e4};
    *(LDS v4u*)(p + 2 * U_CP) = (v4u){pv[1], un[0], un[1], un[2]};
    *(LDS v4u*)(p + 3 * U_CP) = (v4u){e0, e1, e2, e3};
    *(LDS v4u*)(L + O_K + (w >> 2) * 4608 + 512 + (tid & 255) * 16) = kv;
  }
  bar();
  STAMP(1, 3);
  float base[2][4];
  {
    u32 sn0 = ch ? un[0] : un[2], sn1 = ch ? un[1] : un[3], sx0 = ch ? xr[0] : xr[2], sx1 = ch ? xr[1] : xr[3];
    u32 kn0 = ch ? un[2] : un[0], kn1 = ch ? un[3] : un[1], kx0 = ch ? xr[2] : xr[0], kx1 = ch ? xr[3] : xr[1];
    u32 rn0 = dppu<0xB1>(sn0), rn1 = dppu<0xB1>(sn1), rx0 = dppu<0xB1>(sx0), rx1 = dppu<0xB1>(sx1);
    float dk = 1.f + (ch ? hi2f(dsk) : lo2f(dsk)), dr = 1.f + (ch ? lo2f(dsk) : hi2f(dsk));
    float bk[4] = {lo2f(kx0) + lo2f(kn0) * dk, hi2f(kx0) + hi2f(kn0) * dk, lo2f(kx1) + lo2f(kn1) * dk, hi2f(kx1) + hi2f(kn1) * dk};
    float br[4] = {lo2f(rx0) + lo2f(rn0) * dr, hi2f(rx0) + hi2f(rn0) * dr, lo2f(rx1) + lo2f(rn1) * dr, hi2f(rx1) + hi2f(rn1) * dr};
#pragma unroll
    for (int i = 0; i < 4; i++) { base[0][i] = ch ? br[i] : bk[i]; base[1][i] = ch ? bk[i] : br[i]; }
  }
  const int c16 = lane & 15, kg = lane >> 4;
  if (w < 4) {
    const int mc = w & 1, kh = w >> 1;
    LDS char* pa = L + O_U + mc * U_CH + ((lane >> 2) & 3) * U_CP + 32 - 16 - 8 + kg * 16 + (lane & 3) * 8 + kh * 256;
    LDS char* pb = L + O_K + mc * 4608 + 528 + c16 * 32 - kg * 16 - kh * 256 - 192;
    LDS char* pw = L + O_P + w * 8192 + c16 * 64 + 16 * (kg ^ ((c16 >> 1) & 3));
    v4f acc[4];
#pragma unroll
    for (int s = 0; s < 4; s++) acc[s] = (v4f){0.f, 0.f, 0.f, 0.f};
#pragma unroll
    for (int js = 0; js < 4; js++) {
      v8bf A[4], B[4];
#pragma unroll
      for (int a = 0; a < 4; a++) {
        A[a] = cat(trd(pa + a * 512 + js * 64 + 8), trd(pa + a * 512 + js * 64));
        B[a] = *(LDS v8bf*)(pb + a * 512 + (3 - js) * 64);
      }
#pragma unroll
      for (int s = 0; s < 4; s++)
#pragma unroll
        for (int a = 0; a <= s; a++) acc[s] = __builtin_amdgcn_mfma_f32_16x16x32_bf16(A[a], B[s - a], acc[s], 0, 0, 0);
    }
#pragma unroll
    for (int s = 0; s < 4; s++) { *(LDS v4f*)(pw + s * 1024) = acc[s]; acc[s] = (v4f){0.f, 0.f, 0.f, 0.f}; }
    lds_sig(L + O_F, lane);
    STAMP(1, 4);
#pragma unroll
    for (int js = 0; js < 4; js++) {
      v8bf A[8], B[8];
#pragma unroll
      for (int a = 0; a < 8; a++) {
        A[a] = cat(trd(pa + a * 512 + js * 64 + 8), trd(pa + a * 512 + js * 64));
        B[a] = *(LDS v8bf*)(pb + a * 512 + (3 - js) * 64);
      }
#pragma unroll
      for (int s = 0; s < 4; s++)
#pragma unroll
        for (int a = 0; a <= s + 4; a++) acc[s] = __builtin_amdgcn_mfma_f32_16x16x32_bf16(A[a], B[s + 4 - a], acc[s], 0, 0, 0);
    }
#pragma unroll
    for (int s = 0; s < 4; s++) *(LDS v4f*)(pw + 4096 + s * 1024) = acc[s];
    lds_sig(L + O_F + 4, lane);
    STAMP(1, 5);
    lds_wait(L + O_F + 4, 4);
  } else {
    if (ffn) {
      LDS char* wl = L + O_W + fw * 12288 + lane * 8;
      v4f g = {0.f, 0.f, 0.f, 0.f}, u = {0.f, 0.f, 0.f, 0.f};
#pragma unroll
      for (int ks = 0; ks < 4; ks++) {
        v8bf xb = __builtin_bit_cast(v8bf, xf[ks]);
        g = __builtin_amdgcn_mfma_f32_16x16x32_bf16(cat(trd(wl + ks * 1024), trd(wl + ks * 1024 + 512)), xb, g, 0, 0, 0);
        u = __builtin_amdgcn_mfma_f32_16x16x32_bf16(cat(trd(wl + 4096 + ks * 1024), trd(wl + 4096 + ks * 1024 + 512)), xb, u, 0, 0, 0);
      }
      v4s wdf[8];
      LDS char* dl = L + O_W + fw * 12288 + 8192 + (lane >> 2) * 64 + (lane & 3) * 8;
#pragma unroll
      for (int j = 0; j < 8; j++) wdf[j] = trd(dl + (j >> 1) * 1024 + (j & 1) * 32);
      STAMP(1, 4);
      LDS char* ex = L + O_F + 1024 + (kg * 2 + (lane & 1)) * 32;
      if (c16 < 2) { *(LDS v4f*)(ex + fw * 256) = g; *(LDS v4f*)(ex + fw * 256 + 16) = u; }
      lds_sig(L + O_F + 8, lane);
      lds_wait(L + O_F + 8, 4);
      g = *(LDS v4f*)ex; u = *(LDS v4f*)(ex + 16);
#pragma unroll
      for (int j = 1; j < 4; j++) { g += *(LDS v4f*)(ex + j * 256); u += *(LDS v4f*)(ex + j * 256 + 16); }
      float h[4];
#pragma unroll
      for (int i = 0; i < 4; i++) {
        float gg = rbf(g[i]);
        float sg = rbf(gg * __builtin_amdgcn_rcpf(1.f + __builtin_amdgcn_exp2f(gg * -1.44269504089f)));
        h[i] = sg * rbf(u[i]);
      }
      v4s hf = __builtin_bit_cast(v4s, (v2u){pk(h[0], h[1]), pk(h[2], h[3])});
      v4f o[8];
#pragma unroll
      for (int j = 0; j < 8; j++) o[j] = __builtin_amdgcn_mfma_f32_16x16x16bf16_1k(hf, wdf[j], (v4f){0.f, 0.f, 0.f, 0.f}, 0, 0, 0);
      __amdgpu_buffer_rsrc_t rs = __builtin_amdgcn_make_buffer_rsrc(ws + WS_PART, 0, NFFN * 4096, 0x00020000);
#pragma unroll
      for (int hh = 0; hh < 2; hh++) {
        v4f e = kg == 0 ? o[4 * hh] : kg == 1 ? o[4 * hh + 1] : kg == 2 ? o[4 * hh + 2] : o[4 * hh + 3];
        u32 e0 = __float_as_uint(e[0]), e1 = __float_as_uint(e[1]);
        e0 = e0 == SENT ? 0x7fc00000u : e0; e1 = e1 == SENT ? 0x7fc00000u : e1;
        __builtin_amdgcn_raw_buffer_store_b64((v2u){e0, e1}, rs, fs * 4096 + fw * 1024 + hh * 512 + lane * 8, 0, 16);
      }
      STAMP(1, 5);
    }
    lds_wait(L + O_F, 4);
  }
  float sv[2][2] = {{0.f, 0.f}, {0.f, 0.f}};
  {
    LDS char* pr = L + O_P + slice * 1024 + (lane >> 2) * 64 + 16 * ((lane & 3) ^ ((lane >> 3) & 3));
    v4f y0 = *(LDS v4f*)pr + *(LDS v4f*)(pr + 16384), y1 = *(LDS v4f*)(pr + 8192) + *(LDS v4f*)(pr + 24576);
    const int t0 = slice * 256 + lane * 4;
    char* op = (char*)out + t0 * 1024 + pair * 4;
#pragma unroll
    for (int i = 0; i < 4; i++) {
      float o0 = base[0][i] + y0[i], o1 = base[1][i] + y1[i];
      if (t0 + i == ts0) { sv[0][0] = o0; sv[0][1] = o1; }
      else if (t0 + i == ts1) { sv[1][0] = o0; sv[1][1] = o1; }
      else *(u32*)(op + i * 1024) = pk(o0, o1);
    }
  }
  STAMP(1, 6);
  if (w == 7) {
    __amdgpu_buffer_rsrc_t rs = __builtin_amdgcn_make_buffer_rsrc(ws + WS_PART, 0, NFFN * 4096, 0x00020000);
    const int j2 = lane < NFFN - 64 ? lane + 64 : lane;
    v4u a, c;
    for (int spin = 0; spin < 20000; spin++) {
      asm volatile("" ::: "memory");
      a = __builtin_amdgcn_raw_buffer_load_b128(rs, lane * 4096 + pair * 16, 0, 16);
      c = __builtin_amdgcn_raw_buffer_load_b128(rs, j2 * 4096 + pair * 16, 0, 16);
      bool bad = a[0] == SENT || a[1] == SENT || a[2] == SENT || a[3] == SENT || c[0] == SENT || c[1] == SENT || c[2] == SENT || c[3] == SENT;
      if (!__any(bad)) break;
      __builtin_amdgcn_s_sleep(2);
    }
    float up[4];
#pragma unroll
    for (int i = 0; i < 4; i++) {
      float v = __uint_as_float(a[i]) + (lane < NFFN - 64 ? __uint_as_float(c[i]) : 0.f);
      u32 kk = (i & 1) ? s1 : s0;
      float r = __uint_as_float(((kk & 0x80000000u) ? (kk & 0x7fffffffu) : ~kk) & 0xffff0000u);
      float sg = rbf(__builtin_amdgcn_rcpf(1.f + __builtin_amdgcn_exp2f(r * -1.44269504089f)));
      up[i] = rbf(rbf(wave_sum(v)) * sg);
    }
    if (lane == 0) *(LDS v4f*)(L + O_F + 16) = (v4f){up[0], up[1], up[2], up[3]};
    lds_sig(L + O_F + 12, lane);
  }
  if (slice == (ts0 >> 8) || slice == (ts1 >> 8)) {
    lds_wait(L + O_F + 12, 1);
    v4f up = *(LDS v4f*)(L + O_F + 16);
    const int tq = slice * 64 + lane;
    if (tq == (ts0 >> 2)) *(u32*)((char*)out + ts0 * 1024 + pair * 4) = pk(sv[0][0] + up[0], sv[0][1] + up[2]);
    if (tq == (ts1 >> 2)) *(u32*)((char*)out + ts1 * 1024 + pair * 4) = pk(sv[1][0] + up[1], sv[1][1] + up[3]);
  }
  STAMP(1, 7);
}

__global__ void __launch_bounds__(64) k0(char* ws) {
  const int lane = threadIdx.x, w = 0;
  STAMP(2, 0);
  STAMP(2, 1);
}

__global__ void __launch_bounds__(64) kprobe(char* ws) {
  u64 ts; asm volatile("s_memrealtime %0\n\ts_waitcnt lgkmcnt(0)" : "=s"(ts) :: "memory");
  const int lane = threadIdx.x;
  const u64* st = (const u64*)(ws + WS_STAMP);
  u64 t0 = ~0ull, mn = ~0ull, mx = 0;
  for (int j = lane; j < PB0 * PW0; j += 64) { u64 v = st[((PK0 * 512 + j / PW0) * 16 + j % PW0) * 8 + 0]; t0 = v < t0 ? v : t0; }
  for (int j = lane; j < PB * PW; j += 64) { u64 v = st[((PK * 512 + PBO + j / PW) * 16 + PWO + j % PW) * 8 + PI]; mn = v < mn ? v : mn; mx = v > mx ? v : mx; }
  for (int o = 32; o; o >>= 1) {
    u64 a = __shfl_xor(t0, o), b = __shfl_xor(mn, o), c = __shfl_xor(mx, o);
    t0 = a < t0 ? a : t0; mn = b < mn ? b : mn; mx = c > mx ? c : mx;
  }
  u64 x = (PMODE == 0 ? t0 : PMODE == 1 ? mn : PMODE == 2 ? mx : ts) - t0;
  if (x > 5000) x = 5000;
  u64 t1; asm volatile("s_memrealtime %0\n\ts_waitcnt lgkmcnt(0)" : "=s"(t1) :: "memory");
  u64 end = ts + 800 + 4 * x;
  for (int i = 0; i < 2000000; i++) {
    asm volatile("s_memrealtime %0\n\ts_waitcnt lgkmcnt(0)" : "=s"(t1) :: "memory");
    if (t1 >= end) break;
  }
}

extern "C" void kernel_launch(void* const* d_in, const int* in_sizes, int n_in, void* d_out, int out_size, void* d_ws,
                              size_t ws_size, hipStream_t stream) {
  k0<<<1, 64, 0, stream>>>((char*)d_ws);
  k1<<<512, 512, 0, stream>>>((const u16*)d_in[0], (const u16*)d_in[10], (const u16*)d_in[1], (const u16*)d_in[2],
                              (const u16*)d_in[3], (const u16*)d_in[4], (const u16*)d_in[5], (const u16*)d_in[6],
                              (const u16*)d_in[7], (char*)d_ws);
  k2<<<256, 512, 0, stream>>>((const u16*)d_in[8], (const u16*)d_in[11], (const u16*)d_in[12], (const u16*)d_in[13],
                              (char*)d_ws, (u16*)d_out);
  kprobe<<<1, 64, 0, stream>>>((char*)d_ws);
}
```

```cpp
#define DBG_STAMP
#define PK0 2
#define PB0 1
#define PW0 1
#define PK 0
#define PI 3
#define PMODE 0
#define PBO 0
#define PB 512
#define PWO 0
#define PW 8
#include <hip/hip_runtime.h>

typedef unsigned short u16;
typedef unsigned u32;
typedef unsigned long long u64;
typedef u32 v4u __attribute__((ext_vector_type(4)));
typedef u32 v2u __attribute__((ext_vector_type(2)));
typedef float v4f __attribute__((ext_vector_type(4)));
typedef float v2f __attribute__((ext_vector_type(2)));
typedef __bf16 v8bf __attribute__((ext_vector_type(8)));
typedef __bf16 v2bf __attribute__((ext_vector_type(2)));
typedef short v4s __attribute__((ext_vector_type(4)));
#define LDS __attribute__((address_space(3)))
#define GLB __attribute__((address_space(1)))
#define DEVI __device__ __forceinline__

#define WS_XT 0
#define WS_KT (4u << 20)
#define WS_XROW (6u << 20)
#define WS_CAND (8u << 20)
#define WS_PART ((8u << 20) + 65536)
#define NFFN 112
#define SENT 0xFFFFFFFFu

#define WS_STAMP (16u << 20)
#define STAMP(k, i) do { if (lane == 0) { u64 t_; asm volatile("s_memrealtime %0\n\ts_waitcnt lgkmcnt(0)" : "=s"(t_) :: "memory"); \
  ((u64*)(ws + WS_STAMP))[(((k) * 512 + blockIdx.x) * 16 + w) * 8 + (i)] = t_; } } while (0)
#define STAMPT u64 tt_; asm volatile("s_memrealtime %0" : "=s"(tt_) :: "memory");
#define STAMPW(k, i) do { if (lane == 0) { asm volatile("s_waitcnt lgkmcnt(0)" : "+s"(tt_) :: "memory"); \
  ((u64*)(ws + WS_STAMP))[(((k) * 512 + blockIdx.x) * 16 + w) * 8 + (i)] = tt_; } } while (0)

DEVI u32 pk(float a, float b) { return __builtin_bit_cast(u32, __builtin_convertvector((v2f){a, b}, v2bf)); }
DEVI float rbf(float a) { return __uint_as_float(pk(0.f, a)); }
DEVI float lo2f(u32 v) { return __uint_as_float(v << 16); }
DEVI float hi2f(u32 v) { return __uint_as_float(v & 0xffff0000u); }
template <int CTRL, int RM> DEVI float dppf(float v) {
  return __int_as_float(__builtin_amdgcn_update_dpp(0, __float_as_int(v), CTRL, RM, 0xf, false)); }
template <int CTRL> DEVI u32 dppu(u32 v) { return (u32)__builtin_amdgcn_update_dpp(0, (int)v, CTRL, 0xf, 0xf, false); }
DEVI float wave_sum(float v) {
  v += dppf<0xB1, 0xf>(v); v += dppf<0x4E, 0xf>(v); v += dppf<0x141, 0xf>(v); v += dppf<0x140, 0xf>(v);
  v += dppf<0x142, 0xa>(v); v += dppf<0x143, 0xc>(v);
  return __int_as_float(__builtin_amdgcn_readlane(__float_as_int(v), 63));
}
DEVI void bar() { asm volatile("s_waitcnt lgkmcnt(0)\n\ts_barrier" ::: "memory"); }
DEVI v4s trd(LDS char* p) { return __builtin_amdgcn_ds_read_tr16_b64_v4i16((LDS v4s*)p); }
DEVI v8bf cat(v4s a, v4s b) { return __builtin_bit_cast(v8bf, __builtin_shufflevector(a, b, 0, 1, 2, 3, 4, 5, 6, 7)); }
DEVI void lds_wait(LDS char* p, u32 n) {
  while (*(volatile LDS u32*)p < n) __builtin_amdgcn_s_sleep(1);
  asm volatile("" ::: "memory");
}
DEVI void lds_sig(LDS char* p, int lane) {
  asm volatile("s_waitcnt lgkmcnt(0)" ::: "memory");
  if (lane == 0) __hip_atomic_fetch_add((LDS u32*)p, 1u, __ATOMIC_RELAXED, __HIP_MEMORY_SCOPE_WORKGROUP);
}

struct cf { float r, i; };
DEVI cf cmul(cf a, cf b) { return {a.r * b.r - a.i * b.i, a.r * b.i + a.i * b.r}; }
DEVI cf cpow(float er, float ph, float s) {
  float e = __builtin_amdgcn_exp2f(er * s), f = __builtin_amdgcn_fractf(ph * s);
  return {e * __builtin_amdgcn_cosf(f), e * __builtin_amdgcn_sinf(f)};
}
#define GROW 528
#define GCH (63 * GROW)
template <int N> DEVI void chain(LDS char* p, int row, cf x, cf m) {
#pragma unroll
  for (int j = 0; j < N; j++) { *(LDS u32*)(p + (row + j) * GROW) = pk(x.r, x.i); if (j + 1 < N) x = cmul(x, m); }
}

#define K1_LDS (2 * GCH)
__global__ void __launch_bounds__(512) k1(const u16* __restrict__ x, const u16* __restrict__ wr, const u16* __restrict__ Are,
    const u16* __restrict__ Aim, const u16* __restrict__ Bre, const u16* __restrict__ Bim, const u16* __restrict__ Cre,
    const u16* __restrict__ Cim, const u16* __restrict__ logdt, char* __restrict__ ws) {
  STAMPT
  __shared__ __attribute__((aligned(16))) char smem[K1_LDS];
  LDS char* L = (LDS char*)smem;
  const int lane = threadIdx.x & 63, w = __builtin_amdgcn_readfirstlane(threadIdx.x >> 6);
  const int b = blockIdx.x & 255, sw = (b & 7) * 32 + (b >> 3);
  STAMP(0, 0);
  STAMPW(0, 4);
  if (blockIdx.x < 256) {
    const int t = sw * 8 + w;
    v4u xr = *(const v4u*)(x + t * 512 + lane * 8);
    v4u wv = *(const v4u*)(wr + lane * 8);
    if (w < 2 && threadIdx.x < NFFN) *(v4u*)(ws + WS_PART + (b * NFFN + threadIdx.x) * 16) = (v4u){SENT, SENT, SENT, SENT};
    float v[8];
#pragma unroll
    for (int j = 0; j < 4; j++) { v[2 * j] = lo2f(xr[j]); v[2 * j + 1] = hi2f(xr[j]); }
    float s = ((v[0] + v[1]) + (v[2] + v[3])) + ((v[4] + v[5]) + (v[6] + v[7]));
    STAMP(0, 1);
    float mean = wave_sum(s) * (1.f / 512.f);
    float q = 0.f;
#pragma unroll
    for (int j = 0; j < 8; j++) { float d = v[j] - mean; q += d * d; }
    float var = rbf(wave_sum(q) * (1.f / 512.f));
    float mb = rbf(mean);
    float rs = __builtin_amdgcn_rcpf(rbf(__builtin_amdgcn_sqrtf(rbf(var + 1e-5f))));
    v4u xn; float acc = 0.f;
#pragma unroll
    for (int j = 0; j < 4; j++) {
      u32 xj = pk(rbf(v[2 * j] - mb) * rs, rbf(v[2 * j + 1] - mb) * rs), wj = wv[j];
      xn[j] = xj;
      acc += lo2f(xj) * lo2f(wj) + hi2f(xj) * hi2f(wj);
    }
    *(LDS v4u*)(L + w * 2112 + lane * 32) = (v4u){xn[0], xr[0], xn[1], xr[1]};
    *(LDS v4u*)(L + w * 2112 + lane * 32 + 16) = (v4u){xn[2], xr[2], xn[3], xr[3]};
    u32 rb = __float_as_uint(rbf(wave_sum(acc)));
    u32 key = ((rb & 0x80000000u) ? (~rb & 0xffff0000u) : (rb | 0x80000000u)) | (u32)(2047 - t);
    if (lane == 0) *(LDS u32*)(L + 17408 + w * 4) = key;
    STAMP(0, 2);
    bar();
    {
      LDS char* p = L + ((lane >> 2) & 3) * 2112 + w * 256 + (lane >> 4) * 32 + (lane & 3) * 8;
      v4s a0 = trd(p), a1 = trd(p + 4 * 2112), b0 = trd(p + 128), b1 = trd(p + 4 * 2112 + 128);
      char* dst = ws + WS_XT + (size_t)(w * 32 + (lane >> 2)) * 16384 + sw * 64 + (lane & 3) * 16;
      *(v8bf*)dst = cat(a0, a1);
      *(v8bf*)(dst + 16 * 16384) = cat(b0, b1);
    }
    v4u k0 = *(LDS v4u*)(L + 17408), k1v = *(LDS v4u*)(L + 17424);
    int rank = 0;
#pragma unroll
    for (int j = 0; j < 4; j++) rank += (k0[j] > key) + (k1v[j] > key);
    if (rank < 2) {
      *(v4u*)(ws + WS_XROW + t * 1024 + lane * 16) = xn;
      if (lane == 0) *(u32*)(ws + WS_CAND + b * 8 + rank * 4) = key;
    }
    STAMP(0, 3);
  } else {
    const int ch = w >> 2, d = sw * 2 + ch;
    {
      const int n = ((w >> 1) & 1) * 64 + lane, idx = d * 128 + n;
      float are = lo2f(Are[idx]), aim = lo2f(Aim[idx]), bre = lo2f(Bre[idx]), bim = lo2f(Bim[idx]), cre = lo2f(Cre[idx]), cim = lo2f(Cim[idx]);
      float dt = rbf(expf(lo2f(logdt[d])));
      float er = dt * are * 1.44269504089f, ph = dt * aim * 0.15915494309f;
      STAMP(0, 1);
      cf cb = cmul({cre, cim}, {dt * bre, dt * bim});
      cf E = cpow(er, ph, 256.f);
      LDS char* p = L + ch * GCH + n * 4;
      if (w & 1) {
        cf z4 = cpow(er, ph, 4.f), z8 = cmul(z4, z4), z16 = cmul(z8, z8);
        cf xx = cmul(cb, cpow(er, ph, 768.f));
        chain<5>(p, 30, xx, z4);
#pragma unroll
        for (int a = 0; a < 4; a++) { xx = cmul(xx, E); chain<3>(p, 35 + 3 * a, xx, z8); }
        z16.i = -z16.i;
        *(LDS u32*)(p + 47 * GROW) = 0x3f80u;
        chain<15>(p, 48, z16, z16);
      } else {
        cf z = cpow(er, ph, 1.f), z2 = cmul(z, z), z4 = cmul(z2, z2);
        chain<16>(p, 0, cb, z);
        cf xx = cmul(cb, E);
        chain<9>(p, 16, xx, z2);
        chain<5>(p, 25, cmul(xx, E), z4);
      }
    }
    STAMP(0, 2);
    bar();
    const int T = w & 3, r = lane & 15, kg = lane >> 4, i2 = min(r & 3, 2);
    int row = T == 0 ? r : T == 1 ? 16 + 2 * (r >> 2) + max((r & 3) - 1, 0) : T == 2 ? 25 + 5 * (r >> 3) + 2 * ((r >> 2) & 1) + i2 : 35 + 3 * (r >> 2) + i2;
    LDS char* pa = L + ch * GCH + row * GROW + kg * 16;
    LDS char* pb = L + ch * GCH + (47 + r) * GROW + kg * 16;
    v4f acc = {0.f, 0.f, 0.f, 0.f};
#pragma unroll
    for (int ks = 0; ks < 8; ks++)
      acc = __builtin_amdgcn_mfma_f32_16x16x32_bf16(*(LDS v8bf*)(pa + ks * 64), *(LDS v8bf*)(pb + ks * 64), acc, 0, 0, 0);
    char* dst = ws + WS_KT + d * 4096 + r * 32;
    if (T < 2) {
      *(v2u*)(dst + T * 512 + kg * 8) = (v2u){pk(acc[0], acc[1]), pk(acc[2], acc[3])};
    } else {
      u32 p00 = pk(acc[0], acc[0]), p01 = pk(acc[0], acc[1]), p11 = pk(acc[1], acc[1]), p22 = pk(acc[2], acc[2]);
      if (T == 2) *(v4u*)(dst + 1024 + (kg >> 1) * 512 + (kg & 1) * 16) = (v4u){p00, p01, p11, p22};
      else { *(v4u*)(dst + 2048 + kg * 512) = (v4u){p00, p00, p01, p11}; *(v4u*)(dst + 2048 + kg * 512 + 16) = (v4u){p11, p11, p22, p22}; }
    }
    STAMP(0, 3);
  }
}

#define U_CP 4160
#define U_CH 16704
#define O_U 0
#define O_K (2 * U_CH)
#define O_P (O_K + 2 * 4608)
#define O_F (O_P + 32768)
#define O_W (O_F + 2048)
#define K2_LDS (O_W + 49152)

DEVI void merge2(u32& a0, u32& a1, u32 b0, u32 b1) { u32 m = min(a0, b0); a0 = max(a0, b0); a1 = max(m, max(a1, b1)); }

__global__ void __launch_bounds__(512) k2(const u16* __restrict__ dskip, const u16* __restrict__ wg, const u16* __restrict__ wu,
    const u16* __restrict__ wd, char* __restrict__ ws, u16* __restrict__ out) {
  STAMPT
  __shared__ __attribute__((aligned(16))) char smem[K2_LDS];
  LDS char* L = (LDS char*)smem;
  const int tid = threadIdx.x, lane = tid & 63, w = __builtin_amdgcn_readfirstlane(tid >> 6);
  const int b = blockIdx.x, pair = (b & 7) * 32 + (b >> 3);
  const int slice = (w + 4) & 7;
  STAMP(1, 0);
  STAMPW(1, 2);
  v4u c0 = *(const v4u*)(ws + WS_CAND + lane * 32), c1 = *(const v4u*)(ws + WS_CAND + lane * 32 + 16);
  const int tb8 = slice * 32 + (lane >> 1), ch = lane & 1;
  const char* xt = ws + WS_XT + pair * 16384 + tb8 * 64 + ch * 16;
  v4u un = *(const v4u*)xt;
  v2u pv = *(const v2u*)(xt + (tb8 ? -56 : 0));
  v4u kv = *(const v4u*)(ws + WS_KT + pair * 8192 + tid * 16);
  v4u xr = *(const v4u*)(xt + 32);
  u32 dsk = *(const u32*)(dskip + pair * 2);
  const bool ffn = (w >= 4) && ((b >> 3) < 14);
  const int fs = (b & 7) * 14 + (b >> 3), fw = w & 3;
  if (ffn) {
    const int rho = lane >> 1, kk = 8 * ((rho >> 2) & 3) + 4 * (rho >> 4) + (rho & 3);
    const size_t go = (size_t)(fw * 128 + kk) * 3584 + fs * 32 + (lane & 1) * 16;
    LDS char* wl = L + O_W + fw * 12288;
#pragma unroll
    for (int ks = 0; ks < 4; ks++) {
      __builtin_amdgcn_global_load_lds((const GLB void*)((const char*)wg + go + ks * 32 * 3584), (LDS void*)(wl + ks * 1024), 16, 0, 0);
      __builtin_amdgcn_global_load_lds((const GLB void*)((const char*)wu + go + ks * 32 * 3584), (LDS void*)(wl + 4096 + ks * 1024), 16, 0, 0);
    }
    const size_t dofs = (size_t)(fs * 16 + (lane >> 2)) * 1024 + fw * 256 + (lane & 3) * 16;
#pragma unroll
    for (int j = 0; j < 4; j++)
      __builtin_amdgcn_global_load_lds((const GLB void*)((const char*)wd + dofs + j * 64), (LDS void*)(wl + 8192 + j * 1024), 16, 0, 0);
  }
  if (w == 0) *(LDS v4u*)(L + O_K + (lane >> 5) * 4608 + (lane & 31) * 16) = (v4u){0, 0, 0, 0};
  if (w == 1 && lane < 16) *(LDS v4u*)(L + O_U + (lane >> 3) * U_CH + ((lane >> 1) & 3) * U_CP + (lane & 1) * 16) = (v4u){0, 0, 0, 0};
  if (w == 2 && lane < 4) *(LDS v4u*)(L + O_F + lane * 16) = (v4u){0, 0, 0, 0};
  u32 k0 = c0[0], k1v = c0[1];
  merge2(k0, k1v, c0[2], c0[3]); merge2(k0, k1v, c1[0], c1[1]); merge2(k0, k1v, c1[2], c1[3]);
  merge2(k0, k1v, dppu<0xB1>(k0), dppu<0xB1>(k1v));
  merge2(k0, k1v, dppu<0x4E>(k0), dppu<0x4E>(k1v));
  merge2(k0, k1v, dppu<0x141>(k0), dppu<0x141>(k1v));
  merge2(k0, k1v, dppu<0x140>(k0), dppu<0x140>(k1v));
  u32 s0 = __builtin_amdgcn_readlane(k0, 0), s1 = __builtin_amdgcn_readlane(k1v, 0);
  merge2(s0, s1, __builtin_amdgcn_readlane(k0, 16), __builtin_amdgcn_readlane(k1v, 16));
  merge2(s0, s1, __builtin_amdgcn_readlane(k0, 32), __builtin_amdgcn_readlane(k1v, 32));
  merge2(s0, s1, __builtin_amdgcn_readlane(k0, 48), __builtin_amdgcn_readlane(k1v, 48));
  const int ts0 = 2047 - (s0 & 2047), ts1 = 2047 - (s1 & 2047);
  STAMP(1, 1);
  v4u xf[4];
  if (ffn) {
    const char* xp = ws + WS_XROW + ((lane & 1) ? ts1 : ts0) * 1024 + fw * 256 + (lane >> 4) * 16;
#pragma unroll
    for (int ks = 0; ks < 4; ks++) xf[ks] = *(const v4u*)(xp + ks * 64);
  }
  {
    if (tb8 == 0) pv = (v2u){0, 0};
    LDS char* p = L + O_U + ch * U_CH + 32 + tb8 * 16;
    u32 e0 = __builtin_amdgcn_alignbit(pv[1], pv[0], 16), e1 = __builtin_amdgcn_alignbit(un[0], pv[1], 16),
        e2 = __builtin_amdgcn_alignbit(un[1], un[0], 16), e3 = __builtin_amdgcn_alignbit(un[2], un[1], 16),
        e4 = __builtin_amdgcn_alignbit(un[3], un[2], 16);
    *(LDS v4u*)p = un;
    *(LDS v4u*)(p + U_CP) = (v4u){e1, e2, e3, e4};
    *(LDS v4u*)(p + 2 * U_CP) = (v4u){pv[1], un[0], un[1], un[2]};
    *(LDS v4u*)(p + 3 * U_CP) = (v4u){e0, e1, e2, e3};
    *(LDS v4u*)(L + O_K + (w >> 2) * 4608 + 512 + (tid & 255) * 16) = kv;
  }
  bar();
  STAMP(1, 3);
  float base[2][4];
  {
    u32 sn0 = ch ? un[0] : un[2], sn1 = ch ? un[1] : un[3], sx0 = ch ? xr[0] : xr[2], sx1 = ch ? xr[1] : xr[3];
    u32 kn0 = ch ? un[2] : un[0], kn1 = ch ? un[3] : un[1], kx0 = ch ? xr[2] : xr[0], kx1 = ch ? xr[3] : xr[1];
    u32 rn0 = dppu<0xB1>(sn0), rn1 = dppu<0xB1>(sn1), rx0 = dppu<0xB1>(sx0), rx1 = dppu<0xB1>(sx1);
    float dk = 1.f + (ch ? hi2f(dsk) : lo2f(dsk)), dr = 1.f + (ch ? lo2f(dsk) : hi2f(dsk));
    float bk[4] = {lo2f(kx0) + lo2f(kn0) * dk, hi2f(kx0) + hi2f(kn0) * dk, lo2f(kx1) + lo2f(kn1) * dk, hi2f(kx1) + hi2f(kn1) * dk};
    float br[4] = {lo2f(rx0) + lo2f(rn0) * dr, hi2f(rx0) + hi2f(rn0) * dr, lo2f(rx1) + lo2f(rn1) * dr, hi2f(rx1) + hi2f(rn1) * dr};
#pragma unroll
    for (int i = 0; i < 4; i++) { base[0][i] = ch ? br[i] : bk[i]; base[1][i] = ch ? bk[i] : br[i]; }
  }
  const int c16 = lane & 15, kg = lane >> 4;
  if (w < 4) {
    const int mc = w & 1, kh = w >> 1;
    LDS char* pa = L + O_U + mc * U_CH + ((lane >> 2) & 3) * U_CP + 32 - 16 - 8 + kg * 16 + (lane & 3) * 8 + kh * 256;
    LDS char* pb = L + O_K + mc * 4608 + 528 + c16 * 32 - kg * 16 - kh * 256 - 192;
    LDS char* pw = L + O_P + w * 8192 + c16 * 64 + 16 * (kg ^ ((c16 >> 1) & 3));
    v4f acc[4];
#pragma unroll
    for (int s = 0; s < 4; s++) acc[s] = (v4f){0.f, 0.f, 0.f, 0.f};
#pragma unroll
    for (int js = 0; js < 4; js++) {
      v8bf A[4], B[4];
#pragma unroll
      for (int a = 0; a < 4; a++) {
        A[a] = cat(trd(pa + a * 512 + js * 64 + 8), trd(pa + a * 512 + js * 64));
        B[a] = *(LDS v8bf*)(pb + a * 512 + (3 - js) * 64);
      }
#pragma unroll
      for (int s = 0; s < 4; s++)
#pragma unroll
        for (int a = 0; a <= s; a++) acc[s] = __builtin_amdgcn_mfma_f32_16x16x32_bf16(A[a], B[s - a], acc[s], 0, 0, 0);
    }
#pragma unroll
    for (int s = 0; s < 4; s++) { *(LDS v4f*)(pw + s * 1024) = acc[s]; acc[s] = (v4f){0.f, 0.f, 0.f, 0.f}; }
    lds_sig(L + O_F, lane);
    STAMP(1, 4);
#pragma unroll
    for (int js = 0; js < 4; js++) {
      v8bf A[8], B[8];
#pragma unroll
      for (int a = 0; a < 8; a++) {
        A[a] = cat(trd(pa + a * 512 + js * 64 + 8), trd(pa + a * 512 + js * 64));
        B[a] = *(LDS v8bf*)(pb + a * 512 + (3 - js) * 64);
      }
#pragma unroll
      for (int s = 0; s < 4; s++)
#pragma unroll
        for (int a = 0; a <= s + 4; a++) acc[s] = __builtin_amdgcn_mfma_f32_16x16x32_bf16(A[a], B[s + 4 - a], acc[s], 0, 0, 0);
    }
#pragma unroll
    for (int s = 0; s < 4; s++) *(LDS v4f*)(pw + 4096 + s * 1024) = acc[s];
    lds_sig(L + O_F + 4, lane);
    STAMP(1, 5);
    lds_wait(L + O_F + 4, 4);
  } else {
    if (ffn) {
      LDS char* wl = L + O_W + fw * 12288 + lane * 8;
      v4f g = {0.f, 0.f, 0.f, 0.f}, u = {0.f, 0.f, 0.f, 0.f};
#pragma unroll
      for (int ks = 0; ks < 4; ks++) {
        v8bf xb = __builtin_bit_cast(v8bf, xf[ks]);
        g = __builtin_amdgcn_mfma_f32_16x16x32_bf16(cat(trd(wl + ks * 1024), trd(wl + ks * 1024 + 512)), xb, g, 0, 0, 0);
        u = __builtin_amdgcn_mfma_f32_16x16x32_bf16(cat(trd(wl + 4096 + ks * 1024), trd(wl + 4096 + ks * 1024 + 512)), xb, u, 0, 0, 0);
      }
      v4s wdf[8];
      LDS char* dl = L + O_W + fw * 12288 + 8192 + (lane >> 2) * 64 + (lane & 3) * 8;
#pragma unroll
      for (int j = 0; j < 8; j++) wdf[j] = trd(dl + (j >> 1) * 1024 + (j & 1) * 32);
      STAMP(1, 4);
      LDS char* ex = L + O_F + 1024 + (kg * 2 + (lane & 1)) * 32;
      if (c16 < 2) { *(LDS v4f*)(ex + fw * 256) = g; *(LDS v4f*)(ex + fw * 256 + 16) = u; }
      lds_sig(L + O_F + 8, lane);
      lds_wait(L + O_F + 8, 4);
      g = *(LDS v4f*)ex; u = *(LDS v4f*)(ex + 16);
#pragma unroll
      for (int j = 1; j < 4; j++) { g += *(LDS v4f*)(ex + j * 256); u += *(LDS v4f*)(ex + j * 256 + 16); }
      float h[4];
#pragma unroll
      for (int i = 0; i < 4; i++) {
        float gg = rbf(g[i]);
        float sg = rbf(gg * __builtin_amdgcn_rcpf(1.f + __builtin_amdgcn_exp2f(gg * -1.44269504089f)));
        h[i] = sg * rbf(u[i]);
      }
      v4s hf = __builtin_bit_cast(v4s, (v2u){pk(h[0], h[1]), pk(h[2], h[3])});
      v4f o[8];
#pragma unroll
      for (int j = 0; j < 8; j++) o[j] = __builtin_amdgcn_mfma_f32_16x16x16bf16_1k(hf, wdf[j], (v4f){0.f, 0.f, 0.f, 0.f}, 0, 0, 0);
      __amdgpu_buffer_rsrc_t rs = __builtin_amdgcn_make_buffer_rsrc(ws + WS_PART, 0, NFFN * 4096, 0x00020000);
#pragma unroll
      for (int hh = 0; hh < 2; hh++) {
        v4f e = kg == 0 ? o[4 * hh] : kg == 1 ? o[4 * hh + 1] : kg == 2 ? o[4 * hh + 2] : o[4 * hh + 3];
        u32 e0 = __float_as_uint(e[0]), e1 = __float_as_uint(e[1]);
        e0 = e0 == SENT ? 0x7fc00000u : e0; e1 = e1 == SENT ? 0x7fc00000u : e1;
        __builtin_amdgcn_raw_buffer_store_b64((v2u){e0, e1}, rs, fs * 4096 + fw * 1024 + hh * 512 + lane * 8, 0, 16);
      }
      STAMP(1, 5);
    }
    lds_wait(L + O_F, 4);
  }
  float sv[2][2] = {{0.f, 0.f}, {0.f, 0.f}};
  {
    LDS char* pr = L + O_P + slice * 1024 + (lane >> 2) * 64 + 16 * ((lane & 3) ^ ((lane >> 3) & 3));
    v4f y0 = *(LDS v4f*)pr + *(LDS v4f*)(pr + 16384), y1 = *(LDS v4f*)(pr + 8192) + *(LDS v4f*)(pr + 24576);
    const int t0 = slice * 256 + lane * 4;
    char* op = (char*)out + t0 * 1024 + pair * 4;
#pragma unroll
    for (int i = 0; i < 4; i++) {
      float o0 = base[0][i] + y0[i], o1 = base[1][i] + y1[i];
      if (t0 + i == ts0) { sv[0][0] = o0; sv[0][1] = o1; }
      else if (t0 + i == ts1) { sv[1][0] = o0; sv[1][1] = o1; }
      else *(u32*)(op + i * 1024) = pk(o0, o1);
    }
  }
  STAMP(1, 6);
  if (w == 7) {
    __amdgpu_buffer_rsrc_t rs = __builtin_amdgcn_make_buffer_rsrc(ws + WS_PART, 0, NFFN * 4096, 0x00020000);
    const int j2 = lane < NFFN - 64 ? lane + 64 : lane;
    v4u a, c;
    for (int spin = 0; spin < 20000; spin++) {
      asm volatile("" ::: "memory");
      a = __builtin_amdgcn_raw_buffer_load_b128(rs, lane * 4096 + pair * 16, 0, 16);
      c = __builtin_amdgcn_raw_buffer_load_b128(rs, j2 * 4096 + pair * 16, 0, 16);
      bool bad = a[0] == SENT || a[1] == SENT || a[2] == SENT || a[3] == SENT || c[0] == SENT || c[1] == SENT || c[2] == SENT || c[3] == SENT;
      if (!__any(bad)) break;
      __builtin_amdgcn_s_sleep(2);
    }
    float up[4];
#pragma unroll
    for (int i = 0; i < 4; i++) {
      float v = __uint_as_float(a[i]) + (lane < NFFN - 64 ? __uint_as_float(c[i]) : 0.f);
      u32 kk = (i & 1) ? s1 : s0;
      float r = __uint_as_float(((kk & 0x80000000u) ? (kk & 0x7fffffffu) : ~kk) & 0xffff0000u);
      float sg = rbf(__builtin_amdgcn_rcpf(1.f + __builtin_amdgcn_exp2f(r * -1.44269504089f)));
      up[i] = rbf(rbf(wave_sum(v)) * sg);
    }
    if (lane == 0) *(LDS v4f*)(L + O_F + 16) = (v4f){up[0], up[1], up[2], up[3]};
    lds_sig(L + O_F + 12, lane);
  }
  if (slice == (ts0 >> 8) || slice == (ts1 >> 8)) {
    lds_wait(L + O_F + 12, 1);
    v4f up = *(LDS v4f*)(L + O_F + 16);
    const int tq = slice * 64 + lane;
    if (tq == (ts0 >> 2)) *(u32*)((char*)out + ts0 * 1024 + pair * 4) = pk(sv[0][0] + up[0], sv[0][1] + up[2]);
    if (tq == (ts1 >> 2)) *(u32*)((char*)out + ts1 * 1024 + pair * 4) = pk(sv[1][0] + up[1], sv[1][1] + up[3]);
  }
  STAMP(1, 7);
}

__global__ void __launch_bounds__(64) k0(char* ws, int i) {
  const int lane = threadIdx.x, w = 0;
  STAMP(2, i);
}

__global__ void __launch_bounds__(64) kprobe(char* ws) {
  u64 ts; asm volatile("s_memrealtime %0\n\ts_waitcnt lgkmcnt(0)" : "=s"(ts) :: "memory");
  const int lane = threadIdx.x;
  const u64* st = (const u64*)(ws + WS_STAMP);
  u64 t0 = ~0ull, mn = ~0ull, mx = 0;
  for (int j = lane; j < PB0 * PW0; j += 64) { u64 v = st[((PK0 * 512 + j / PW0) * 16 + j % PW0) * 8 + 0]; t0 = v < t0 ? v : t0; }
  for (int j = lane; j < PB * PW; j += 64) { u64 v = st[((PK * 512 + PBO + j / PW) * 16 + PWO + j % PW) * 8 + PI]; mn = v < mn ? v : mn; mx = v > mx ? v : mx; }
  for (int o = 32; o; o >>= 1) {
    u64 a = __shfl_xor(t0, o), b = __shfl_xor(mn, o), c = __shfl_xor(mx, o);
    t0 = a < t0 ? a : t0; mn = b < mn ? b : mn; mx = c > mx ? c : mx;
  }
  u64 x = (PMODE == 0 ? t0 : PMODE == 1 ? mn : PMODE == 2 ? mx : ts) - t0;
  if (x > 5000) x = 5000;
  u64 t1; asm volatile("s_memrealtime %0\n\ts_waitcnt lgkmcnt(0)" : "=s"(t1) :: "memory");
  u64 end = ts + 800 + 4 * x;
  for (int i = 0; i < 2000000; i++) {
    asm volatile("s_memrealtime %0\n\ts_waitcnt lgkmcnt(0)" : "=s"(t1) :: "memory");
    if (t1 >= end) break;
  }
}

extern "C" void kernel_launch(void* const* d_in, const int* in_sizes, int n_in, void* d_out, int out_size, void* d_ws,
                              size_t ws_size, hipStream_t stream) {
  k0<<<1, 64, 0, stream>>>((char*)d_ws, 0);
  k0<<<1, 64, 0, stream>>>((char*)d_ws, 1);
  k0<<<1, 64, 0, stream>>>((char*)d_ws, 2);
  k0<<<1, 64, 0, stream>>>((char*)d_ws, 3);
  k1<<<512, 512, 0, stream>>>((const u16*)d_in[0], (const u16*)d_in[10], (const u16*)d_in[1], (const u16*)d_in[2],
                              (const u16*)d_in[3], (const u16*)d_in[4], (const u16*)d_in[5], (const u16*)d_in[6],
                              (const u16*)d_in[7], (char*)d_ws);
  k2<<<256, 512, 0, stream>>>((const u16*)d_in[8], (const u16*)d_in[11], (const u16*)d_in[12], (const u16*)d_in[13],
                              (char*)d_ws, (u16*)d_out);
  kprobe<<<1, 64, 0, stream>>>((char*)d_ws);
}
```

```cpp
#include <hip/hip_runtime.h>

typedef unsigned short u16;
typedef unsigned u32;
typedef unsigned long long u64;
typedef u32 v4u __attribute__((ext_vector_type(4)));
typedef u32 v2u __attribute__((ext_vector_type(2)));
typedef float v4f __attribute__((ext_vector_type(4)));
typedef float v2f __attribute__((ext_vector_type(2)));
typedef __bf16 v8bf __attribute__((ext_vector_type(8)));
typedef __bf16 v2bf __attribute__((ext_vector_type(2)));
typedef short v4s __attribute__((ext_vector_type(4)));
#define LDS __attribute__((address_space(3)))
#define GLB __attribute__((address_space(1)))
#define CST __attribute__((address_space(4)))
#define DEVI __device__ __forceinline__

#define WS_R 4096
#define WS_S 8192
#define WS_P 32768
#define WS_X (1u << 20)
#define WS_END (3u << 20)
#define NFFN 112
#define SPIN 4000
#define D_A 26
#define D_P 20
#define D_Q 50

#define STAMP(k, i)

DEVI u32 pk(float a, float b) { return __builtin_bit_cast(u32, __builtin_convertvector((v2f){a, b}, v2bf)); }
DEVI float rbf(float a) { return __uint_as_float(pk(0.f, a)); }
DEVI float lo2f(u32 v) { return __uint_as_float(v << 16); }
DEVI float hi2f(u32 v) { return __uint_as_float(v & 0xffff0000u); }
template <int CTRL, int RM> DEVI float dppf(float v) {
  return __int_as_float(__builtin_amdgcn_update_dpp(0, __float_as_int(v), CTRL, RM, 0xf, false)); }
template <int CTRL, int RM> DEVI u32 dppu(u32 v) { return (u32)__builtin_amdgcn_update_dpp(0, (int)v, CTRL, RM, 0xf, false); }
DEVI float wave_sum(float v) {
  v += dppf<0xB1, 0xf>(v); v += dppf<0x4E, 0xf>(v); v += dppf<0x141, 0xf>(v); v += dppf<0x140, 0xf>(v);
  v += dppf<0x142, 0xa>(v); v += dppf<0x143, 0xc>(v);
  return __int_as_float(__builtin_amdgcn_readlane(__float_as_int(v), 63));
}
DEVI u32 wave_max(u32 v) {
  v = max(v, dppu<0xB1, 0xf>(v)); v = max(v, dppu<0x4E, 0xf>(v)); v = max(v, dppu<0x141, 0xf>(v)); v = max(v, dppu<0x140, 0xf>(v));
  v = max(v, dppu<0x142, 0xa>(v)); v = max(v, dppu<0x143, 0xc>(v));
  return (u32)__builtin_amdgcn_readlane((int)v, 63);
}
DEVI v4s trd(LDS char* p) { return __builtin_amdgcn_ds_read_tr16_b64_v4i16((LDS v4s*)p); }
DEVI v8bf cat(v4s a, v4s b) { return __builtin_bit_cast(v8bf, __builtin_shufflevector(a, b, 0, 1, 2, 3, 4, 5, 6, 7)); }
template <int SL> DEVI void lds_wait(LDS char* p, u32 m) {
  u32 v, s, c;
  if (SL)
    asm volatile("s_mov_b32 %2, 0x20000\n"
                 "1:\n\tds_read_b32 %0, %3\n\ts_waitcnt lgkmcnt(0)\n\tv_readfirstlane_b32 %1, %0\n\ts_andn2_b32 %1, %4, %1\n\t"
                 "s_cbranch_scc0 2f\n\ts_sleep 1\n\ts_sub_u32 %2, %2, 1\n\ts_cbranch_scc0 1b\n2:"
                 : "=&v"(v), "=&s"(s), "=&s"(c) : "v"(p), "s"(m) : "memory", "scc");
  else
    asm volatile("s_mov_b32 %2, 0x20000\n"
                 "1:\n\tds_read_b32 %0, %3\n\ts_waitcnt lgkmcnt(0)\n\tv_readfirstlane_b32 %1, %0\n\ts_andn2_b32 %1, %4, %1\n\t"
                 "s_cbranch_scc0 2f\n\ts_sub_u32 %2, %2, 1\n\ts_cbranch_scc0 1b\n2:"
                 : "=&v"(v), "=&s"(s), "=&s"(c) : "v"(p), "s"(m) : "memory", "scc");
}
DEVI void lds_sig(LDS char* p, u32 bit, int) {
  asm volatile("s_mov_b64 exec, 1\n\tds_or_b32 %0, %1\n\ts_mov_b64 exec, -1" :: "v"(p), "v"(bit) : "memory");
}
DEVI void merge2(u32& a0, u32& a1, u32 b0, u32 b1) { u32 m = min(a0, b0); a0 = max(a0, b0); a1 = max(m, max(a1, b1)); }

DEVI v2f cmul(v2f x, v2f m) { return __builtin_elementwise_fma(x.yy, (v2f){-m.y, m.x}, x.xx * m); }
DEVI v2f cpow(float er, float ph, float s) {
  float e = __builtin_amdgcn_exp2f(er * s), f = __builtin_amdgcn_fractf(ph * s);
  return (v2f){e * __builtin_amdgcn_cosf(f), e * __builtin_amdgcn_sinf(f)};
}

#define GROW 528
#define CHP 33408
#define O_KC 12288
#define KCP 4160
#define O_PP 28928
#define O_U (4 * CHP)
#define UP 4640
#define O_LN (O_U + 4 * UP)
#define O_F (O_LN + 8704)
#define F_LN 0
#define F_U 4
#define F_K1 8
#define F_K2 12
#define F_PH 16
#define F_EX 20
#define F_SP 24
#define F_TS 40
#define F_ST 64
#define F_TOP 96
#define F_SV 128
#define LDS_SZ (O_F + 256)

template <int N> DEVI void chain(LDS char* p, int row, v2f x, v2f m) {
#pragma unroll
  for (int j = 0; j < N; j++) { *(LDS u32*)(p + (row + j) * GROW) = pk(x.x, x.y); if (j + 1 < N) x = cmul(x, m); }
}

template <int S> DEVI void conv_pair(LDS char* pa, LDS char* pb, LDS char* pw) {
  v4f a0 = {0.f, 0.f, 0.f, 0.f}, a1 = {0.f, 0.f, 0.f, 0.f};
  pa += 448;
#pragma unroll 1
  for (int js = 0; js < 8; js++) {
    v8bf Ap = cat(trd(pa + (S + 1) * 512 + 8), trd(pa + (S + 1) * 512));
#pragma unroll
    for (int a = 0; a <= S + 1; a++) {
      v8bf B = *(LDS v8bf*)(pb + a * 512);
      a1 = __builtin_amdgcn_mfma_f32_16x16x32_bf16(Ap, B, a1, 0, 0, 0);
      if (a <= S) {
        Ap = cat(trd(pa + (S - a) * 512 + 8), trd(pa + (S - a) * 512));
        a0 = __builtin_amdgcn_mfma_f32_16x16x32_bf16(Ap, B, a0, 0, 0, 0);
      }
    }
    pa -= 64; pb += 64;
  }
  *(LDS v4f*)pw = a0;
  *(LDS v4f*)(pw + 1024) = a1;
}

__global__ void __launch_bounds__(1024, 4) kf(const u16* __restrict__ x, const u16* __restrict__ wr, char* __restrict__ ws,
    const u16* __restrict__ Are, const u16* __restrict__ Aim, const u16* __restrict__ Bre, const u16* __restrict__ Bim,
    const u16* __restrict__ Cre, const u16* __restrict__ Cim, const u16* __restrict__ logdt, const u16* __restrict__ dskip,
    const u16* __restrict__ wg, const u16* __restrict__ wu, const u16* __restrict__ wd, u16* __restrict__ out) {
  __shared__ __attribute__((aligned(16))) char smem[LDS_SZ];
  LDS char* L = (LDS char*)smem;
  LDS char* F = L + O_F;
  const int lane = threadIdx.x & 63, w = __builtin_amdgcn_readfirstlane(threadIdx.x >> 6);
  const int b = blockIdx.x, sw = (b & 7) * 32 + (b >> 3), cg = sw >> 1, h = sw & 1;
  const __amdgpu_buffer_rsrc_t rs = __builtin_amdgcn_make_buffer_rsrc(ws, 0, WS_END, 0x00020000);
  const int c16 = lane & 15, kg = lane >> 4;
  if (w < 8) {
    __builtin_amdgcn_s_setprio(3);
    const int t = sw * 8 + w;
    v4u xr = *(const v4u*)(x + t * 512 + lane * 8);
    v4u wv = *(const v4u*)(wr + lane * 8);
    const u32 tag = *(const CST u32*)(u64)ws + 0x01010101u;
    v2u dsk = *(const CST v2u*)(u64)(dskip + cg * 4);
    STAMP(0, 0);
    asm volatile("s_waitcnt lgkmcnt(0)\n\ts_barrier" ::: "memory");
    u32 pt[4];
    {
      u32 by = (tag ^ 0x3C5A96C3u) >> (8 * (lane & 3));
#pragma unroll
      for (int j = 0; j < 4; j++) pt[j] = ((by >> (2 * j)) & 1) << 14 | ((by >> (2 * j + 1)) & 1) << 30;
    }
    v2f v[4], wf[4];
#pragma unroll
    for (int j = 0; j < 4; j++) { u32 a = xr[j], c = wv[j]; v[j] = (v2f){lo2f(a), hi2f(a)}; wf[j] = (v2f){lo2f(c), hi2f(c)}; }
    v2f s2 = (v[0] + v[1]) + (v[2] + v[3]);
    STAMP(0, 1);
    const float mean = wave_sum(s2.x + s2.y) * (1.f / 512.f), mb = rbf(mean);
    v2f q2 = {0.f, 0.f}, r2 = {0.f, 0.f};
#pragma unroll
    for (int j = 0; j < 4; j++) { v[j] -= mb; q2 = __builtin_elementwise_fma(v[j], v[j], q2); r2 = __builtin_elementwise_fma(v[j], wf[j], r2); }
    const float q = wave_sum(q2.x + q2.y), r = wave_sum(r2.x + r2.y), dm = mean - mb;
    const float sd = rbf(__builtin_amdgcn_sqrtf(rbf(rbf(fmaxf(q * (1.f / 512.f) - dm * dm, 0.f)) + 1e-5f)));
    const float rsd = __builtin_amdgcn_rcpf(sd), r16 = rsd * 0.0625f;
    const u32 rb = pk(0.f, r * rsd);
    const u32 key = (rb ^ (((int)rb >> 31) & 0x7fff0000u) ^ 0x80000000u) | (u32)(2047 - t);
    if (lane == 0) {
      *(LDS u32*)(F + F_TOP + w * 4) = key;
      *(LDS u32*)(F + F_ST + w * 4) = (__float_as_uint(mb) >> 16) | __float_as_uint(sd);
    }
    v4u xn;
#pragma unroll
    for (int j = 0; j < 4; j++) { v2f y = v[j] * r16; xn[j] = pk(y.x, y.y); }
    *(LDS v4u*)(L + O_LN + w * 1088 + lane * 16) = xn;
    lds_sig(F + F_LN, 1u << w, lane);
    lds_wait<0>(F + F_LN, 255);
    STAMP(0, 2);
    {
      LDS char* p = L + O_LN + ((lane >> 2) & 3) * 1088 + w * 128 + kg * 32 + (lane & 3) * 8;
      if (w == 0) {
        v4u ka = *(LDS v4u*)(F + F_TOP), kb = *(LDS v4u*)(F + F_TOP + 16);
        u32 a0 = max(ka[0], ka[1]), a1 = min(ka[0], ka[1]), b0 = max(kb[0], kb[1]), b1 = min(kb[0], kb[1]);
        merge2(a0, a1, max(ka[2], ka[3]), min(ka[2], ka[3])); merge2(b0, b1, max(kb[2], kb[3]), min(kb[2], kb[3]));
        merge2(a0, a1, b0, b1);
        if (lane == 0) __builtin_amdgcn_raw_buffer_store_b128((v4u){a0, a1, tag, tag}, rs, WS_R + sw * 16, 0, 16);
      }
      v2u a0 = __builtin_bit_cast(v2u, trd(p)), a1 = __builtin_bit_cast(v2u, trd(p + 4 * 1088));
      v4u d = {a0[0], a0[1], a1[0], a1[1]};
#pragma unroll
      for (int j = 0; j < 4; j++) d[j] = (d[j] & ~0x40004000u) | pt[j];
      __builtin_amdgcn_raw_buffer_store_b128(d, rs, WS_X + (w * 8 + (lane >> 3)) * 32768 + sw * 128 + (lane & 7) * 16, 0, 16);
      if (w == 1 && lane < 4) {
        v2u st = *(LDS v2u*)(F + F_ST + lane * 8);
        __builtin_amdgcn_raw_buffer_store_b128((v4u){st[0], tag, st[1], tag}, rs, WS_S + sw * 64 + lane * 16, 0, 16);
      }
    }
    STAMP(0, 3);
    __builtin_amdgcn_s_sleep(D_A);
    const int si = w >> 1, s = h ? 2 + si : (si < 2 ? si : si + 4), t0 = s * 256 + (w & 1) * 128 + lane * 2;
    v4u sv;
    if (w < 6 || h == 0) {
      const u32 o = WS_X + (cg >> 1) * 32768 + (w * 32 + (lane >> 2)) * 128 + (cg & 1) * 64 + (lane & 3) * 16;
      v4u a, c;
      _Pragma("unroll 1") for (int i = 0; i < SPIN; i++) {
        a = __builtin_amdgcn_raw_buffer_load_b128(rs, o, 0, 16);
        c = __builtin_amdgcn_raw_buffer_load_b128(rs, o + 2048, 0, 16);
        sv = __builtin_amdgcn_raw_buffer_load_b128(rs, WS_S + t0 * 8, 0, 16);
#pragma unroll
        for (int j = 0; j < 4; j++) { a[j] ^= pt[j]; c[j] ^= pt[j]; }
        u32 e = (a[0] | a[1] | a[2]) | (a[3] | c[0] | c[1]) | (c[2] | c[3]);
        if (!__any(e & 0x40004000u)) break;
        asm volatile("" ::: "memory");
      }
      LDS char* p = L + O_U + (lane & 3) * UP + 512 + (w * 32 + (lane >> 2)) * 16;
      *(LDS v4u*)p = a;
      *(LDS v4u*)(p + 256) = c;
      lds_sig(F + F_U, 1u << w, lane);
    } else {
      sv = __builtin_amdgcn_raw_buffer_load_b128(rs, WS_S + t0 * 8, 0, 16);
    }
    __builtin_amdgcn_s_setprio(1);
    STAMP(0, 4);
    _Pragma("unroll 1") for (int i = 0; i < SPIN; i++) {
      if (!__any(sv[1] != tag || sv[3] != tag)) break;
      asm volatile("" ::: "memory");
      sv = __builtin_amdgcn_raw_buffer_load_b128(rs, WS_S + t0 * 8, 0, 16);
    }
    lds_wait<1>(F + F_U, 1u << s);
    v2f base[4];
    {
      const v2f m2 = {lo2f(sv[0]), lo2f(sv[2])}, sd2 = (v2f){hi2f(sv[0]), hi2f(sv[2])} * 16.f;
#pragma unroll
      for (int c = 0; c < 4; c++) {
        u32 uu = *(LDS u32*)(L + O_U + c * UP + 512 + t0 * 2), dd = dsk[c >> 1];
        float dk = ((c & 1) ? hi2f(dd) : lo2f(dd)) * 16.f + 16.f;
        base[c] = __builtin_elementwise_fma((v2f){lo2f(uu), hi2f(uu)}, sd2 + dk, m2);
      }
    }
    lds_wait<1>(F + F_SP, 256);
    const v2u ts = *(LDS v2u*)(F + F_TS);
    const int ts0 = 2047 - (ts[0] & 2047), ts1 = 2047 - (ts[1] & 2047);
    const int n = (w & 1) * 8 + (lane >> 3);
    LDS char* pr = L + O_PP + si * 1024 + n * 64 + 16 * (((lane >> 1) & 3) ^ ((n >> 1) & 3)) + (lane & 1) * 8;
    char* op = (char*)out + t0 * 1024 + cg * 8;
    STAMP(0, 5);
    lds_wait<1>(F + F_PH, si < 2 ? 15 : 240);
    STAMP(0, 6);
#pragma unroll
    for (int c = 0; c < 4; c++) base[c] += *(LDS v2f*)(pr + c * CHP);
#pragma unroll
    for (int i = 0; i < 2; i++) {
      if (t0 + i != ts0 && t0 + i != ts1) *(v2u*)(op + i * 1024) = (v2u){pk(base[0][i], base[1][i]), pk(base[2][i], base[3][i])};
      else {
        *(LDS v4f*)(F + F_SV + (t0 + i == ts0 ? 0 : 16)) = (v4f){base[0][i], base[1][i], base[2][i], base[3][i]};
        asm volatile("ds_or_b32 %0, %1" :: "v"(F + F_SP), "v"(t0 + i == ts0 ? 1u : 2u) : "memory");
      }
    }
    STAMP(0, 7);
  } else {
    const int ch = w & 3, half = (w >> 2) & 1, d = cg * 4 + ch;
    LDS char* C = L + ch * CHP;
    u32 pf = 0;
    const int idx = d * 128 + half * 64 + lane;
    float are = lo2f(Are[idx]), aim = lo2f(Aim[idx]), bre = lo2f(Bre[idx]), bim = lo2f(Bim[idx]), cre = lo2f(Cre[idx]), cim = lo2f(Cim[idx]);
    const u32 ldt = *(const CST u32*)(u64)(logdt + (d & ~1));
    const u32 tag = *(const CST u32*)(u64)ws + 0x01010101u;
    if (w == 15) { u64 pc; asm volatile("s_getpc_b64 %0" : "=s"(pc)); pf = *(const u32*)((pc & ~127ull) + lane * 128); }
    if (w == 8) { if (lane < 16) *(LDS v4u*)(F + lane * 16) = (v4u){0, 0, 0, 0}; }
    else if (w < 11) *(LDS v4u*)(L + O_U + ((w - 9) * 2 + (lane >> 5)) * UP + (lane & 31) * 16) = (v4u){0, 0, 0, 0};
    STAMP(0, 0);
    asm volatile("s_waitcnt lgkmcnt(0)\n\ts_barrier" ::: "memory");
    if (w < 11) {
      const int g = b & 7, i = b >> 3;
      const char* pp = w == 10 ? (const char*)wd + (224 * g + 7 * i + min(lane >> 3, 6)) * 1024 + (lane & 7) * 128
                               : (const char*)(w == 8 ? wg : wu) + (16 * i + (lane >> 2)) * 3584 + ((448 * g) & ~127) + (lane & 3) * 128;
      pf = *(const u32*)pp;
    }
    {
      const float dt = rbf(__builtin_amdgcn_exp2f(((d & 1) ? hi2f(ldt) : lo2f(ldt)) * 1.44269504089f));
      const float er = dt * are * 1.44269504089f, ph = dt * aim * 0.15915494309f, d16 = dt * 16.f;
      STAMP(0, 1);
      const v2f cb = cmul((v2f){cre, cim}, (v2f){d16 * bre, d16 * bim});
      const v2f E = cpow(er, ph, 256.f), z = cpow(er, ph, 1.f), z2 = cmul(z, z);
      const v2f z4 = cpow(er, ph, 4.f), z8 = cmul(z4, z4);
      v2f z16 = cmul(z8, z8);
      LDS char* p = C + 144 + half * 256 + lane * 4;
      z16.y = -z16.y;
      *(LDS u32*)(p + 47 * GROW) = 0x3f80u;
      chain<15>(p, 48, z16, z16);
      chain<16>(p, 0, cb, z);
      v2f xx = cmul(cb, E);
      chain<9>(p, 16, xx, z2);
      xx = cmul(xx, E); chain<5>(p, 25, xx, z4);
      xx = cmul(xx, E); chain<5>(p, 30, xx, z4);
#pragma unroll
      for (int a = 0; a < 4; a++) { xx = cmul(xx, E); chain<3>(p, 35 + 3 * a, xx, z8); }
    }
    lds_sig(F + F_K1, 1u << (w - 8), lane);
    STAMP(0, 2);
    lds_wait<0>(F + F_K1, 17u << ch);
    {
      const int i2 = min(c16 & 3, 2);
      const int r0 = half ? 25 + 5 * (c16 >> 3) + 2 * ((c16 >> 2) & 1) + i2 : c16;
      const int r1 = half ? 35 + 3 * (c16 >> 2) + i2 : 16 + 2 * (c16 >> 2) + max((c16 & 3) - 1, 0);
      LDS char* p0 = C + 144 + r0 * GROW + kg * 16;
      LDS char* p1 = C + 144 + r1 * GROW + kg * 16;
      LDS char* pb = C + 144 + (47 + c16) * GROW + kg * 16;
      v4f e0 = {0.f, 0.f, 0.f, 0.f}, e1 = {0.f, 0.f, 0.f, 0.f};
#pragma unroll
      for (int ks = 0; ks < 8; ks++) {
        v8bf B = *(LDS v8bf*)(pb + ks * 64);
        e0 = __builtin_amdgcn_mfma_f32_16x16x32_bf16(*(LDS v8bf*)(p0 + ks * 64), B, e0, 0, 0, 0);
        e1 = __builtin_amdgcn_mfma_f32_16x16x32_bf16(*(LDS v8bf*)(p1 + ks * 64), B, e1, 0, 0, 0);
      }
      LDS char* dst = C + O_KC + 32 + c16 * 32;
      if (!half) {
        *(LDS v2u*)(dst + kg * 8) = (v2u){pk(e0[0], e0[1]), pk(e0[2], e0[3])};
        *(LDS v2u*)(dst + 512 + kg * 8) = (v2u){pk(e1[0], e1[1]), pk(e1[2], e1[3])};
        if (lane < 2) *(LDS v4u*)(C + O_KC + lane * 16) = (v4u){0, 0, 0, 0};
      } else {
        *(LDS v4u*)(dst + 1024 + (kg >> 1) * 512 + (kg & 1) * 16) = (v4u){pk(e0[0], e0[0]), pk(e0[0], e0[1]), pk(e0[1], e0[1]), pk(e0[2], e0[2])};
        u32 p00 = pk(e1[0], e1[0]), p01 = pk(e1[0], e1[1]), p11 = pk(e1[1], e1[1]), p22 = pk(e1[2], e1[2]);
        *(LDS v4u*)(dst + 2048 + kg * 512) = (v4u){p00, p00, p01, p11};
        *(LDS v4u*)(dst + 2048 + kg * 512 + 16) = (v4u){p11, p11, p22, p22};
      }
    }
    lds_sig(F + F_K2, 1u << (w - 8), lane);
    STAMP(0, 3);
    if (!half) {
      lds_wait<0>(F + F_K2, 17u << ch);
      if (lane < 6) *(LDS v4u*)(C + O_KC + (1 + (lane >> 1)) * KCP + (lane & 1) * 16) = (v4u){0, 0, 0, 0};
#pragma unroll
      for (int q = 0; q < 4; q++) {
        LDS char* p = C + O_KC + 32 + q * 1024 + lane * 16;
        v4u un = *(LDS v4u*)p; v2u pv = *(LDS v2u*)(p - 8);
        u32 e0 = __builtin_amdgcn_alignbit(pv[1], pv[0], 16), e1 = __builtin_amdgcn_alignbit(un[0], pv[1], 16),
            e2 = __builtin_amdgcn_alignbit(un[1], un[0], 16), e3 = __builtin_amdgcn_alignbit(un[2], un[1], 16),
            e4 = __builtin_amdgcn_alignbit(un[3], un[2], 16);
        *(LDS v4u*)(p + KCP) = (v4u){e1, e2, e3, e4};
        *(LDS v4u*)(p + 2 * KCP) = (v4u){pv[1], un[0], un[1], un[2]};
        *(LDS v4u*)(p + 3 * KCP) = (v4u){e0, e1, e2, e3};
      }
      LDS char* pa = C + O_KC + ((lane >> 2) & 3) * KCP + 56 - kg * 16 + (lane & 3) * 8;
      LDS char* pb = L + O_U + ch * UP + 32 + c16 * 32 + kg * 16;
      LDS char* pw = C + O_PP + c16 * 64 + 16 * (kg ^ ((c16 >> 1) & 3));
      __builtin_amdgcn_s_setprio(2);
      STAMP(0, 4);
      lds_wait<0>(F + F_U, h ? 15 : 3);
      STAMP(0, 5);
      if (h) conv_pair<2>(pa, pb, pw); else conv_pair<0>(pa, pb, pw);
      lds_sig(F + F_PH, 1u << ch, lane);
      STAMP(0, 6);
      lds_wait<0>(F + F_U, h ? 63 : 255);
      if (h) conv_pair<4>(pa, pb, pw + 2048); else conv_pair<6>(pa, pb, pw + 2048);
      lds_sig(F + F_PH, 16u << ch, lane);
      STAMP(0, 7);
    } else {
      __builtin_amdgcn_s_setprio(3);
      const bool ffn = (b >> 3) < 14;
      const int fs = (b & 7) * 14 + (b >> 3);
      v4u k[4];
#pragma unroll
      for (int j = 0; j < 4; j++) k[j] = __builtin_amdgcn_raw_buffer_load_b128(rs, WS_R + j * 1024 + lane * 16, 0, 16);
      if (ffn) {
        lds_wait<0>(F + F_K2, 17u << ch);
        const int rho = lane >> 1, kk = 8 * ((rho >> 2) & 3) + 4 * (rho >> 4) + (rho & 3);
        const size_t go = (size_t)(ch * 128 + kk) * 3584 + fs * 32 + (lane & 1) * 16;
#pragma unroll
        for (int ks = 0; ks < 4; ks++) {
          __builtin_amdgcn_global_load_lds((const GLB void*)((const char*)wg + go + ks * 32 * 3584), (LDS void*)(C + ks * 1024), 16, 0, 0);
          __builtin_amdgcn_global_load_lds((const GLB void*)((const char*)wu + go + ks * 32 * 3584), (LDS void*)(C + 4096 + ks * 1024), 16, 0, 0);
        }
        const size_t dofs = (size_t)(fs * 16 + (lane >> 2)) * 1024 + ch * 256 + (lane & 3) * 16;
#pragma unroll
        for (int j = 0; j < 4; j++)
          __builtin_amdgcn_global_load_lds((const GLB void*)((const char*)wd + dofs + j * 64), (LDS void*)(C + 8192 + j * 1024), 16, 0, 0);
      }
      _Pragma("unroll 1") for (int i = 0; i < SPIN; i++) {
        if (!__any(k[0][2] != tag || k[1][2] != tag || k[2][2] != tag || k[3][2] != tag)) break;
        asm volatile("" ::: "memory");
#pragma unroll
        for (int j = 0; j < 4; j++) k[j] = __builtin_amdgcn_raw_buffer_load_b128(rs, WS_R + j * 1024 + lane * 16, 0, 16);
      }
      u32 k0 = k[0][0], k1 = k[0][1], k2 = k[2][0], k3 = k[2][1];
      merge2(k0, k1, k[1][0], k[1][1]); merge2(k2, k3, k[3][0], k[3][1]); merge2(k0, k1, k2, k3);
      const u32 s0 = wave_max(k0), s1 = wave_max(k0 == s0 ? k1 : k0);
      const int ts0 = 2047 - (s0 & 2047), ts1 = 2047 - (s1 & 2047);
      STAMP(0, 4);
      if (ffn) {
        const int tk = (lane & 1) ? ts1 : ts0;
        const char* xp = (const char*)x + tk * 1024 + ch * 256 + kg * 16;
        v4u xf[4];
#pragma unroll
        for (int ks = 0; ks < 4; ks++) xf[ks] = *(const v4u*)(xp + ks * 64);
        v2u st = __builtin_amdgcn_raw_buffer_load_b64(rs, WS_S + tk * 8, 0, 16);
        if (ch == 0) { if (lane == 0) *(LDS v2u*)(F + F_TS) = (v2u){s0, s1}; lds_sig(F + F_SP, 256, lane); }
        asm volatile("s_waitcnt vmcnt(5)" ::: "memory");
        LDS char* wl = C + lane * 8;
        v8bf fg[4], fu[4]; v4s wdf[8];
#pragma unroll
        for (int ks = 0; ks < 4; ks++) {
          fg[ks] = cat(trd(wl + ks * 1024), trd(wl + ks * 1024 + 512));
          fu[ks] = cat(trd(wl + 4096 + ks * 1024), trd(wl + 4096 + ks * 1024 + 512));
        }
        LDS char* dl = C + 8192 + (lane >> 2) * 64 + (lane & 3) * 8;
#pragma unroll
        for (int j = 0; j < 8; j++) wdf[j] = trd(dl + (j >> 1) * 1024 + (j & 1) * 32);
        const v8bf ones = __builtin_bit_cast(v8bf, (v4u){0x3f803f80u, 0x3f803f80u, 0x3f803f80u, 0x3f803f80u});
        v4f sg = {0.f, 0.f, 0.f, 0.f}, su = sg, g = sg, u = sg;
#pragma unroll
        for (int ks = 0; ks < 4; ks++) {
          sg = __builtin_amdgcn_mfma_f32_16x16x32_bf16(fg[ks], ones, sg, 0, 0, 0);
          su = __builtin_amdgcn_mfma_f32_16x16x32_bf16(fu[ks], ones, su, 0, 0, 0);
        }
#pragma unroll
        for (int ks = 0; ks < 4; ks++) {
          v8bf xb = __builtin_bit_cast(v8bf, xf[ks]);
          g = __builtin_amdgcn_mfma_f32_16x16x32_bf16(fg[ks], xb, g, 0, 0, 0);
          u = __builtin_amdgcn_mfma_f32_16x16x32_bf16(fu[ks], xb, u, 0, 0, 0);
        }
        _Pragma("unroll 1") for (int i = 0; i < SPIN; i++) {
          if (!__any(st[1] != tag)) break;
          asm volatile("" ::: "memory");
          st = __builtin_amdgcn_raw_buffer_load_b64(rs, WS_S + tk * 8, 0, 16);
        }
        STAMP(0, 5);
        const float mm = lo2f(st[0]), rsd = __builtin_amdgcn_rcpf(hi2f(st[0]));
        g -= sg * mm; u -= su * mm;
        LDS char* ex = L + O_LN + (kg * 2 + (lane & 1)) * 32;
        if (c16 < 2) { *(LDS v4f*)(ex + ch * 256) = g; *(LDS v4f*)(ex + ch * 256 + 16) = u; }
        lds_sig(F + F_EX, 1u << ch, lane);
        lds_wait<0>(F + F_EX, 15);
        g = *(LDS v4f*)ex; u = *(LDS v4f*)(ex + 16);
#pragma unroll
        for (int j = 1; j < 4; j++) { g += *(LDS v4f*)(ex + j * 256); u += *(LDS v4f*)(ex + j * 256 + 16); }
        float hh[4];
#pragma unroll
        for (int i = 0; i < 4; i++) {
          float gg = g[i] * rsd;
          hh[i] = gg * __builtin_amdgcn_rcpf(1.f + __builtin_amdgcn_exp2f(gg * -1.44269504089f)) * (u[i] * rsd);
        }
        v4s hf = __builtin_bit_cast(v4s, (v2u){pk(hh[0], hh[1]), pk(hh[2], hh[3])});
#pragma unroll
        for (int j = 0; j < 2; j++) {
          const v4f zz = {0.f, 0.f, 0.f, 0.f};
          v4f e0 = __builtin_amdgcn_mfma_f32_16x16x16bf16_1k(hf, wdf[4 * j], zz, 0, 0, 0);
          v4f e1 = __builtin_amdgcn_mfma_f32_16x16x16bf16_1k(hf, wdf[4 * j + 1], zz, 0, 0, 0);
          v4f e2 = __builtin_amdgcn_mfma_f32_16x16x16bf16_1k(hf, wdf[4 * j + 2], zz, 0, 0, 0);
          v4f e3 = __builtin_amdgcn_mfma_f32_16x16x16bf16_1k(hf, wdf[4 * j + 3], zz, 0, 0, 0);
          u32 o0 = pk(e0[0], e0[1]), o1 = pk(e1[0], e1[1]), o2 = pk(e2[0], e2[1]), o3 = pk(e3[0], e3[1]);
          asm volatile("" : "+v"(o0), "+v"(o1), "+v"(o2), "+v"(o3));
          u32 lo = (kg & 1) ? o1 : o0, hi = (kg & 1) ? o3 : o2;
          asm volatile("" : "+v"(lo), "+v"(hi));
          __builtin_amdgcn_raw_buffer_store_b64((v2u){(kg & 2) ? hi : lo, tag}, rs, WS_P + fs * 4096 + ch * 1024 + j * 512 + lane * 8, 0, 16);
        }
        STAMP(0, 6);
        __builtin_amdgcn_s_sleep(D_P);
      } else {
        if (ch == 0) { if (lane == 0) *(LDS v2u*)(F + F_TS) = (v2u){s0, s1}; lds_sig(F + F_SP, 256, lane); }
        if (b == 255 && w == 12 && lane == 0) *(u32*)ws = tag;
        __builtin_amdgcn_s_sleep(D_Q);
      }
      const u32 own = h ? 0x3Cu : 0xC3u;
      const u32 need = ((own >> (ts0 >> 8)) & 1) | ((own >> (ts1 >> 8)) & 1) << 1;
      if (need) {
        const u32 o0 = WS_P + lane * 4096 + d * 8, o1 = o0 + (lane < NFFN - 64 ? 64 * 4096 : 0);
        v2u a, c;
        _Pragma("unroll 1") for (int i = 0; i < SPIN; i++) {
          a = __builtin_amdgcn_raw_buffer_load_b64(rs, o0, 0, 16);
          c = __builtin_amdgcn_raw_buffer_load_b64(rs, o1, 0, 16);
          if (!__any(a[1] != tag || c[1] != tag)) break;
          asm volatile("" ::: "memory");
        }
        if (lane >= NFFN - 64) c[0] = 0;
        const float y0 = wave_sum(lo2f(a[0]) + lo2f(c[0])), y1 = wave_sum(hi2f(a[0]) + hi2f(c[0]));
        const u32 kk = (lane & 1) ? s1 : s0;
        const float r = __uint_as_float((kk ^ 0x80000000u ^ ((~(int)kk >> 31) & 0x7fff0000u)) & 0xffff0000u);
        const float sgm = rbf(__builtin_amdgcn_rcpf(1.f + __builtin_amdgcn_exp2f(r * -1.44269504089f)));
        lds_wait<0>(F + F_SP, need);
        if (lane < 2 && ((need >> lane) & 1)) {
          float o = *(LDS float*)(F + F_SV + lane * 16 + ch * 4) + rbf(lane ? y1 : y0) * sgm;
          out[(lane ? ts1 : ts0) * 512 + d] = (u16)(pk(o, 0.f));
        }
      }
      STAMP(0, 7);
    }
    asm volatile("" :: "v"(pf));
  }
}

extern "C" void kernel_launch(void* const* d_in, const int* in_sizes, int n_in, void* d_out, int out_size, void* d_ws,
                              size_t ws_size, hipStream_t stream) {
  kf<<<256, 1024, 0, stream>>>((const u16*)d_in[0], (const u16*)d_in[10], (char*)d_ws, (const u16*)d_in[1], (const u16*)d_in[2],
                               (const u16*)d_in[3], (const u16*)d_in[4], (const u16*)d_in[5], (const u16*)d_in[6],
                               (const u16*)d_in[7], (const u16*)d_in[8], (const u16*)d_in[11], (const u16*)d_in[12],
                               (const u16*)d_in[13], (u16*)d_out);
}
```

```cpp
#define DBG_STAMP
#define PI 0
#define PMODE 0
#define PX 255
#define PWO 0
#define PW 1
#include <hip/hip_runtime.h>

typedef unsigned short u16;
typedef unsigned u32;
typedef unsigned long long u64;
typedef u32 v4u __attribute__((ext_vector_type(4)));
typedef u32 v2u __attribute__((ext_vector_type(2)));
typedef float v4f __attribute__((ext_vector_type(4)));
typedef float v2f __attribute__((ext_vector_type(2)));
typedef __bf16 v8bf __attribute__((ext_vector_type(8)));
typedef __bf16 v2bf __attribute__((ext_vector_type(2)));
typedef short v4s __attribute__((ext_vector_type(4)));
#define LDS __attribute__((address_space(3)))
#define GLB __attribute__((address_space(1)))
#define CST __attribute__((address_space(4)))
#define DEVI __device__ __forceinline__
#define SB() __builtin_amdgcn_sched_barrier(0)
#define MEMB() asm volatile("" ::: "memory")

#define WS_R 4096
#define WS_S 8192
#define WS_P 32768
#define WS_X (1u << 20)
#define WS_END (3u << 20)
#define SPIN 4000
#define PF_LATE 0
#define CODE_SZ 13824
#define D_A 26
#define D_W 2
#define D_K 4
#define D_Q 30

#define WS_STAMP (16u << 20)
#define GROUP(b, w) ((w) >= 12 ? 2 : (w) >= 8 ? 1 : ((w) >= 6 && ((b) & 8) && ((b) >> 4) < 14) ? 3 : 0)
#define ST_(i) "s_memrealtime s[84+2*" #i ":85+2*" #i "]\n\t"
#define STAMP(i) asm volatile(ST_(i) ::: "memory", "s99")
#define STAMPD(i, v) asm volatile(ST_(i) :: "v"(v) : "memory", "s99")
#define FL_(i) "v_mov_b32 v8, s[84+4*" #i "]\n\tv_mov_b32 v9, s[85+4*" #i "]\n\tv_mov_b32 v10, s[86+4*" #i "]\n\tv_mov_b32 v11, s[87+4*" #i "]\n\t" \
               "global_store_dwordx4 v[6:7], v[8:11], off offset:16*" #i "\n\ts_nop 1\n\t"
#define FLUSH_ "s_mov_b64 exec, 1\n\ts_waitcnt lgkmcnt(0)\n\t" FL_(0) FL_(1) FL_(2) FL_(3)
#define STADDR "{v[6:7]}"((u64*)(ws + WS_STAMP) + (blockIdx.x * 16 + w) * 8)
#define FLUSH() asm volatile(FLUSH_ "s_endpgm" :: STADDR : "memory", "v8", "v9", "v10", "v11")

DEVI u32 pk(float a, float b) { return __builtin_bit_cast(u32, __builtin_convertvector((v2f){a, b}, v2bf)); }
DEVI float rbf(float a) { return __uint_as_float(pk(0.f, a)); }
DEVI float lo2f(u32 v) { return __uint_as_float(v << 16); }
DEVI float hi2f(u32 v) { return __uint_as_float(v & 0xffff0000u); }
DEVI float dot2(u32 a, u32 b, float c) { return __builtin_amdgcn_fdot2_f32_bf16(__builtin_bit_cast(v2bf, a), __builtin_bit_cast(v2bf, b), c, false); }
template <int CTRL> DEVI float dppf(float v) { return __int_as_float(__builtin_amdgcn_update_dpp(0, __float_as_int(v), CTRL, 0xf, 0xf, true)); }
template <int CTRL, int RM> DEVI u32 dppu(u32 v) { return (u32)__builtin_amdgcn_update_dpp(0, (int)v, CTRL, RM, 0xf, false); }
DEVI float rdl(float v, int l) { return __int_as_float(__builtin_amdgcn_readlane(__float_as_int(v), l)); }
DEVI float fold4(float a, float b, float c, float d) {
  v2u p = __builtin_amdgcn_permlane32_swap(__float_as_uint(a), __float_as_uint(b), false, false);
  v2u q = __builtin_amdgcn_permlane32_swap(__float_as_uint(c), __float_as_uint(d), false, false);
  float t = __uint_as_float(p[0]) + __uint_as_float(p[1]), s = __uint_as_float(q[0]) + __uint_as_float(q[1]);
  v2u r = __builtin_amdgcn_permlane16_swap(__float_as_uint(t), __float_as_uint(s), false, false);
  return __uint_as_float(r[0]) + __uint_as_float(r[1]);
}
DEVI u32 wave_max(u32 v) {
  v = max(v, dppu<0xB1, 0xf>(v)); v = max(v, dppu<0x4E, 0xf>(v)); v = max(v, dppu<0x141, 0xf>(v)); v = max(v, dppu<0x140, 0xf>(v));
  v = max(v, dppu<0x142, 0xa>(v)); v = max(v, dppu<0x143, 0xc>(v));
  return (u32)__builtin_amdgcn_readlane((int)v, 63);
}
DEVI v4s trd(LDS char* p) { return __builtin_amdgcn_ds_read_tr16_b64_v4i16((LDS v4s*)p); }
DEVI v8bf cat(v4s a, v4s b) { return __builtin_bit_cast(v8bf, __builtin_shufflevector(a, b, 0, 1, 2, 3, 4, 5, 6, 7)); }
template <int SL> DEVI void lds_wait(LDS char* p, u32 m) {
  u32 v, s, c;
  asm volatile("s_mov_b32 %2, 0x20000\n"
               "1:\n\tds_read_b32 %0, %3\n\ts_waitcnt lgkmcnt(0)\n\tv_readfirstlane_b32 %1, %0\n\ts_andn2_b32 %1, %4, %1\n\t"
               "s_cbranch_scc0 2f\n\ts_sleep %5\n\ts_sub_u32 %2, %2, 1\n\ts_cbranch_scc0 1b\n2:"
               : "=&v"(v), "=&s"(s), "=&s"(c) : "v"(p), "s"(m), "n"(SL) : "memory", "scc");
}
DEVI void lds_sig(LDS char* p, u32 bit) {
  asm volatile("s_mov_b64 exec, 1\n\tds_or_b32 %0, %1\n\ts_mov_b64 exec, -1" :: "v"(p), "v"(bit) : "memory");
}
DEVI void merge2(u32& a0, u32& a1, u32 b0, u32 b1) { u32 m = min(a0, b0); a0 = max(a0, b0); a1 = max(m, max(a1, b1)); }
DEVI v2f cpow(float er, float ph) {
  float e = __builtin_amdgcn_exp2f(er), f = __builtin_amdgcn_fractf(ph);
  return (v2f){__builtin_amdgcn_cosf(f), __builtin_amdgcn_sinf(f)} * e;
}

#define RP 272
#define BC 17408
#define O_A 69632
#define AC 11776
#define AH 5888
#define A_HI 3584
#define O_U (O_A + 4 * AC)
#define UP 4640
#define O_F (O_U + 4 * UP)
#define O_LN (O_F + 768)
#define O_W (O_LN + 8704)
#define F_KS 0
#define F_U 64
#define F_PH 68
#define F_EX 72
#define F_SP 76
#define F_TS 80
#define F_TAG 88
#define F_X 92
#define F_SV 128
#define F_E 256
#define LDS_SZ (O_W + 16384 + 256)
#define AOFF 49632

#define CM1(t, x, m) "v_pk_mul_f32 " t ", " x ", " m " op_sel_hi:[0,1]\n\t"
#define CM2(r, t, x, m) "v_pk_fma_f32 " r ", " x ", " m ", " t " op_sel:[1,1,0] op_sel_hi:[1,0,1] neg_lo:[0,1,0]\n\t"
DEVI v2f cmul(v2f x, v2f m) {
  v2f t, r;
  asm(CM1("%1", "%2", "%3") CM2("%0", "%1", "%2", "%3") : "=v"(r), "=&v"(t) : "v"(x), "v"(m));
  return r;
}
template <int O1, int O2> DEVI void step2(v2f& r1, v2f x1, v2f m1, v2f& r2, v2f x2, v2f m2) {
  v2f t1, t2;
  asm volatile(CM1("%2", "%4", "%5") CM1("%3", "%6", "%7") CM2("%0", "%2", "%4", "%5") CM2("%1", "%3", "%6", "%7")
               : "=&v"(r1), "=v"(r2), "=&v"(t1), "=&v"(t2) : "v"(x1), "v"(m1), "v"(x2), "v"(m2));
  u32 p1 = pk(r1.x, r1.y), p2 = pk(r2.x, r2.y);
  asm volatile("ds_write_addtid_b32 %0 offset:%2\n\tds_write_addtid_b32 %1 offset:%3" :: "v"(p1), "v"(p2), "n"(O1), "n"(O2) : "memory");
}
template <int O1> DEVI void put1(u32 p) { asm volatile("ds_write_addtid_b32 %0 offset:%1" :: "v"(p), "n"(O1) : "memory"); }
template <int J> DEVI void brows(v2f& e, v2f& o, v2f m) {
  if constexpr (J < 16) { step2<J * RP, (15 + J) * RP>(e, e, m, o, o, m); brows<J + 1>(e, o, m); }
}
#define MF(a, b, c) __builtin_amdgcn_mfma_f32_16x16x32_bf16(a, b, c, 0, 0, 0)
#define BF8(x) __builtin_bit_cast(v8bf, x)
template <int LO, int HI> DEVI v4u band(int e, u32 one) {
  const u32 m = (u32)(((((u64)1 << (HI - LO + 1)) - 1) << (e - HI + 24)) >> 24);
  v4u r;
#pragma unroll
  for (int j = 0; j < 4; j++) { u32 y = m >> (2 * j); r[j] = ((y & 1) | ((y & 2) << 15)) * one; }
  return r;
}
DEVI v8bf wop(v8bf a, v8bf b, v4u ba, v4u bb) {
  const v4f zz = {0.f, 0.f, 0.f, 0.f};
  v4f p = MF(a, BF8(ba), zz), q = MF(b, BF8(bb), zz);
  return BF8(((v4u){pk(p[0], q[0]), pk(p[1], q[1]), pk(p[2], q[2]), pk(p[3], q[3])}));
}
#define TB(a, b, s) "ds_read2_b32 %" #a ", %[p] offset0:16*" #s "+3 offset1:16*" #s "+2\n\tds_read2_b32 %" #b ", %[p] offset0:16*" #s "+1 offset1:16*" #s "\n\t"
DEVI v8bf cat2(v2u a, v2u b) { return BF8(__builtin_shufflevector(a, b, 0, 1, 2, 3)); }

#define VR(b, j) "v[" #b "+4*" #j ":" #b "+3+4*" #j "]"
#define LA(b, j, o) "ds_read_b64_tr_b16 v[" #b "+4*" #j ":" #b "+1+4*" #j "], v0 offset:" #o "+456-64*" #j "\n\t" \
                    "ds_read_b64_tr_b16 v[" #b "+2+4*" #j ":" #b "+3+4*" #j "], v0 offset:" #o "+448-64*" #j "\n\t"
#define LA8(b, o) LA(b, 0, o) LA(b, 1, o) LA(b, 2, o) LA(b, 3, o) LA(b, 4, o) LA(b, 5, o) LA(b, 6, o) LA(b, 7, o)
#define LB(j) "ds_read_b128 " VR(76, j) ", v1 offset:1024+64*" #j "\n\t"
#define LB8 LB(0) LB(1) LB(2) LB(3) LB(4) LB(5) LB(6) LB(7)
#define AX "v[112:115]"
#define AY "v[116:119]"
#define MA(d, a, j, c) "v_mfma_f32_16x16x32_bf16 " d ", " VR(a, j) ", " VR(76, j) ", " c "\n\t"
#define H0(j, d) "s_waitcnt lgkmcnt(7-" #j ")\n\t" MA(d, 8, j, AX)
#define H1(j, c) "s_waitcnt lgkmcnt(8)\n\t" MA(AX, 40, j, c) LB(j)
#define PUB "ds_write_b128 v2, " AY "\n\ts_mov_b64 exec, 1\n\tds_or_b32 v3, v4 offset:4\n\ts_mov_b64 exec, -1\n\t"
#define WG_ "global_load_lds_dwordx4 %[go], %[wg]\n\ts_add_u32 m0, m0, 0x400\n\tv_add_u32 %[go], 0x1c000, %[go]\n\t"
#define WU(j) "global_load_dwordx4 %" #j ", %[gu], %[wu]\n\tv_add_u32 %[gu], 0x1c000, %[gu]\n\t"
#define WD(j, o) "global_load_dwordx4 %" #j ", %[dd], %[wd] offset:" #o "\n\t"

__global__ void __launch_bounds__(1024, 4) kf(const u16* __restrict__ x, const u16* __restrict__ wr, char* __restrict__ ws,
    const u16* __restrict__ dskip, u16* __restrict__ out, const u16* __restrict__ logdt,
    const u16* __restrict__ Are, const u16* __restrict__ Aim, const u16* __restrict__ Bre, const u16* __restrict__ Bim,
    const u16* __restrict__ Cre, const u16* __restrict__ Cim,
    const u16* __restrict__ wg, const u16* __restrict__ wu, const u16* __restrict__ wd) {
  __shared__ __attribute__((aligned(16))) char smem[LDS_SZ];
  LDS char* L = (LDS char*)smem;
  LDS char* F = L + O_F;
  const int lane = threadIdx.x & 63, w = __builtin_amdgcn_readfirstlane(threadIdx.x >> 6);
  u64 pc;
  asm volatile("ds_write_b32 %1, %2\n\ts_getpc_b64 %0" : "=s"(pc) : "v"(O_U + min(w >> 1, 4) * UP + (threadIdx.x & 127) * 4), "v"(0) : "memory");
  asm volatile("s_mov_b64 s[84:85], 0\n\ts_mov_b64 s[86:87], 0\n\ts_mov_b64 s[88:89], 0\n\ts_mov_b64 s[90:91], 0\n\ts_mov_b64 s[92:93], 0\n\t"
               "s_mov_b64 s[94:95], 0\n\ts_mov_b64 s[96:97], 0\n\ts_mov_b64 s[98:99], 0" ::: "s99");
  const int b = blockIdx.x, sw = (b & 7) * 32 + (b >> 3), cg = sw >> 1, h = sw & 1;
  const int c16 = lane & 15, kg = lane >> 4;
  const int t = sw * 8 + w, ch = w & 3, half = (w >> 2) & 1;
  v4u xr, wv;
  u32 tag0; v2u dsk; char* wsb;
  {
    const int d = cg * 4 + ch;
    asm volatile("s_waitcnt lgkmcnt(0)\n\ts_cmp_lt_u32 %[w], 8\n\ts_cbranch_scc0 1f\n\t"
                 "global_load_dwordx4 %0, %[xo], %[xp]\n\tglobal_load_dwordx4 %1, %[lo], %[wr]\n\t"
                 "s_load_dword %2, %[ws], 0\n\ts_load_dwordx2 %3, %[dk], %[co]\n\ts_setprio 3\n\ts_branch 2f\n"
                 "1:\n\tglobal_load_ushort v12, %[ix], %[p0]\n\tglobal_load_ushort v13, %[ix], %[p1]\n\tglobal_load_ushort v14, %[ix], %[p2]\n\t"
                 "global_load_ushort v15, %[ix], %[p3]\n\tglobal_load_ushort v16, %[ix], %[p4]\n\tglobal_load_ushort v17, %[ix], %[p5]\n\t"
                 "s_load_dwordx2 %3, %[ld], %[co]\n\ts_setprio 1\n2:\n\ts_mov_b64 %4, %[ws]"
                 : "=&{v[12:15]}"(xr), "=&{v[16:19]}"(wv), "=&s"(tag0), "=&{s[40:41]}"(dsk), "=&s"(wsb)
                 : [w] "s"(w), [lo] "v"(lane * 16), [xo] "v"(t * 1024 + lane * 16), [xp] "s"(x), [wr] "s"(wr), [ws] "s"(ws), [dk] "s"(dskip), [co] "s"(cg * 8),
                   [ix] "v"((d * 128 + half * 64 + lane) * 2), [p0] "s"(Are), [p1] "s"(Aim), [p2] "s"(Bre), [p3] "s"(Bim), [p4] "s"(Cre), [p5] "s"(Cim),
                   [ld] "s"(logdt), "s"(out), "s"(wg), "s"(wu), "s"(wd)
                 : "scc");
  }
  STAMP(0);
  const __amdgpu_buffer_rsrc_t rs = __builtin_amdgcn_make_buffer_rsrc(wsb, 0, WS_END, 0x00020000);
  if (__builtin_expect(w < 8, 1)) {
    LDS char* a_xn = L + O_LN + w * 1088 + lane * 16;
    LDS char* a_ks = F + F_KS + w * 8;
    LDS char* a_f = F;
    LDS char* a_kr = F + F_KS + (lane & 7) * 8;
    LDS char* a_tr = L + O_LN + ((lane >> 2) & 3) * 1088 + w * 128 + kg * 32 + (lane & 3) * 8;
    u32 o_pub = (lane >> 3) * 32768 + (lane & 7) * 16, rsh = (lane & 3) * 4 + 1, o_r = WS_R + sw * 16, o_s = lane * 8;
    u32 so_pub = WS_X + w * 8 * 32768 + sw * 128, so_s = WS_S + sw * 64, kix = 0x80000000u | (u32)(2047 - t);
    const u32 fq = ((b >> 4) + ((~b & 8) << 1)) | ((w - 6) & 0x100);
    const bool ffn = fq < 14;
    const int fs = (b & 7) * 14 + (b >> 4), fw = w & 1;
    asm volatile("s_barrier\n\ts_waitcnt vmcnt(0)"
                 : "+v"(xr), "+v"(wv), "+v"(a_xn), "+v"(a_ks), "+v"(a_f), "+v"(a_kr), "+v"(a_tr), "+v"(o_pub), "+v"(rsh), "+v"(o_r), "+v"(o_s),
                   "+s"(so_pub), "+s"(so_s), "+s"(kix) :: "memory");
    STAMP(1);
    float s1 = 0.f, s2 = 0.f, sr = 0.f, sw_ = 0.f;
#pragma unroll
    for (int j = 0; j < 4; j++) { s1 = dot2(xr[j], 0x3f803f80u, s1); s2 = dot2(xr[j], xr[j], s2); sr = dot2(xr[j], wv[j], sr); sw_ = dot2(wv[j], 0x3f803f80u, sw_); }
    float f4 = fold4(s1, s2, sr, sw_);
    f4 += dppf<0xB1>(f4); f4 += dppf<0x4E>(f4); f4 += dppf<0x141>(f4); f4 += dppf<0x140>(f4);
    f4 *= 1.f / 512.f;
    const float dm = rdl(f4, 0), mb = rbf(dm);
    const float sd = rbf(__builtin_amdgcn_sqrtf(rbf(rbf(fmaxf(rdl(f4, 32) - dm * dm, 0.f)) + 1e-5f)));
    const float r16 = __builtin_amdgcn_rcpf(sd * 16.f), off = -mb * r16;
    v4u xn;
#pragma unroll
    for (int j = 0; j < 4; j++) { v2f y = __builtin_elementwise_fma((v2f){lo2f(xr[j]), hi2f(xr[j])}, (v2f){r16, r16}, (v2f){off, off}); xn[j] = pk(y.x, y.y); }
    asm volatile("s_waitcnt lgkmcnt(0)" : "+s"(tag0), "+s"(dsk), "+v"(xn) :: "memory");
    const u32 tag = tag0 + 0x11111111u;
    *(LDS v4u*)a_xn = xn;
    const u32 rb = pk(0.f, (rdl(f4, 16) - mb * rdl(f4, 48)) * 8192.f * r16);
    const u32 key = rb ^ (((int)rb >> 31) & 0x7fff0000u) ^ kix;
    asm volatile("s_mov_b64 exec, 1\n\tds_write_b32 %0, %2 offset:%4\n\tds_write_b64 %1, %3\n\ts_mov_b64 exec, -1"
                 :: "v"(a_f), "v"(a_ks), "v"(tag), "v"((v2u){key, (__float_as_uint(mb) >> 16) | __float_as_uint(sd)}), "n"(F_TAG) : "memory");
    u32 pt[4];
    pt[0] = __builtin_amdgcn_alignbit(tag, tag, rsh) ^ 0x3C5A96C3u;
    pt[1] = pt[0] << 1; pt[2] = pt[0] << 2; pt[3] = pt[0] << 3;
    asm volatile("" : "+v"(pt[0]), "+v"(pt[1]), "+v"(pt[2]), "+v"(pt[3]));
    {
      v2u ks, a0, a1; u32 cnt;
      asm volatile("s_mov_b32 %3, 0x20000\n"
                   "1:\n\tds_read_b64 %0, %4\n\tds_read_b64_tr_b16 %1, %5\n\tds_read_b64_tr_b16 %2, %5 offset:4352\n\ts_waitcnt lgkmcnt(2)\n\t"
                   "v_cmp_gt_u64 vcc, %6, %0\n\ts_cbranch_vccz 2f\n\ts_sub_u32 %3, %3, 1\n\ts_cbranch_scc0 1b\n2:\n\ts_waitcnt lgkmcnt(0)"
                   : "=&v"(ks), "=&v"(a0), "=&v"(a1), "=&s"(cnt)
                   : "v"(a_kr), "v"(a_tr), "s"(1ull << 32)
                   : "memory", "scc", "vcc");
      if (w == 0) {
        u32 o = dppu<0xB1, 0xf>(ks[0]), k0 = max(ks[0], o), k1 = min(ks[0], o);
        merge2(k0, k1, dppu<0x4E, 0xf>(k0), dppu<0x4E, 0xf>(k1));
        merge2(k0, k1, dppu<0x141, 0xf>(k0), dppu<0x141, 0xf>(k1));
        if (lane == 0) __builtin_amdgcn_raw_buffer_store_b128((v4u){k0, k1, tag, tag}, rs, o_r, 0, 16);
      }
      v4u d = {a0[0], a0[1], a1[0], a1[1]};
#pragma unroll
      for (int j = 0; j < 4; j++) d[j] = (d[j] & ~0x40004000u) | (pt[j] & 0x40004000u);
      __builtin_amdgcn_raw_buffer_store_b128(d, rs, o_pub, so_pub, 16);
      if (w == 1 && lane < 8) __builtin_amdgcn_raw_buffer_store_b64((v2u){ks[1], tag}, rs, o_s, so_s, 16);
    }
    STAMP(2);
    const int si = w >> 1, s = h ? 2 + si : (si < 2 ? si : si + 4), t0 = s * 256 + (w & 1) * 128 + lane * 2;
    v8bf fg[8], fu[8];
    v4f sg = {0.f, 0.f, 0.f, 0.f}, su = sg;
    LDS char* const wl = L + O_W + fw * 8192;
    if (ffn) {
      v4u wq[16];
      const int rho = lane >> 1, kk = 8 * ((rho >> 2) & 3) + 4 * (rho >> 4) + (rho & 3);
      u32 go = (fw * 256 + kk) * 3584 + fs * 32 + (lane & 1) * 16, gu = go;
      asm volatile("s_mov_b32 m0, %[m]\n\ts_sleep %[sl]\n\t"
                   WG_ WG_ WG_ WG_ WG_ WG_ WG_ WG_ WU(0) WU(1) WU(2) WU(3) WU(4) WU(5) WU(6) WU(7)
                   WD(8, 0) WD(9, 64) WD(10, 128) WD(11, 192) WD(12, 256) WD(13, 320) WD(14, 384) WD(15, 448)
                   "s_waitcnt vmcnt(16)"
                   : "=&v"(wq[0]), "=&v"(wq[1]), "=&v"(wq[2]), "=&v"(wq[3]), "=&v"(wq[4]), "=&v"(wq[5]), "=&v"(wq[6]), "=&v"(wq[7]),
                     "=&v"(wq[8]), "=&v"(wq[9]), "=&v"(wq[10]), "=&v"(wq[11]), "=&v"(wq[12]), "=&v"(wq[13]), "=&v"(wq[14]), "=&v"(wq[15]),
                     [go] "+v"(go), [gu] "+v"(gu)
                   : [m] "s"(O_W + fw * 8192), [sl] "n"(D_W), [wg] "s"(wg), [wu] "s"(wu), [wd] "s"(wd),
                     [dd] "v"((fs * 16 + (lane >> 2)) * 1024 + fw * 512 + (lane & 3) * 16)
                   : "memory", "scc");
#pragma unroll
      for (int ks = 0; ks < 8; ks++) fg[ks] = cat(trd(wl + lane * 8 + ks * 1024), trd(wl + lane * 8 + ks * 1024 + 512));
      asm volatile("s_waitcnt vmcnt(8)"
                   : "+v"(wq[0]), "+v"(wq[1]), "+v"(wq[2]), "+v"(wq[3]), "+v"(wq[4]), "+v"(wq[5]), "+v"(wq[6]), "+v"(wq[7]) :: "memory");
#pragma unroll
      for (int ks = 0; ks < 8; ks++) *(LDS v4u*)(wl + ks * 1024 + lane * 16) = wq[ks];
      MEMB();
#pragma unroll
      for (int ks = 0; ks < 8; ks++) fu[ks] = cat(trd(wl + lane * 8 + ks * 1024), trd(wl + lane * 8 + ks * 1024 + 512));
      asm volatile("s_waitcnt vmcnt(0)"
                   : "+v"(wq[8]), "+v"(wq[9]), "+v"(wq[10]), "+v"(wq[11]), "+v"(wq[12]), "+v"(wq[13]), "+v"(wq[14]), "+v"(wq[15]) :: "memory");
#pragma unroll
      for (int ks = 0; ks < 8; ks++) *(LDS v4u*)(wl + ks * 1024 + lane * 16) = wq[8 + ks];
      MEMB();
      const v8bf ones = BF8(((v4u){0x3f803f80u, 0x3f803f80u, 0x3f803f80u, 0x3f803f80u}));
#pragma unroll
      for (int ks = 0; ks < 8; ks++) { sg = MF(fg[ks], ones, sg); su = MF(fu[ks], ones, su); }
      asm volatile("" : "+v"(sg), "+v"(su));
      STAMP(3);
      __builtin_amdgcn_s_sleep(D_K);
    } else {
      __builtin_amdgcn_s_sleep(D_A);
      if (w < 6 || h == 0) {
        const u32 o = (lane >> 2) * 128 + (lane & 3) * 16, so = WS_X + (cg >> 1) * 32768 + w * 4096 + (cg & 1) * 64;
        v4u a, c;
        int i = SPIN;
        bool bad;
        _Pragma("unroll 1") do {
          a = __builtin_amdgcn_raw_buffer_load_b128(rs, o, so, 16);
          c = __builtin_amdgcn_raw_buffer_load_b128(rs, o + 2048, so, 16);
#pragma unroll
          for (int j = 0; j < 4; j++) { a[j] ^= pt[j] & 0x40004000u; c[j] ^= pt[j] & 0x40004000u; }
          u32 e = (a[0] | a[1] | a[2]) | (a[3] | c[0] | c[1]) | (c[2] | c[3]);
          bad = __any(e & 0x40004000u);
          MEMB();
        } while (bad && --i);
        LDS char* q = L + O_U + (lane & 3) * UP + 512 + (w * 32 + (lane >> 2)) * 16;
        *(LDS v4u*)q = a;
        *(LDS v4u*)(q + 256) = c;
        lds_sig(F + F_U, 1u << w);
      }
      STAMP(3);
    }
    if (w == 7 || ffn) {
      v4u k[4];
      {
        int i = SPIN;
        bool bad;
        _Pragma("unroll 1") do {
#pragma unroll
          for (int j = 0; j < 4; j++) k[j] = __builtin_amdgcn_raw_buffer_load_b128(rs, j * 1024 + lane * 16, WS_R, 16);
          bad = __any(((k[0][2] ^ tag) | (k[1][2] ^ tag) | (k[2][2] ^ tag) | (k[3][2] ^ tag)) != 0);
          MEMB();
        } while (bad && --i);
      }
      u32 k0 = k[0][0], k1 = k[0][1], k2 = k[2][0], k3 = k[2][1];
      merge2(k0, k1, k[1][0], k[1][1]); merge2(k2, k3, k[3][0], k[3][1]); merge2(k0, k1, k2, k3);
      const u32 s0 = wave_max(k0), s1 = wave_max(k0 == s0 ? k1 : k0);
      const int tk = 2047 - (((lane & 1) ? s1 : s0) & 2047);
      v4u xf[8]; v2u st;
      if (ffn) {
#pragma unroll
        for (int ks = 0; ks < 8; ks++) xf[ks] = *(const GLB v4u*)((const GLB char*)x + fw * 512 + ks * 64 + (u32)(tk * 1024 + kg * 16));
        st = __builtin_amdgcn_raw_buffer_load_b64(rs, tk * 8, WS_S, 16);
      }
      STAMP(4);
      asm volatile("s_mov_b64 exec, 1\n\tds_write_b64 %0, %1 offset:%3\n\tds_or_b32 %0, %2 offset:%4\n\ts_mov_b64 exec, -1"
                   :: "v"(F), "v"((v2u){s0, s1}), "v"(256u), "n"(F_TS), "n"(F_SP) : "memory");
      if (ffn) {
        v4f g = {0.f, 0.f, 0.f, 0.f}, u = g;
#pragma unroll
        for (int ks = 0; ks < 8; ks++) { g = MF(fg[ks], BF8(xf[ks]), g); u = MF(fu[ks], BF8(xf[ks]), u); }
        SB();
        v8bf wdf[8];
        LDS char* dl = wl + (lane >> 2) * 64 + (lane & 3) * 8;
#pragma unroll
        for (int j = 0; j < 8; j++) wdf[j] = cat(trd(dl + j * 1024), trd(dl + j * 1024 + 32));
        v2u hz[8];
#pragma unroll
        for (int j = 0; j < 8; j++) hz[j] = (v2u){0, 0};
        asm volatile("" : "+v"(hz[0]), "+v"(hz[1]), "+v"(hz[2]), "+v"(hz[3]), "+v"(hz[4]), "+v"(hz[5]), "+v"(hz[6]), "+v"(hz[7]));
        _Pragma("unroll 1") for (int i = 0; i < SPIN; i++) {
          if (!__any(st[1] != tag)) break;
          MEMB();
          st = __builtin_amdgcn_raw_buffer_load_b64(rs, tk * 8, WS_S, 16);
        }
        const float mm = lo2f(st[0]), rsd = __builtin_amdgcn_rcpf(hi2f(st[0]));
        g -= sg * mm; u -= su * mm;
        LDS char* ex = F + F_E + (kg * 2 + (lane & 1)) * 32;
        if (c16 < 2) { *(LDS v4f*)(ex + fw * 256) = g; *(LDS v4f*)(ex + fw * 256 + 16) = u; }
        lds_sig(F + F_EX, 1u << fw);
        STAMP(5);
        v4f eg, eu;
        {
          u32 f, sc, cnt;
          asm volatile("s_mov_b32 %4, 0x20000\n"
                       "1:\n\tds_read_b32 %2, %5\n\tds_read_b128 %0, %6\n\tds_read_b128 %1, %6 offset:16\n\t"
                       "s_waitcnt lgkmcnt(2)\n\tv_readfirstlane_b32 %3, %2\n\ts_cmp_eq_u32 %3, 3\n\t"
                       "s_cbranch_scc1 2f\n\ts_sub_u32 %4, %4, 1\n\ts_cbranch_scc0 1b\n2:\n\ts_waitcnt lgkmcnt(0)"
                       : "=&v"(eg), "=&v"(eu), "=&v"(f), "=&s"(sc), "=&s"(cnt) : "v"(F + F_EX), "v"(ex + (fw ^ 1) * 256) : "memory", "scc");
        }
        g = (g + eg) * rsd; u = (u + eu) * rsd;
        float hh[4];
#pragma unroll
        for (int i = 0; i < 4; i++) hh[i] = g[i] * __builtin_amdgcn_rcpf(1.f + __builtin_amdgcn_exp2f(g[i] * -1.44269504089f)) * u[i];
        const v2u hf = {pk(hh[0], hh[1]), pk(hh[2], hh[3])};
#define HM(j) "v_mov_b64 %" #j ", %8\n\ts_lshl_b64 exec, exec, 2\n\t"
        asm volatile("s_mov_b64 exec, %9\n\t" HM(0) HM(1) HM(2) HM(3) HM(4) HM(5) HM(6) "v_mov_b64 %7, %8\n\ts_mov_b64 exec, -1\n\ts_nop 0"
                     : "+v"(hz[0]), "+v"(hz[1]), "+v"(hz[2]), "+v"(hz[3]), "+v"(hz[4]), "+v"(hz[5]), "+v"(hz[6]), "+v"(hz[7])
                     : "v"(hf), "s"(0x0003000300030003ull) : "scc");
        v4f e0 = {0.f, 0.f, 0.f, 0.f}, e1 = e0;
#pragma unroll
        for (int j = 0; j < 4; j++) {
          v8bf hb = BF8(((v4u){hz[2 * j][0], hz[2 * j][1], hz[2 * j + 1][0], hz[2 * j + 1][1]}));
          e0 = MF(wdf[j], hb, e0); e1 = MF(wdf[4 + j], hb, e1);
        }
        const u32 po = (c16 >> 1) * 128 + kg * 32 + (lane & 1) * 16;
        __builtin_amdgcn_raw_buffer_store_b128((v4u){pk(e0[0], e0[1]), pk(e0[2], e0[3]), tag, tag}, rs, po, WS_P + fs * 4096 + fw * 2048, 16);
        __builtin_amdgcn_raw_buffer_store_b128((v4u){pk(e1[0], e1[1]), pk(e1[2], e1[3]), tag, tag}, rs, po + 1024, WS_P + fs * 4096 + fw * 2048, 16);
        STAMP(6);
      } else if (b == 0 && lane == 0) *(GLB u32*)ws = tag;
    }
    __builtin_amdgcn_s_setprio(0);
    v4u sv;
    {
      int i = SPIN;
      bool bad;
      _Pragma("unroll 1") do {
        sv = __builtin_amdgcn_raw_buffer_load_b128(rs, WS_S + t0 * 8, 0, 16);
        bad = __any(((sv[1] ^ tag) | (sv[3] ^ tag)) != 0);
        MEMB();
      } while (bad && --i);
    }
    lds_wait<1>(F + F_U, 1u << s);
    v2f base[4];
    {
      const v2f m2 = {lo2f(sv[0]), lo2f(sv[2])}, sd2 = (v2f){hi2f(sv[0]), hi2f(sv[2])} * 16.f;
#pragma unroll
      for (int c = 0; c < 4; c++) {
        u32 uu = *(LDS u32*)(L + O_U + c * UP + 512 + t0 * 2), dd = dsk[c >> 1];
        float dk = ((c & 1) ? hi2f(dd) : lo2f(dd)) * 16.f + 16.f;
        base[c] = __builtin_elementwise_fma((v2f){lo2f(uu), hi2f(uu)}, sd2 + dk, m2);
      }
    }
    lds_wait<1>(F + F_SP, 256);
    const v2u ts = *(LDS v2u*)(F + F_TS);
    const int ts0 = 2047 - (ts[0] & 2047), ts1 = 2047 - (ts[1] & 2047);
    const int n = (w & 1) * 8 + (lane >> 3);
    u64 m0 = __builtin_amdgcn_ballot_w64(t0 != ts0 && t0 != ts1), m1 = __builtin_amdgcn_ballot_w64(t0 + 1 != ts0 && t0 + 1 != ts1);
    GLB char* op = (GLB char*)out + cg * 8;
    u32 oo = t0 * 1024;
    asm volatile("" : "+v"(oo), "+s"(op), "+s"(m0), "+s"(m1), "+v"(base[0]), "+v"(base[1]), "+v"(base[2]), "+v"(base[3]));
    if (!ffn) STAMP(5);
    v2f pr[8];
    {
      u32 f, sc, cnt;
      asm volatile("s_mov_b32 %10, 0x20000\n"
                   "1:\n\tds_read_b32 %8, %11\n\ts_waitcnt lgkmcnt(0)\n\tv_readfirstlane_b32 %9, %8\n\ts_and_b32 %9, %13, %9\n\t"
                   "s_cmp_gt_u32 %9, %14\n\ts_cbranch_scc1 3f\n\ts_sleep 1\n\ts_sub_u32 %10, %10, 1\n\ts_cbranch_scc0 1b\n"
                   "3:\n\tds_read_b32 %8, %11\n\tds_read_b64 %0, %12\n\tds_read_b64 %1, %12 offset:%15\n\tds_read_b64 %2, %12 offset:%15*2\n\t"
                   "ds_read_b64 %3, %12 offset:%15*3\n\tds_read_b64 %4, %12 offset:4096\n\tds_read_b64 %5, %12 offset:%15+4096\n\t"
                   "ds_read_b64 %6, %12 offset:%15*2+4096\n\tds_read_b64 %7, %12 offset:%15*3+4096\n\t"
                   "s_waitcnt lgkmcnt(8)\n\tv_readfirstlane_b32 %9, %8\n\ts_andn2_b32 %9, %13, %9\n\t"
                   "s_cbranch_scc0 2f\n\ts_waitcnt lgkmcnt(0)\n\ts_sub_u32 %10, %10, 1\n\ts_cbranch_scc0 3b\n2:\n\ts_setprio 3\n\ts_waitcnt lgkmcnt(0)"
                   : "=&v"(pr[0]), "=&v"(pr[1]), "=&v"(pr[2]), "=&v"(pr[3]), "=&v"(pr[4]), "=&v"(pr[5]), "=&v"(pr[6]), "=&v"(pr[7]),
                     "=&v"(f), "=&s"(sc), "=&s"(cnt)
                   : "v"(F + F_PH), "v"(L + si * 1024 + n * 64 + 16 * (((lane >> 1) & 3) ^ ((n >> 1) & 3)) + (lane & 1) * 8),
                     "s"(0xf000fu << (4 * si)), "s"(0xf0000u << (4 * si)), "n"(BC) : "memory", "scc");
    }
    if (!ffn) STAMP(6);
#pragma unroll
    for (int c = 0; c < 4; c++) base[c] += pr[c] + pr[4 + c];
    asm volatile("s_mov_b64 exec, %4\n\tglobal_store_dwordx2 %0, %1, %3\n\ts_mov_b64 exec, %5\n\tglobal_store_dwordx2 %0, %2, %3 offset:1024\n\ts_mov_b64 exec, -1"
                 :: "v"(oo), "v"((v2u){pk(base[0][0], base[1][0]), pk(base[2][0], base[3][0])}),
                    "v"((v2u){pk(base[0][1], base[1][1]), pk(base[2][1], base[3][1])}), "s"(op), "s"(m0), "s"(m1) : "memory");
    if (__builtin_expect((m0 & m1) != ~0ull, 0)) {
#pragma unroll
      for (int i = 0; i < 2; i++) {
        if (t0 + i == ts0 || t0 + i == ts1) {
          *(LDS v4f*)(F + F_SV + (t0 + i == ts0 ? 0 : 16)) = (v4f){base[0][i], base[1][i], base[2][i], base[3][i]};
          asm volatile("ds_or_b32 %0, %1" :: "v"(F + F_SP), "v"(t0 + i == ts0 ? 1u : 2u) : "memory");
        }
      }
    }
    STAMP(7);
    FLUSH();
    asm volatile("s_endpgm" ::: "memory");
  }
  {
    asm volatile("" : "={v[12:15]}"(xr), "={v[16:19]}"(wv), "={s[40:41]}"(dsk));
    SB();
    asm volatile("s_barrier" ::: "memory");
    u64 pfb; u32 pfo;
    {
      const int g = b & 7, i = b >> 3;
      pfb = w == 8 ? (u64)wg : w == 9 ? (u64)wu : w == 10 ? (u64)wd : pc;
      const u32 o = w < 10 ? i * 16 * 3584 + ((448 * g) & ~127) : w == 10 ? (224 * g + 7 * i) * 1024 : (i & 3) * 4096;
      pfo = min(o + (lane >> 2) * (w < 10 ? 3584 : w == 10 ? 512 : 256) + (lane & 3) * (w < 11 ? 128 : 64), w < 10 ? ~0u : w == 10 ? o + 55 * 128 : CODE_SZ);
      asm volatile("s_cmp_ge_u32 %3, %4\n\ts_cbranch_scc1 1f\n\ts_mov_b32 m0, %2\n\ts_nop 0\n\tglobal_load_lds_dword %0, %1\n1:"
                   :: "v"(pfo), "s"(pfb), "n"(LDS_SZ - 256), "s"(PF_LATE ? w - 11 : w - 8), "n"(PF_LATE ? 1 : 4) : "memory", "scc");
    }
    LDS char* const Bc = L + ch * BC;
    LDS char* const Ac = L + O_A + ch * AC;
    LDS char* const Uc = L + O_U + ch * UP;
    const u32 tb = half ? (c16 < 8 ? 0x4f32f10fu : 0xfffff765u) : (c16 < 8 ? 0x76543210u : 0xcbaaa988u);
    const u32 ar = (tb >> ((c16 & 7) * 4)) & 15;
    LDS char* pa = (ar == 15 ? Uc : Ac + half * A_HI + ar * RP + (kg & 1) * AH) + (kg >> 1) * 16;
    LDS char* pb = Bc + c16 * RP + (kg & 1) * (BC / 2) + (kg >> 1) * 16;
    const int be = c16 + 16 - 8 * kg;
    v4u b35 = band<3, 5>(be, 0x3f80), b6a = band<6, 10>(be, 0x3f80), b5b = band<5, 11>(be, 0x3f80), b4c = band<4, 12>(be, 0x3f80), b34 = band<3, 4>(be, 0xffff);
    asm volatile("" : "+v"(pa), "+v"(pb), "+v"(b35), "+v"(b6a), "+v"(b5b), "+v"(b4c), "+v"(b34));
    {
      asm volatile("s_waitcnt lgkmcnt(0)" : "+s"(dsk) :: "memory");
      const u32 ldt = dsk[ch >> 1];
      const float dt = rbf(__builtin_amdgcn_exp2f(((ch & 1) ? hi2f(ldt) : lo2f(ldt)) * 1.44269504089f));
      float d1 = dt * 1.44269504089f, d2 = dt * 0.15915494309f, d16 = dt * 16.f;
      asm volatile("s_cmp_lt_u32 %6, %7\n\ts_cbranch_scc1 1f\n\ts_waitcnt vmcnt(0)\n1:\n\ts_waitcnt vmcnt(1)\n\t"
                   "s_mov_b32 m0, %5"
                   : "+v"(xr), "+v"(wv), "+v"(d1), "+v"(d2), "+v"(d16)
                   : "s"(ch * BC + half * (BC / 2)), "s"(PF_LATE ? w - 11 : w - 8), "n"(PF_LATE ? 1 : 4), "s"(w), "v"(pfo), "s"(pfb), "n"(LDS_SZ - 256) : "memory", "scc");
      STAMP(1);
      const float er = d1 * lo2f(xr[0]), ph = d2 * lo2f(xr[1]);
      const v2f z = cpow(er, ph), E = cpow(er * 256.f, ph * 256.f);
      const v2f z2 = cmul(z, z), cb = cmul((v2f){lo2f(wv[0]), lo2f(wv[1])}, (v2f){lo2f(xr[2]), lo2f(xr[3])} * d16);
      const v2f z4 = cmul(z2, z2), z8 = cmul(z4, z4);
      v2f o = {z8.x, -z8.y};
      const v2f m = cmul(o, o);
      v2f e = m;
      put1<0>(0x3f80u); put1<16 * RP>(pk(o.x, o.y)); put1<RP>(pk(e.x, e.y));
      brows<2>(e, o, m);
      v2f x1, y, c0, c1;
      step2<31 * RP, 31 * RP>(x1, cb, E, o, o, m);
      asm volatile("s_mov_b32 m0, %0\n\ts_nop 0" :: "s"(20000 + ch * AC + half * AH) : "memory");
#define AL(r) (AOFF + (r) * RP)
#define AU(r) (AOFF + A_HI + (r) * RP)
      put1<AL(0)>(pk(cb.x, cb.y)); put1<AL(8)>(pk(x1.x, x1.y));
      step2<AL(1), AL(12)>(c1, cb, z, y, x1, z8);
      step2<AL(2), AU(2)>(c0, cb, z2, y, y, E);
      step2<AL(3), AU(3)>(c1, c1, z2, y, y, E);
      step2<AL(4), AU(4)>(c0, c0, z2, y, y, E);
      step2<AL(5), AU(5)>(c1, c1, z2, y, y, E);
      step2<AL(6), AU(6)>(c0, c0, z2, y, y, E);
      step2<AL(9), AL(10)>(c0, x1, z2, e, x1, z4);
      step2<AL(7), AU(7)>(c1, c1, z2, y, y, E);
      step2<AL(11), AU(0)>(c0, e, z2, e, e, E);
      e = cmul(e, E); put1<AU(1)>(pk(e.x, e.y));
    }
    lds_sig(F + F_X, 1u << (w - 8));
    STAMP(2);
    lds_wait<1>(F + F_X, 17u << ch);
    v4f e0 = {0.f, 0.f, 0.f, 0.f}, e1 = e0;
    {
      v8bf fa[4], f0[4], f1[4];
#pragma unroll
      for (int k = 0; k < 4; k++) { fa[k] = *(LDS v8bf*)(pa + k * 32); f0[k] = *(LDS v8bf*)(pb + k * 32); f1[k] = *(LDS v8bf*)(pb + 16 * RP + k * 32); }
      SB();
#pragma unroll
      for (int k = 0; k < 4; k++) {
        e0 = MF(fa[k], f0[k], e0); e1 = MF(fa[k], f1[k], e1);
        fa[k] = *(LDS v8bf*)(pa + 128 + k * 32); f0[k] = *(LDS v8bf*)(pb + 128 + k * 32); f1[k] = *(LDS v8bf*)(pb + 16 * RP + 128 + k * 32);
        SB();
      }
      lds_sig(F + F_U, 256u << (w - 8));
#pragma unroll
      for (int k = 0; k < 4; k++) { e0 = MF(fa[k], f0[k], e0); e1 = MF(fa[k], f1[k], e1); }
    }
    LDS char* const pw = Bc + half * 4096 + c16 * 64 + 16 * (kg ^ ((c16 >> 1) & 3));
    const u32 umask = (h ? 63 : 255) | 0x1100u << ch;
    if (!half) {
      LDS char* const K0 = Ac + 32;
      LDS char* const cpy = Ac + (kg >> 1) * (AH + 128) + (kg & 1) * 1344;
      const v2u w0 = {pk(e0[0], e0[1]), pk(e0[2], e0[3])}, w1 = {pk(e1[0], e1[1]), pk(e1[2], e1[3])};
      LDS char* q = K0 + (kg >> 1) * 512 + c16 * 32 + (kg & 1) * 8;
      *(LDS v2u*)q = w0;
      *(LDS v2u*)(q + 16) = w1;
      *(LDS u32*)(cpy + (c16 & 7) * 4) = 0;
      MEMB();
      LDS char* p = K0 + lane * 16;
      const v4u un = *(LDS v4u*)p; const v2u pv = *(LDS v2u*)(p - 8);
      {
        const int i0 = 2 * c16 - kg;
        const u32 sl = i0 > 2 ? 0x0c0cu : i0 == -3 ? 0x0302u : i0 == -2 ? 0x0504u : 0x0706u, sh = i0 > 1 ? 0x0c0cu : i0 == -3 ? 0x0504u : 0x0706u;
        *(LDS u32*)(cpy + 1056 + c16 * 4) = __builtin_amdgcn_perm(__builtin_amdgcn_readlane(w1[1], 63), __builtin_amdgcn_readlane(w1[0], 63), sl | sh << 16);
      }
      const u32 a0 = __builtin_amdgcn_alignbit(pv[1], pv[0], 16), a1 = __builtin_amdgcn_alignbit(un[0], pv[1], 16),
                a2 = __builtin_amdgcn_alignbit(un[1], un[0], 16), a3 = __builtin_amdgcn_alignbit(un[2], un[1], 16),
                a4 = __builtin_amdgcn_alignbit(un[3], un[2], 16);
      *(LDS v4u*)(p + 1344) = (v4u){a1, a2, a3, a4};
      *(LDS v4u*)(p + AH + 128) = (v4u){pv[1], un[0], un[1], un[2]};
      *(LDS v4u*)(p + AH + 128 + 1344) = (v4u){a0, a1, a2, a3};
      STAMP(3);
      u32 c0, c1, si = 0;
      asm volatile(
        LA8(40, 512) LA8(8, 0) LA(72, 0, 576)
        "v_mov_b32 v4, 0\n\tv_mov_b64 v[112:113], 0\n\tv_mov_b64 v[114:115], 0\n\ts_setprio 2\n\ts_mov_b32 %1, 0x20000\n" ST_(4)
        "1:\n\tds_read_b32 v5, v3\n\ts_waitcnt lgkmcnt(0)\n\tv_readfirstlane_b32 %0, v5\n\ts_andn2_b32 %0, %[msk], %0\n\t"
        "s_cbranch_scc0 2f\n\ts_sub_u32 %1, %1, 1\n\ts_cbranch_scc0 1b\n2:\n\t" ST_(5)
        LB8
        "s_cmp_eq_u32 %[h], 0\n\ts_cbranch_scc1 4f\n\ts_branch 6f\n"
        "3:\n\ts_waitcnt lgkmcnt(8)\n\tv_mfma_f32_16x16x32_bf16 " AX ", v[72:75], v[108:111], " AX "\n"
        "4:\n\t" H0(0, AX) H0(1, AX) H0(2, AX) H0(3, AX) H0(4, AX) H0(5, AX) H0(6, AX) H0(7, AY)
        "v_add_u32 v2, %0, v2\n\ts_movk_i32 %0, 0x400\n\tv_mov_b32 v4, %[bit]\n\ts_lshl_b32 %[bit], %[bit], 4\n\t"
        "s_add_u32 %2, %2, 1\n\ts_cmp_eq_u32 %2, 4\n\ts_cbranch_scc1 7f\n\t"
        "s_cmp_lg_u32 %2, %[jmp]\n\ts_cbranch_scc1 6f\n\t"
        "v_add_u32 v1, 0x800, v1\n\t" LB8
        "6:\n\tv_add_u32 v1, 0x200, v1\n\tds_read_b128 v[108:111], v1 offset:448\n\t"
        H1(0, "0") H1(1, AX) PUB H1(2, AX) H1(3, AX) H1(4, AX) H1(5, AX) H1(6, AX) H1(7, AX)
        "s_cmp_lg_u32 %2, %[one]\n\ts_cbranch_scc1 3b\n\ts_branch 4b\n"
        "7:\n\ts_nop 2\n\t" PUB ST_(6) FLUSH_
        "s_waitcnt lgkmcnt(0)\n\ts_endpgm"
        : "=&s"(c0), "=&s"(c1), "+s"(si)
        : "{v0}"(Ac + ((lane >> 3) & 1) * (AH + 128) + ((lane >> 2) & 1) * 1344 + 56 - kg * 16 + (lane & 3) * 8),
          "{v1}"(Uc + 32 - 1024 + h * 512 + c16 * 32 + kg * 16), "{v2}"(pw), "{v3}"(F + F_U), [msk] "s"(umask), [bit] "s"(1u << ch),
          [h] "s"(h), [jmp] "s"(h ? 9 : 2), [one] "s"(h ? 9 : 1)
          , STADDR
        : "memory", "scc", "v127");
    }
    {
      LDS char* const T = Ac + A_HI;
      {
        LDS u32* q = (LDS u32*)(T + kg * 256 + c16 * 4);
        q[0] = pk(e0[0], e1[0]); q[16] = pk(e0[1], e1[1]); q[32] = pk(e0[2], e1[2]); q[48] = pk(e0[3], e1[3]);
      }
      MEMB();
      LDS char* tp = T + (c16 - 4 * kg - 3) * 4;
      v8bf ta[3], tb4[3], t8[2];
      {
        v2u q[16]; u32 kk;
        asm volatile(TB(0, 1, 1) TB(2, 3, 2) TB(4, 5, 3) TB(6, 7, 4) TB(8, 9, 5) TB(10, 11, 6) TB(12, 13, 7) TB(14, 15, 8)
                     "ds_read_b32 %16, %[t] offset:380\n\ts_waitcnt lgkmcnt(0)"
                     : "=&v"(q[0]), "=&v"(q[1]), "=&v"(q[2]), "=&v"(q[3]), "=&v"(q[4]), "=&v"(q[5]), "=&v"(q[6]), "=&v"(q[7]),
                       "=&v"(q[8]), "=&v"(q[9]), "=&v"(q[10]), "=&v"(q[11]), "=&v"(q[12]), "=&v"(q[13]), "=&v"(q[14]), "=&v"(q[15]), "=&v"(kk)
                     : [p] "v"(tp), [t] "v"(T) : "memory");
#pragma unroll
        for (int i = 0; i < 3; i++) { ta[i] = cat2(q[2 * i], q[2 * i + 1]); tb4[i] = cat2(q[6 + 2 * i], q[7 + 2 * i]); }
        t8[0] = cat2(q[12], q[13]); t8[1] = cat2(q[14], q[15]);
        kk = __builtin_amdgcn_perm(kk, kk, 0x03020302u);
#pragma unroll
        for (int j = 0; j < 4; j++) b34[j] &= kk;
      }
      asm volatile("" : "+v"(ta[0]), "+v"(ta[1]), "+v"(ta[2]), "+v"(tb4[0]), "+v"(tb4[1]), "+v"(tb4[2]), "+v"(t8[0]), "+v"(t8[1]), "+v"(b34));
      LDS char* pu = Uc + 512 - 48 + (h ? 0 : 1024) + c16 * 32 + kg * 16;
      __builtin_amdgcn_s_setprio(2);
      STAMP(3);
      lds_wait<0>(F + F_U, umask);
      STAMP(4);
      const v4f zz = {0.f, 0.f, 0.f, 0.f};
      v8bf ua[4], ub[4];
#pragma unroll
      for (int a = 0; a < 4; a++) { ua[a] = *(LDS v8bf*)(pu + a * 512 + 16); ub[a] = *(LDS v8bf*)(pu + a * 512); }
      v4f r0 = zz, r1 = zz, r2 = zz, r3 = zz;
      v8bf wa0 = wop(ua[0], ub[0], b35, b35), wb0 = wop(ua[0], ub[0], b6a, b6a);
      v8bf wa1 = wop(ua[1], ub[1], b35, b35), wb1 = wop(ua[1], ub[1], b6a, b6a);
      if (h) {
        r0 = MF(wa0, ta[0], r0); r1 = MF(wa0, ta[1], r1); r0 = MF(wb0, tb4[0], r0); r1 = MF(wb0, tb4[1], r1);
        r1 = MF(wa1, ta[0], r1); r1 = MF(wb1, tb4[0], r1);
      }
      r2 = MF(wa0, ta[2], r2); r3 = MF(wa1, ta[2], r3); r2 = MF(wb0, tb4[2], r2); r3 = MF(wb1, tb4[2], r3);
      *(LDS v4f*)pw = r0;
      *(LDS v4f*)(pw + 1024) = r1;
      lds_sig(F + F_PH, 0x110000u << ch);
      r2 = MF(wa1, ta[1], r2); r2 = MF(wb1, tb4[1], r2);
      {
        v8bf wa2 = wop(ua[2], ub[2], b35, b35), wb2 = wop(ua[2], ub[2], b6a, b6a), w80 = wop(ua[0], ub[0], b5b, b4c);
        r2 = MF(BF8(b34), ua[0], r2); r3 = MF(BF8(b34), ua[1], r3);
        r2 = MF(wa2, ta[0], r2); r3 = MF(wa2, ta[1], r3); r2 = MF(wb2, tb4[0], r2); r3 = MF(wb2, tb4[1], r3);
        r2 = MF(w80, t8[0], r2); r3 = MF(w80, t8[1], r3);
      }
      if (!h) {
        v8bf va0 = *(LDS v8bf*)(pu - 1024 + 16), vb0 = *(LDS v8bf*)(pu - 1024), va1 = *(LDS v8bf*)(pu - 512 + 16), vb1 = *(LDS v8bf*)(pu - 512);
        v2u q[4];
        asm volatile(TB(0, 1, 9) TB(2, 3, 10) "s_waitcnt lgkmcnt(0)" : "=&v"(q[0]), "=&v"(q[1]), "=&v"(q[2]), "=&v"(q[3]) : [p] "v"(tp) : "memory");
        const v8bf t2 = cat2(q[0], q[1]), t3 = cat2(q[2], q[3]);
        v8bf x0 = wop(va0, vb0, b5b, b4c), x1 = wop(va1, vb1, b5b, b4c);
        r2 = MF(x0, t2, r2); r3 = MF(x0, t3, r3); r2 = MF(x1, t8[1], r2); r3 = MF(x1, t2, r3);
      }
      {
        v8bf wa3 = wop(ua[3], ub[3], b35, b35), wb3 = wop(ua[3], ub[3], b6a, b6a), w81 = wop(ua[1], ub[1], b5b, b4c);
        *(LDS v4f*)(pw + 2048) = r2;
        lds_sig(F + F_PH, 0x1000000u << ch);
        r3 = MF(wa3, ta[0], r3); r3 = MF(wb3, tb4[0], r3); r3 = MF(w81, t8[0], r3);
      }
      *(LDS v4f*)(pw + 3072) = r3;
      lds_sig(F + F_PH, 0x10000000u << ch);
      STAMP(5);
      if (ch == 0) {
        __builtin_amdgcn_s_setprio(3);
        lds_wait<1>(F + F_SP, 256);
        const v2u ts = *(LDS v2u*)(F + F_TS);
        const u32 tag = *(LDS u32*)(F + F_TAG);
        const u32 s0 = __builtin_amdgcn_readfirstlane(ts[0]), s1 = __builtin_amdgcn_readfirstlane(ts[1]);
        const int ts0 = 2047 - (s0 & 2047), ts1 = 2047 - (s1 & 2047);
        const u32 own = h ? 0x3Cu : 0xC3u;
        const u32 need = ((own >> (ts0 >> 8)) & 1) | ((own >> (ts1 >> 8)) & 1) << 1;
        if (need) {
          const u32 kk = (lane & 1) ? s1 : s0;
          const float r = __uint_as_float((kk ^ 0x80000000u ^ ((~(int)kk >> 31) & 0x7fff0000u)) & 0xffff0000u);
          float sgm = rbf(__builtin_amdgcn_rcpf(1.f + __builtin_amdgcn_exp2f(r * -1.44269504089f)));
          u32 c_lo = 0x3f80u, c_hi = 0x3f800000u;
          asm volatile("" : "+v"(sgm), "+s"(c_lo), "+s"(c_hi));
          __builtin_amdgcn_s_sleep(D_Q);
          const u32 o0 = (lane >> 1) * 4096 + (lane & 1) * 16, o3 = o0 + (lane < 32 ? 96 * 4096 : 0);
          v4u a[4];
          int i = SPIN;
          bool bad;
          _Pragma("unroll 1") do {
#pragma unroll
            for (int j = 0; j < 4; j++) a[j] = __builtin_amdgcn_raw_buffer_load_b128(rs, j < 3 ? o0 : o3, WS_P + cg * 32 + (j < 3 ? j * 32 * 4096 : 0), 16);
            bad = __any(((a[0][2] ^ tag) | (a[1][2] ^ tag) | (a[2][2] ^ tag) | (a[3][2] ^ tag)) != 0);
            MEMB();
          } while (bad && --i);
          STAMP(6);
          if (lane >= 32) { a[3][0] = 0; a[3][1] = 0; }
          float y[4] = {0.f, 0.f, 0.f, 0.f};
#pragma unroll
          for (int j = 0; j < 4; j++) {
            y[0] = dot2(a[j][0], c_lo, y[0]); y[1] = dot2(a[j][0], c_hi, y[1]);
            y[2] = dot2(a[j][1], c_lo, y[2]); y[3] = dot2(a[j][1], c_hi, y[3]);
          }
          float f4 = fold4(y[0], y[2], y[1], y[3]);
          f4 += dppf<0x4E>(f4); f4 += dppf<0x124>(f4); f4 += dppf<0x128>(f4);
          const float up = rbf(f4) * sgm;
          lds_wait<0>(F + F_SP, need);
          if (c16 < 2 && ((need >> c16) & 1))
            out[(c16 ? ts1 : ts0) * 512 + cg * 4 + kg] = (u16)pk(*(LDS float*)(F + F_SV + c16 * 16 + kg * 4) + up, 0.f);
        }
      }
      STAMP(7);
      FLUSH();
    }
  }
}

__global__ void __launch_bounds__(64) kprobe(char* ws) {
  u64 ts; asm volatile("s_memrealtime %0\n\ts_waitcnt lgkmcnt(0)" : "=s"(ts) :: "memory");
  const int lane = threadIdx.x;
  const u64* st = (const u64*)(ws + WS_STAMP);
  u64 t0 = ~0ull, mn = ~0ull, mx = 0;
  for (int j = lane; j < 256; j += 64) { u64 v = st[(j * 16) * 8]; t0 = v < t0 ? v : t0; }
  for (int j = lane; j < 256 * PW; j += 64) { int b = j / PW; if ((PX >> (b & 7)) & 1) { u64 v = st[(b * 16 + PWO + j % PW) * 8 + PI]; if (v) { mn = v < mn ? v : mn; mx = v > mx ? v : mx; } } }
  for (int o = 32; o; o >>= 1) {
    u64 a = __shfl_xor(t0, o), b = __shfl_xor(mn, o), c = __shfl_xor(mx, o);
    t0 = a < t0 ? a : t0; mn = b < mn ? b : mn; mx = c > mx ? c : mx;
  }
  u64 x = (PMODE == 0 ? t0 : PMODE == 1 ? mn : mx) - t0;
  if (x > 5000) x = 5000;
  u64 t1;
  u64 end = t0 + 3000 + 10 * x;
  for (int i = 0; i < 2000000; i++) {
    asm volatile("s_memrealtime %0\n\ts_waitcnt lgkmcnt(0)" : "=s"(t1) :: "memory");
    if (t1 >= end) break;
  }
}

extern "C" void kernel_launch(void* const* d_in, const int* in_sizes, int n_in, void* d_out, int out_size, void* d_ws,
                              size_t ws_size, hipStream_t stream) {
  kf<<<256, 1024, 0, stream>>>((const u16*)d_in[0], (const u16*)d_in[10], (char*)d_ws, (const u16*)d_in[8], (u16*)d_out,
                               (const u16*)d_in[7], (const u16*)d_in[1], (const u16*)d_in[2], (const u16*)d_in[3], (const u16*)d_in[4],
                               (const u16*)d_in[5], (const u16*)d_in[6], (const u16*)d_in[11], (const u16*)d_in[12], (const u16*)d_in[13]);
  kprobe<<<1, 64, 0, stream>>>((char*)d_ws);
}
```

```cpp
#define DBG_STAMP
#define PB 256
#define PT 1024
#define PL 160000
#define PV 128
#define PM 0
#include <hip/hip_runtime.h>

typedef unsigned short u16;
typedef unsigned u32;
typedef unsigned long long u64;
typedef u32 v4u __attribute__((ext_vector_type(4)));
typedef u32 v2u __attribute__((ext_vector_type(2)));
typedef float v4f __attribute__((ext_vector_type(4)));
typedef float v2f __attribute__((ext_vector_type(2)));
typedef __bf16 v8bf __attribute__((ext_vector_type(8)));
typedef __bf16 v2bf __attribute__((ext_vector_type(2)));
typedef short v4s __attribute__((ext_vector_type(4)));
#define LDS __attribute__((address_space(3)))
#define GLB __attribute__((address_space(1)))
#define CST __attribute__((address_space(4)))
#define DEVI __device__ __forceinline__
#define SB() __builtin_amdgcn_sched_barrier(0)
#define MEMB() asm volatile("" ::: "memory")

#define WS_R 4096
#define WS_S 8192
#define WS_P 32768
#define WS_X (1u << 20)
#define WS_END (3u << 20)
#define SPIN 4000
#define PF_LATE 0
#define CODE_SZ 13824
#define D_A 26
#define D_W 2
#define D_K 4
#define D_Q 30

#define WS_STAMP (16u << 20)
#define GROUP(b, w) ((w) >= 12 ? 2 : (w) >= 8 ? 1 : ((w) >= 6 && ((b) & 8) && ((b) >> 4) < 14) ? 3 : 0)
#define ST_(i) "s_memrealtime s[84+2*" #i ":85+2*" #i "]\n\t"
#define STAMP(i) asm volatile(ST_(i) ::: "memory", "s99")
#define STAMPD(i, v) asm volatile(ST_(i) :: "v"(v) : "memory", "s99")
#define FL_(i) "v_mov_b32 v8, s[84+4*" #i "]\n\tv_mov_b32 v9, s[85+4*" #i "]\n\tv_mov_b32 v10, s[86+4*" #i "]\n\tv_mov_b32 v11, s[87+4*" #i "]\n\t" \
               "global_store_dwordx4 v[6:7], v[8:11], off offset:16*" #i "\n\ts_nop 1\n\t"
#define FLUSH_ "s_mov_b64 exec, 1\n\ts_waitcnt lgkmcnt(0)\n\t" FL_(0) FL_(1) FL_(2) FL_(3)
#define STADDR "{v[6:7]}"((u64*)(ws + WS_STAMP) + (blockIdx.x * 16 + w) * 8)
#define FLUSH() asm volatile(FLUSH_ "s_endpgm" :: STADDR : "memory", "v8", "v9", "v10", "v11")

DEVI u32 pk(float a, float b) { return __builtin_bit_cast(u32, __builtin_convertvector((v2f){a, b}, v2bf)); }
DEVI float rbf(float a) { return __uint_as_float(pk(0.f, a)); }
DEVI float lo2f(u32 v) { return __uint_as_float(v << 16); }
DEVI float hi2f(u32 v) { return __uint_as_float(v & 0xffff0000u); }
DEVI float dot2(u32 a, u32 b, float c) { return __builtin_amdgcn_fdot2_f32_bf16(__builtin_bit_cast(v2bf, a), __builtin_bit_cast(v2bf, b), c, false); }
template <int CTRL> DEVI float dppf(float v) { return __int_as_float(__builtin_amdgcn_update_dpp(0, __float_as_int(v), CTRL, 0xf, 0xf, true)); }
template <int CTRL, int RM> DEVI u32 dppu(u32 v) { return (u32)__builtin_amdgcn_update_dpp(0, (int)v, CTRL, RM, 0xf, false); }
DEVI float rdl(float v, int l) { return __int_as_float(__builtin_amdgcn_readlane(__float_as_int(v), l)); }
DEVI float fold4(float a, float b, float c, float d) {
  v2u p = __builtin_amdgcn_permlane32_swap(__float_as_uint(a), __float_as_uint(b), false, false);
  v2u q = __builtin_amdgcn_permlane32_swap(__float_as_uint(c), __float_as_uint(d), false, false);
  float t = __uint_as_float(p[0]) + __uint_as_float(p[1]), s = __uint_as_float(q[0]) + __uint_as_float(q[1]);
  v2u r = __builtin_amdgcn_permlane16_swap(__float_as_uint(t), __float_as_uint(s), false, false);
  return __uint_as_float(r[0]) + __uint_as_float(r[1]);
}
DEVI u32 wave_max(u32 v) {
  v = max(v, dppu<0xB1, 0xf>(v)); v = max(v, dppu<0x4E, 0xf>(v)); v = max(v, dppu<0x141, 0xf>(v)); v = max(v, dppu<0x140, 0xf>(v));
  v = max(v, dppu<0x142, 0xa>(v)); v = max(v, dppu<0x143, 0xc>(v));
  return (u32)__builtin_amdgcn_readlane((int)v, 63);
}
DEVI v4s trd(LDS char* p) { return __builtin_amdgcn_ds_read_tr16_b64_v4i16((LDS v4s*)p); }
DEVI v8bf cat(v4s a, v4s b) { return __builtin_bit_cast(v8bf, __builtin_shufflevector(a, b, 0, 1, 2, 3, 4, 5, 6, 7)); }
template <int SL> DEVI void lds_wait(LDS char* p, u32 m) {
  u32 v, s, c;
  asm volatile("s_mov_b32 %2, 0x20000\n"
               "1:\n\tds_read_b32 %0, %3\n\ts_waitcnt lgkmcnt(0)\n\tv_readfirstlane_b32 %1, %0\n\ts_andn2_b32 %1, %4, %1\n\t"
               "s_cbranch_scc0 2f\n\ts_sleep %5\n\ts_sub_u32 %2, %2, 1\n\ts_cbranch_scc0 1b\n2:"
               : "=&v"(v), "=&s"(s), "=&s"(c) : "v"(p), "s"(m), "n"(SL) : "memory", "scc");
}
DEVI void lds_sig(LDS char* p, u32 bit) {
  asm volatile("s_mov_b64 exec, 1\n\tds_or_b32 %0, %1\n\ts_mov_b64 exec, -1" :: "v"(p), "v"(bit) : "memory");
}
DEVI void merge2(u32& a0, u32& a1, u32 b0, u32 b1) { u32 m = min(a0, b0); a0 = max(a0, b0); a1 = max(m, max(a1, b1)); }
DEVI v2f cpow(float er, float ph) {
  float e = __builtin_amdgcn_exp2f(er), f = __builtin_amdgcn_fractf(ph);
  return (v2f){__builtin_amdgcn_cosf(f), __builtin_amdgcn_sinf(f)} * e;
}

#define RP 272
#define BC 17408
#define O_A 69632
#define AC 11776
#define AH 5888
#define A_HI 3584
#define O_U (O_A + 4 * AC)
#define UP 4640
#define O_F (O_U + 4 * UP)
#define O_LN (O_F + 768)
#define O_W (O_LN + 8704)
#define F_KS 0
#define F_U 64
#define F_PH 68
#define F_EX 72
#define F_SP 76
#define F_TS 80
#define F_TAG 88
#define F_X 92
#define F_SV 128
#define F_E 256
#define LDS_SZ (O_W + 16384 + 256)
#define AOFF 49632

#define CM1(t, x, m) "v_pk_mul_f32 " t ", " x ", " m " op_sel_hi:[0,1]\n\t"
#define CM2(r, t, x, m) "v_pk_fma_f32 " r ", " x ", " m ", " t " op_sel:[1,1,0] op_sel_hi:[1,0,1] neg_lo:[0,1,0]\n\t"
DEVI v2f cmul(v2f x, v2f m) {
  v2f t, r;
  asm(CM1("%1", "%2", "%3") CM2("%0", "%1", "%2", "%3") : "=v"(r), "=&v"(t) : "v"(x), "v"(m));
  return r;
}
template <int O1, int O2> DEVI void step2(v2f& r1, v2f x1, v2f m1, v2f& r2, v2f x2, v2f m2) {
  v2f t1, t2;
  asm volatile(CM1("%2", "%4", "%5") CM1("%3", "%6", "%7") CM2("%0", "%2", "%4", "%5") CM2("%1", "%3", "%6", "%7")
               : "=&v"(r1), "=v"(r2), "=&v"(t1), "=&v"(t2) : "v"(x1), "v"(m1), "v"(x2), "v"(m2));
  u32 p1 = pk(r1.x, r1.y), p2 = pk(r2.x, r2.y);
  asm volatile("ds_write_addtid_b32 %0 offset:%2\n\tds_write_addtid_b32 %1 offset:%3" :: "v"(p1), "v"(p2), "n"(O1), "n"(O2) : "memory");
}
template <int O1> DEVI void put1(u32 p) { asm volatile("ds_write_addtid_b32 %0 offset:%1" :: "v"(p), "n"(O1) : "memory"); }
template <int J> DEVI void brows(v2f& e, v2f& o, v2f m) {
  if constexpr (J < 16) { step2<J * RP, (15 + J) * RP>(e, e, m, o, o, m); brows<J + 1>(e, o, m); }
}
#define MF(a, b, c) __builtin_amdgcn_mfma_f32_16x16x32_bf16(a, b, c, 0, 0, 0)
#define BF8(x) __builtin_bit_cast(v8bf, x)
template <int LO, int HI> DEVI v4u band(int e, u32 one) {
  const u32 m = (u32)(((((u64)1 << (HI - LO + 1)) - 1) << (e - HI + 24)) >> 24);
  v4u r;
#pragma unroll
  for (int j = 0; j < 4; j++) { u32 y = m >> (2 * j); r[j] = ((y & 1) | ((y & 2) << 15)) * one; }
  return r;
}
DEVI v8bf wop(v8bf a, v8bf b, v4u ba, v4u bb) {
  const v4f zz = {0.f, 0.f, 0.f, 0.f};
  v4f p = MF(a, BF8(ba), zz), q = MF(b, BF8(bb), zz);
  return BF8(((v4u){pk(p[0], q[0]), pk(p[1], q[1]), pk(p[2], q[2]), pk(p[3], q[3])}));
}
#define TB(a, b, s) "ds_read2_b32 %" #a ", %[p] offset0:16*" #s "+3 offset1:16*" #s "+2\n\tds_read2_b32 %" #b ", %[p] offset0:16*" #s "+1 offset1:16*" #s "\n\t"
DEVI v8bf cat2(v2u a, v2u b) { return BF8(__builtin_shufflevector(a, b, 0, 1, 2, 3)); }

#define VR(b, j) "v[" #b "+4*" #j ":" #b "+3+4*" #j "]"
#define LA(b, j, o) "ds_read_b64_tr_b16 v[" #b "+4*" #j ":" #b "+1+4*" #j "], v0 offset:" #o "+456-64*" #j "\n\t" \
                    "ds_read_b64_tr_b16 v[" #b "+2+4*" #j ":" #b "+3+4*" #j "], v0 offset:" #o "+448-64*" #j "\n\t"
#define LA8(b, o) LA(b, 0, o) LA(b, 1, o) LA(b, 2, o) LA(b, 3, o) LA(b, 4, o) LA(b, 5, o) LA(b, 6, o) LA(b, 7, o)
#define LB(j) "ds_read_b128 " VR(76, j) ", v1 offset:1024+64*" #j "\n\t"
#define LB8 LB(0) LB(1) LB(2) LB(3) LB(4) LB(5) LB(6) LB(7)
#define AX "v[112:115]"
#define AY "v[116:119]"
#define MA(d, a, j, c) "v_mfma_f32_16x16x32_bf16 " d ", " VR(a, j) ", " VR(76, j) ", " c "\n\t"
#define H0(j, d) "s_waitcnt lgkmcnt(7-" #j ")\n\t" MA(d, 8, j, AX)
#define H1(j, c) "s_waitcnt lgkmcnt(8)\n\t" MA(AX, 40, j, c) LB(j)
#define PUB "ds_write_b128 v2, " AY "\n\ts_mov_b64 exec, 1\n\tds_or_b32 v3, v4 offset:4\n\ts_mov_b64 exec, -1\n\t"
#define WG_ "global_load_lds_dwordx4 %[go], %[wg]\n\ts_add_u32 m0, m0, 0x400\n\tv_add_u32 %[go], 0x1c000, %[go]\n\t"
#define WU(j) "global_load_dwordx4 %" #j ", %[gu], %[wu]\n\tv_add_u32 %[gu], 0x1c000, %[gu]\n\t"
#define WD(j, o) "global_load_dwordx4 %" #j ", %[dd], %[wd] offset:" #o "\n\t"

__global__ void __launch_bounds__(1024, 4) kf(const u16* __restrict__ x, const u16* __restrict__ wr, char* __restrict__ ws,
    const u16* __restrict__ dskip, u16* __restrict__ out, const u16* __restrict__ logdt,
    const u16* __restrict__ Are, const u16* __restrict__ Aim, const u16* __restrict__ Bre, const u16* __restrict__ Bim,
    const u16* __restrict__ Cre, const u16* __restrict__ Cim,
    const u16* __restrict__ wg, const u16* __restrict__ wu, const u16* __restrict__ wd) {
  __shared__ __attribute__((aligned(16))) char smem[LDS_SZ];
  LDS char* L = (LDS char*)smem;
  LDS char* F = L + O_F;
  const int lane = threadIdx.x & 63, w = __builtin_amdgcn_readfirstlane(threadIdx.x >> 6);
  u64 pc;
  asm volatile("ds_write_b32 %1, %2\n\ts_getpc_b64 %0" : "=s"(pc) : "v"(O_U + min(w >> 1, 4) * UP + (threadIdx.x & 127) * 4), "v"(0) : "memory");
  asm volatile("s_mov_b64 s[84:85], 0\n\ts_mov_b64 s[86:87], 0\n\ts_mov_b64 s[88:89], 0\n\ts_mov_b64 s[90:91], 0\n\ts_mov_b64 s[92:93], 0\n\t"
               "s_mov_b64 s[94:95], 0\n\ts_mov_b64 s[96:97], 0\n\ts_mov_b64 s[98:99], 0" ::: "s99");
  const int b = blockIdx.x, sw = (b & 7) * 32 + (b >> 3), cg = sw >> 1, h = sw & 1;
  const int c16 = lane & 15, kg = lane >> 4;
  const int t = sw * 8 + w, ch = w & 3, half = (w >> 2) & 1;
  v4u xr, wv;
  u32 tag0; v2u dsk; char* wsb;
  {
    const int d = cg * 4 + ch;
    asm volatile("s_waitcnt lgkmcnt(0)\n\ts_cmp_lt_u32 %[w], 8\n\ts_cbranch_scc0 1f\n\t"
                 "global_load_dwordx4 %0, %[xo], %[xp]\n\tglobal_load_dwordx4 %1, %[lo], %[wr]\n\t"
                 "s_load_dword %2, %[ws], 0\n\ts_load_dwordx2 %3, %[dk], %[co]\n\ts_setprio 3\n\ts_branch 2f\n"
                 "1:\n\tglobal_load_ushort v12, %[ix], %[p0]\n\tglobal_load_ushort v13, %[ix], %[p1]\n\tglobal_load_ushort v14, %[ix], %[p2]\n\t"
                 "global_load_ushort v15, %[ix], %[p3]\n\tglobal_load_ushort v16, %[ix], %[p4]\n\tglobal_load_ushort v17, %[ix], %[p5]\n\t"
                 "s_load_dwordx2 %3, %[ld], %[co]\n\ts_setprio 1\n2:\n\ts_mov_b64 %4, %[ws]"
                 : "=&{v[12:15]}"(xr), "=&{v[16:19]}"(wv), "=&s"(tag0), "=&{s[40:41]}"(dsk), "=&s"(wsb)
                 : [w] "s"(w), [lo] "v"(lane * 16), [xo] "v"(t * 1024 + lane * 16), [xp] "s"(x), [wr] "s"(wr), [ws] "s"(ws), [dk] "s"(dskip), [co] "s"(cg * 8),
                   [ix] "v"((d * 128 + half * 64 + lane) * 2), [p0] "s"(Are), [p1] "s"(Aim), [p2] "s"(Bre), [p3] "s"(Bim), [p4] "s"(Cre), [p5] "s"(Cim),
                   [ld] "s"(logdt), "s"(out), "s"(wg), "s"(wu), "s"(wd)
                 : "scc");
  }
  STAMP(0);
  const __amdgpu_buffer_rsrc_t rs = __builtin_amdgcn_make_buffer_rsrc(wsb, 0, WS_END, 0x00020000);
  if (__builtin_expect(w < 8, 1)) {
    LDS char* a_xn = L + O_LN + w * 1088 + lane * 16;
    LDS char* a_ks = F + F_KS + w * 8;
    LDS char* a_f = F;
    LDS char* a_kr = F + F_KS + (lane & 7) * 8;
    LDS char* a_tr = L + O_LN + ((lane >> 2) & 3) * 1088 + w * 128 + kg * 32 + (lane & 3) * 8;
    u32 o_pub = (lane >> 3) * 32768 + (lane & 7) * 16, rsh = (lane & 3) * 4 + 1, o_r = WS_R + sw * 16, o_s = lane * 8;
    u32 so_pub = WS_X + w * 8 * 32768 + sw * 128, so_s = WS_S + sw * 64, kix = 0x80000000u | (u32)(2047 - t);
    const u32 fq = ((b >> 4) + ((~b & 8) << 1)) | ((w - 6) & 0x100);
    const bool ffn = fq < 14;
    const int fs = (b & 7) * 14 + (b >> 4), fw = w & 1;
    asm volatile("s_barrier\n\ts_waitcnt vmcnt(0)"
                 : "+v"(xr), "+v"(wv), "+v"(a_xn), "+v"(a_ks), "+v"(a_f), "+v"(a_kr), "+v"(a_tr), "+v"(o_pub), "+v"(rsh), "+v"(o_r), "+v"(o_s),
                   "+s"(so_pub), "+s"(so_s), "+s"(kix) :: "memory");
    STAMP(1);
    float s1 = 0.f, s2 = 0.f, sr = 0.f, sw_ = 0.f;
#pragma unroll
    for (int j = 0; j < 4; j++) { s1 = dot2(xr[j], 0x3f803f80u, s1); s2 = dot2(xr[j], xr[j], s2); sr = dot2(xr[j], wv[j], sr); sw_ = dot2(wv[j], 0x3f803f80u, sw_); }
    float f4 = fold4(s1, s2, sr, sw_);
    f4 += dppf<0xB1>(f4); f4 += dppf<0x4E>(f4); f4 += dppf<0x141>(f4); f4 += dppf<0x140>(f4);
    f4 *= 1.f / 512.f;
    const float dm = rdl(f4, 0), mb = rbf(dm);
    const float sd = rbf(__builtin_amdgcn_sqrtf(rbf(rbf(fmaxf(rdl(f4, 32) - dm * dm, 0.f)) + 1e-5f)));
    const float r16 = __builtin_amdgcn_rcpf(sd * 16.f), off = -mb * r16;
    v4u xn;
#pragma unroll
    for (int j = 0; j < 4; j++) { v2f y = __builtin_elementwise_fma((v2f){lo2f(xr[j]), hi2f(xr[j])}, (v2f){r16, r16}, (v2f){off, off}); xn[j] = pk(y.x, y.y); }
    asm volatile("s_waitcnt lgkmcnt(0)" : "+s"(tag0), "+s"(dsk), "+v"(xn) :: "memory");
    const u32 tag = tag0 + 0x11111111u;
    *(LDS v4u*)a_xn = xn;
    const u32 rb = pk(0.f, (rdl(f4, 16) - mb * rdl(f4, 48)) * 8192.f * r16);
    const u32 key = rb ^ (((int)rb >> 31) & 0x7fff0000u) ^ kix;
    asm volatile("s_mov_b64 exec, 1\n\tds_write_b32 %0, %2 offset:%4\n\tds_write_b64 %1, %3\n\ts_mov_b64 exec, -1"
                 :: "v"(a_f), "v"(a_ks), "v"(tag), "v"((v2u){key, (__float_as_uint(mb) >> 16) | __float_as_uint(sd)}), "n"(F_TAG) : "memory");
    u32 pt[4];
    pt[0] = __builtin_amdgcn_alignbit(tag, tag, rsh) ^ 0x3C5A96C3u;
    pt[1] = pt[0] << 1; pt[2] = pt[0] << 2; pt[3] = pt[0] << 3;
    asm volatile("" : "+v"(pt[0]), "+v"(pt[1]), "+v"(pt[2]), "+v"(pt[3]));
    {
      v2u ks, a0, a1; u32 cnt;
      asm volatile("s_mov_b32 %3, 0x20000\n"
                   "1:\n\tds_read_b64 %0, %4\n\tds_read_b64_tr_b16 %1, %5\n\tds_read_b64_tr_b16 %2, %5 offset:4352\n\ts_waitcnt lgkmcnt(2)\n\t"
                   "v_cmp_gt_u64 vcc, %6, %0\n\ts_cbranch_vccz 2f\n\ts_sub_u32 %3, %3, 1\n\ts_cbranch_scc0 1b\n2:\n\ts_waitcnt lgkmcnt(0)"
                   : "=&v"(ks), "=&v"(a0), "=&v"(a1), "=&s"(cnt)
                   : "v"(a_kr), "v"(a_tr), "s"(1ull << 32)
                   : "memory", "scc", "vcc");
      if (w == 0) {
        u32 o = dppu<0xB1, 0xf>(ks[0]), k0 = max(ks[0], o), k1 = min(ks[0], o);
        merge2(k0, k1, dppu<0x4E, 0xf>(k0), dppu<0x4E, 0xf>(k1));
        merge2(k0, k1, dppu<0x141, 0xf>(k0), dppu<0x141, 0xf>(k1));
        if (lane == 0) __builtin_amdgcn_raw_buffer_store_b128((v4u){k0, k1, tag, tag}, rs, o_r, 0, 16);
      }
      v4u d = {a0[0], a0[1], a1[0], a1[1]};
#pragma unroll
      for (int j = 0; j < 4; j++) d[j] = (d[j] & ~0x40004000u) | (pt[j] & 0x40004000u);
      __builtin_amdgcn_raw_buffer_store_b128(d, rs, o_pub, so_pub, 16);
      if (w == 1 && lane < 8) __builtin_amdgcn_raw_buffer_store_b64((v2u){ks[1], tag}, rs, o_s, so_s, 16);
    }
    STAMP(2);
    const int si = w >> 1, s = h ? 2 + si : (si < 2 ? si : si + 4), t0 = s * 256 + (w & 1) * 128 + lane * 2;
    v8bf fg[8], fu[8];
    v4f sg = {0.f, 0.f, 0.f, 0.f}, su = sg;
    LDS char* const wl = L + O_W + fw * 8192;
    if (ffn) {
      v4u wq[16];
      const int rho = lane >> 1, kk = 8 * ((rho >> 2) & 3) + 4 * (rho >> 4) + (rho & 3);
      u32 go = (fw * 256 + kk) * 3584 + fs * 32 + (lane & 1) * 16, gu = go;
      asm volatile("s_mov_b32 m0, %[m]\n\ts_sleep %[sl]\n\t"
                   WG_ WG_ WG_ WG_ WG_ WG_ WG_ WG_ WU(0) WU(1) WU(2) WU(3) WU(4) WU(5) WU(6) WU(7)
                   WD(8, 0) WD(9, 64) WD(10, 128) WD(11, 192) WD(12, 256) WD(13, 320) WD(14, 384) WD(15, 448)
                   "s_waitcnt vmcnt(16)"
                   : "=&v"(wq[0]), "=&v"(wq[1]), "=&v"(wq[2]), "=&v"(wq[3]), "=&v"(wq[4]), "=&v"(wq[5]), "=&v"(wq[6]), "=&v"(wq[7]),
                     "=&v"(wq[8]), "=&v"(wq[9]), "=&v"(wq[10]), "=&v"(wq[11]), "=&v"(wq[12]), "=&v"(wq[13]), "=&v"(wq[14]), "=&v"(wq[15]),
                     [go] "+v"(go), [gu] "+v"(gu)
                   : [m] "s"(O_W + fw * 8192), [sl] "n"(D_W), [wg] "s"(wg), [wu] "s"(wu), [wd] "s"(wd),
                     [dd] "v"((fs * 16 + (lane >> 2)) * 1024 + fw * 512 + (lane & 3) * 16)
                   : "memory", "scc");
#pragma unroll
      for (int ks = 0; ks < 8; ks++) fg[ks] = cat(trd(wl + lane * 8 + ks * 1024), trd(wl + lane * 8 + ks * 1024 + 512));
      asm volatile("s_waitcnt vmcnt(8)"
                   : "+v"(wq[0]), "+v"(wq[1]), "+v"(wq[2]), "+v"(wq[3]), "+v"(wq[4]), "+v"(wq[5]), "+v"(wq[6]), "+v"(wq[7]) :: "memory");
#pragma unroll
      for (int ks = 0; ks < 8; ks++) *(LDS v4u*)(wl + ks * 1024 + lane * 16) = wq[ks];
      MEMB();
#pragma unroll
      for (int ks = 0; ks < 8; ks++) fu[ks] = cat(trd(wl + lane * 8 + ks * 1024), trd(wl + lane * 8 + ks * 1024 + 512));
      asm volatile("s_waitcnt vmcnt(0)"
                   : "+v"(wq[8]), "+v"(wq[9]), "+v"(wq[10]), "+v"(wq[11]), "+v"(wq[12]), "+v"(wq[13]), "+v"(wq[14]), "+v"(wq[15]) :: "memory");
#pragma unroll
      for (int ks = 0; ks < 8; ks++) *(LDS v4u*)(wl + ks * 1024 + lane * 16) = wq[8 + ks];
      MEMB();
      const v8bf ones = BF8(((v4u){0x3f803f80u, 0x3f803f80u, 0x3f803f80u, 0x3f803f80u}));
#pragma unroll
      for (int ks = 0; ks < 8; ks++) { sg = MF(fg[ks], ones, sg); su = MF(fu[ks], ones, su); }
      asm volatile("" : "+v"(sg), "+v"(su));
      STAMP(3);
      __builtin_amdgcn_s_sleep(D_K);
    } else {
      __builtin_amdgcn_s_sleep(D_A);
      if (w < 6 || h == 0) {
        const u32 o = (lane >> 2) * 128 + (lane & 3) * 16, so = WS_X + (cg >> 1) * 32768 + w * 4096 + (cg & 1) * 64;
        v4u a, c;
        int i = SPIN;
        bool bad;
        _Pragma("unroll 1") do {
          a = __builtin_amdgcn_raw_buffer_load_b128(rs, o, so, 16);
          c = __builtin_amdgcn_raw_buffer_load_b128(rs, o + 2048, so, 16);
#pragma unroll
          for (int j = 0; j < 4; j++) { a[j] ^= pt[j] & 0x40004000u; c[j] ^= pt[j] & 0x40004000u; }
          u32 e = (a[0] | a[1] | a[2]) | (a[3] | c[0] | c[1]) | (c[2] | c[3]);
          bad = __any(e & 0x40004000u);
          MEMB();
        } while (bad && --i);
        LDS char* q = L + O_U + (lane & 3) * UP + 512 + (w * 32 + (lane >> 2)) * 16;
        *(LDS v4u*)q = a;
        *(LDS v4u*)(q + 256) = c;
        lds_sig(F + F_U, 1u << w);
      }
      STAMP(3);
    }
    if (w == 7 || ffn) {
      v4u k[4];
      {
        int i = SPIN;
        bool bad;
        _Pragma("unroll 1") do {
#pragma unroll
          for (int j = 0; j < 4; j++) k[j] = __builtin_amdgcn_raw_buffer_load_b128(rs, j * 1024 + lane * 16, WS_R, 16);
          bad = __any(((k[0][2] ^ tag) | (k[1][2] ^ tag) | (k[2][2] ^ tag) | (k[3][2] ^ tag)) != 0);
          MEMB();
        } while (bad && --i);
      }
      u32 k0 = k[0][0], k1 = k[0][1], k2 = k[2][0], k3 = k[2][1];
      merge2(k0, k1, k[1][0], k[1][1]); merge2(k2, k3, k[3][0], k[3][1]); merge2(k0, k1, k2, k3);
      const u32 s0 = wave_max(k0), s1 = wave_max(k0 == s0 ? k1 : k0);
      const int tk = 2047 - (((lane & 1) ? s1 : s0) & 2047);
      v4u xf[8]; v2u st;
      if (ffn) {
#pragma unroll
        for (int ks = 0; ks < 8; ks++) xf[ks] = *(const GLB v4u*)((const GLB char*)x + fw * 512 + ks * 64 + (u32)(tk * 1024 + kg * 16));
        st = __builtin_amdgcn_raw_buffer_load_b64(rs, tk * 8, WS_S, 16);
      }
      STAMP(4);
      asm volatile("s_mov_b64 exec, 1\n\tds_write_b64 %0, %1 offset:%3\n\tds_or_b32 %0, %2 offset:%4\n\ts_mov_b64 exec, -1"
                   :: "v"(F), "v"((v2u){s0, s1}), "v"(256u), "n"(F_TS), "n"(F_SP) : "memory");
      if (ffn) {
        v4f g = {0.f, 0.f, 0.f, 0.f}, u = g;
#pragma unroll
        for (int ks = 0; ks < 8; ks++) { g = MF(fg[ks], BF8(xf[ks]), g); u = MF(fu[ks], BF8(xf[ks]), u); }
        SB();
        v8bf wdf[8];
        LDS char* dl = wl + (lane >> 2) * 64 + (lane & 3) * 8;
#pragma unroll
        for (int j = 0; j < 8; j++) wdf[j] = cat(trd(dl + j * 1024), trd(dl + j * 1024 + 32));
        v2u hz[8];
#pragma unroll
        for (int j = 0; j < 8; j++) hz[j] = (v2u){0, 0};
        asm volatile("" : "+v"(hz[0]), "+v"(hz[1]), "+v"(hz[2]), "+v"(hz[3]), "+v"(hz[4]), "+v"(hz[5]), "+v"(hz[6]), "+v"(hz[7]));
        _Pragma("unroll 1") for (int i = 0; i < SPIN; i++) {
          if (!__any(st[1] != tag)) break;
          MEMB();
          st = __builtin_amdgcn_raw_buffer_load_b64(rs, tk * 8, WS_S, 16);
        }
        const float mm = lo2f(st[0]), rsd = __builtin_amdgcn_rcpf(hi2f(st[0]));
        g -= sg * mm; u -= su * mm;
        LDS char* ex = F + F_E + (kg * 2 + (lane & 1)) * 32;
        if (c16 < 2) { *(LDS v4f*)(ex + fw * 256) = g; *(LDS v4f*)(ex + fw * 256 + 16) = u; }
        lds_sig(F + F_EX, 1u << fw);
        STAMP(5);
        v4f eg, eu;
        {
          u32 f, sc, cnt;
          asm volatile("s_mov_b32 %4, 0x20000\n"
                       "1:\n\tds_read_b32 %2, %5\n\tds_read_b128 %0, %6\n\tds_read_b128 %1, %6 offset:16\n\t"
                       "s_waitcnt lgkmcnt(2)\n\tv_readfirstlane_b32 %3, %2\n\ts_cmp_eq_u32 %3, 3\n\t"
                       "s_cbranch_scc1 2f\n\ts_sub_u32 %4, %4, 1\n\ts_cbranch_scc0 1b\n2:\n\ts_waitcnt lgkmcnt(0)"
                       : "=&v"(eg), "=&v"(eu), "=&v"(f), "=&s"(sc), "=&s"(cnt) : "v"(F + F_EX), "v"(ex + (fw ^ 1) * 256) : "memory", "scc");
        }
        g = (g + eg) * rsd; u = (u + eu) * rsd;
        float hh[4];
#pragma unroll
        for (int i = 0; i < 4; i++) hh[i] = g[i] * __builtin_amdgcn_rcpf(1.f + __builtin_amdgcn_exp2f(g[i] * -1.44269504089f)) * u[i];
        const v2u hf = {pk(hh[0], hh[1]), pk(hh[2], hh[3])};
#define HM(j) "v_mov_b64 %" #j ", %8\n\ts_lshl_b64 exec, exec, 2\n\t"
        asm volatile("s_mov_b64 exec, %9\n\t" HM(0) HM(1) HM(2) HM(3) HM(4) HM(5) HM(6) "v_mov_b64 %7, %8\n\ts_mov_b64 exec, -1\n\ts_nop 0"
                     : "+v"(hz[0]), "+v"(hz[1]), "+v"(hz[2]), "+v"(hz[3]), "+v"(hz[4]), "+v"(hz[5]), "+v"(hz[6]), "+v"(hz[7])
                     : "v"(hf), "s"(0x0003000300030003ull) : "scc");
        v4f e0 = {0.f, 0.f, 0.f, 0.f}, e1 = e0;
#pragma unroll
        for (int j = 0; j < 4; j++) {
          v8bf hb = BF8(((v4u){hz[2 * j][0], hz[2 * j][1], hz[2 * j + 1][0], hz[2 * j + 1][1]}));
          e0 = MF(wdf[j], hb, e0); e1 = MF(wdf[4 + j], hb, e1);
        }
        const u32 po = (c16 >> 1) * 128 + kg * 32 + (lane & 1) * 16;
        __builtin_amdgcn_raw_buffer_store_b128((v4u){pk(e0[0], e0[1]), pk(e0[2], e0[3]), tag, tag}, rs, po, WS_P + fs * 4096 + fw * 2048, 16);
        __builtin_amdgcn_raw_buffer_store_b128((v4u){pk(e1[0], e1[1]), pk(e1[2], e1[3]), tag, tag}, rs, po + 1024, WS_P + fs * 4096 + fw * 2048, 16);
        STAMP(6);
      } else if (b == 0 && lane == 0) *(GLB u32*)ws = tag;
    }
    __builtin_amdgcn_s_setprio(0);
    v4u sv;
    {
      int i = SPIN;
      bool bad;
      _Pragma("unroll 1") do {
        sv = __builtin_amdgcn_raw_buffer_load_b128(rs, WS_S + t0 * 8, 0, 16);
        bad = __any(((sv[1] ^ tag) | (sv[3] ^ tag)) != 0);
        MEMB();
      } while (bad && --i);
    }
    lds_wait<1>(F + F_U, 1u << s);
    v2f base[4];
    {
      const v2f m2 = {lo2f(sv[0]), lo2f(sv[2])}, sd2 = (v2f){hi2f(sv[0]), hi2f(sv[2])} * 16.f;
#pragma unroll
      for (int c = 0; c < 4; c++) {
        u32 uu = *(LDS u32*)(L + O_U + c * UP + 512 + t0 * 2), dd = dsk[c >> 1];
        float dk = ((c & 1) ? hi2f(dd) : lo2f(dd)) * 16.f + 16.f;
        base[c] = __builtin_elementwise_fma((v2f){lo2f(uu), hi2f(uu)}, sd2 + dk, m2);
      }
    }
    lds_wait<1>(F + F_SP, 256);
    const v2u ts = *(LDS v2u*)(F + F_TS);
    const int ts0 = 2047 - (ts[0] & 2047), ts1 = 2047 - (ts[1] & 2047);
    const int n = (w & 1) * 8 + (lane >> 3);
    u64 m0 = __builtin_amdgcn_ballot_w64(t0 != ts0 && t0 != ts1), m1 = __builtin_amdgcn_ballot_w64(t0 + 1 != ts0 && t0 + 1 != ts1);
    GLB char* op = (GLB char*)out + cg * 8;
    u32 oo = t0 * 1024;
    asm volatile("" : "+v"(oo), "+s"(op), "+s"(m0), "+s"(m1), "+v"(base[0]), "+v"(base[1]), "+v"(base[2]), "+v"(base[3]));
    if (!ffn) STAMP(5);
    v2f pr[8];
    {
      u32 f, sc, cnt;
      asm volatile("s_mov_b32 %10, 0x20000\n"
                   "1:\n\tds_read_b32 %8, %11\n\ts_waitcnt lgkmcnt(0)\n\tv_readfirstlane_b32 %9, %8\n\ts_and_b32 %9, %13, %9\n\t"
                   "s_cmp_gt_u32 %9, %14\n\ts_cbranch_scc1 3f\n\ts_sleep 1\n\ts_sub_u32 %10, %10, 1\n\ts_cbranch_scc0 1b\n"
                   "3:\n\tds_read_b32 %8, %11\n\tds_read_b64 %0, %12\n\tds_read_b64 %1, %12 offset:%15\n\tds_read_b64 %2, %12 offset:%15*2\n\t"
                   "ds_read_b64 %3, %12 offset:%15*3\n\tds_read_b64 %4, %12 offset:4096\n\tds_read_b64 %5, %12 offset:%15+4096\n\t"
                   "ds_read_b64 %6, %12 offset:%15*2+4096\n\tds_read_b64 %7, %12 offset:%15*3+4096\n\t"
                   "s_waitcnt lgkmcnt(8)\n\tv_readfirstlane_b32 %9, %8\n\ts_andn2_b32 %9, %13, %9\n\t"
                   "s_cbranch_scc0 2f\n\ts_waitcnt lgkmcnt(0)\n\ts_sub_u32 %10, %10, 1\n\ts_cbranch_scc0 3b\n2:\n\ts_setprio 3\n\ts_waitcnt lgkmcnt(0)"
                   : "=&v"(pr[0]), "=&v"(pr[1]), "=&v"(pr[2]), "=&v"(pr[3]), "=&v"(pr[4]), "=&v"(pr[5]), "=&v"(pr[6]), "=&v"(pr[7]),
                     "=&v"(f), "=&s"(sc), "=&s"(cnt)
                   : "v"(F + F_PH), "v"(L + si * 1024 + n * 64 + 16 * (((lane >> 1) & 3) ^ ((n >> 1) & 3)) + (lane & 1) * 8),
                     "s"(0xf000fu << (4 * si)), "s"(0xf0000u << (4 * si)), "n"(BC) : "memory", "scc");
    }
    if (!ffn) STAMP(6);
#pragma unroll
    for (int c = 0; c < 4; c++) base[c] += pr[c] + pr[4 + c];
    asm volatile("s_mov_b64 exec, %4\n\tglobal_store_dwordx2 %0, %1, %3\n\ts_mov_b64 exec, %5\n\tglobal_store_dwordx2 %0, %2, %3 offset:1024\n\ts_mov_b64 exec, -1"
                 :: "v"(oo), "v"((v2u){pk(base[0][0], base[1][0]), pk(base[2][0], base[3][0])}),
                    "v"((v2u){pk(base[0][1], base[1][1]), pk(base[2][1], base[3][1])}), "s"(op), "s"(m0), "s"(m1) : "memory");
    if (__builtin_expect((m0 & m1) != ~0ull, 0)) {
#pragma unroll
      for (int i = 0; i < 2; i++) {
        if (t0 + i == ts0 || t0 + i == ts1) {
          *(LDS v4f*)(F + F_SV + (t0 + i == ts0 ? 0 : 16)) = (v4f){base[0][i], base[1][i], base[2][i], base[3][i]};
          asm volatile("ds_or_b32 %0, %1" :: "v"(F + F_SP), "v"(t0 + i == ts0 ? 1u : 2u) : "memory");
        }
      }
    }
    STAMP(7);
    FLUSH();
    asm volatile("s_endpgm" ::: "memory");
  }
  {
    asm volatile("" : "={v[12:15]}"(xr), "={v[16:19]}"(wv), "={s[40:41]}"(dsk));
    SB();
    asm volatile("s_barrier" ::: "memory");
    u64 pfb; u32 pfo;
    {
      const int g = b & 7, i = b >> 3;
      pfb = w == 8 ? (u64)wg : w == 9 ? (u64)wu : w == 10 ? (u64)wd : pc;
      const u32 o = w < 10 ? i * 16 * 3584 + ((448 * g) & ~127) : w == 10 ? (224 * g + 7 * i) * 1024 : (i & 3) * 4096;
      pfo = min(o + (lane >> 2) * (w < 10 ? 3584 : w == 10 ? 512 : 256) + (lane & 3) * (w < 11 ? 128 : 64), w < 10 ? ~0u : w == 10 ? o + 55 * 128 : CODE_SZ);
      asm volatile("s_cmp_ge_u32 %3, %4\n\ts_cbranch_scc1 1f\n\ts_mov_b32 m0, %2\n\ts_nop 0\n\tglobal_load_lds_dword %0, %1\n1:"
                   :: "v"(pfo), "s"(pfb), "n"(LDS_SZ - 256), "s"(PF_LATE ? w - 11 : w - 8), "n"(PF_LATE ? 1 : 4) : "memory", "scc");
    }
    LDS char* const Bc = L + ch * BC;
    LDS char* const Ac = L + O_A + ch * AC;
    LDS char* const Uc = L + O_U + ch * UP;
    const u32 tb = half ? (c16 < 8 ? 0x4f32f10fu : 0xfffff765u) : (c16 < 8 ? 0x76543210u : 0xcbaaa988u);
    const u32 ar = (tb >> ((c16 & 7) * 4)) & 15;
    LDS char* pa = (ar == 15 ? Uc : Ac + half * A_HI + ar * RP + (kg & 1) * AH) + (kg >> 1) * 16;
    LDS char* pb = Bc + c16 * RP + (kg & 1) * (BC / 2) + (kg >> 1) * 16;
    const int be = c16 + 16 - 8 * kg;
    v4u b35 = band<3, 5>(be, 0x3f80), b6a = band<6, 10>(be, 0x3f80), b5b = band<5, 11>(be, 0x3f80), b4c = band<4, 12>(be, 0x3f80), b34 = band<3, 4>(be, 0xffff);
    asm volatile("" : "+v"(pa), "+v"(pb), "+v"(b35), "+v"(b6a), "+v"(b5b), "+v"(b4c), "+v"(b34));
    {
      asm volatile("s_waitcnt lgkmcnt(0)" : "+s"(dsk) :: "memory");
      const u32 ldt = dsk[ch >> 1];
      const float dt = rbf(__builtin_amdgcn_exp2f(((ch & 1) ? hi2f(ldt) : lo2f(ldt)) * 1.44269504089f));
      float d1 = dt * 1.44269504089f, d2 = dt * 0.15915494309f, d16 = dt * 16.f;
      asm volatile("s_cmp_lt_u32 %6, %7\n\ts_cbranch_scc1 1f\n\ts_waitcnt vmcnt(0)\n1:\n\ts_waitcnt vmcnt(1)\n\t"
                   "s_mov_b32 m0, %5"
                   : "+v"(xr), "+v"(wv), "+v"(d1), "+v"(d2), "+v"(d16)
                   : "s"(ch * BC + half * (BC / 2)), "s"(PF_LATE ? w - 11 : w - 8), "n"(PF_LATE ? 1 : 4), "s"(w), "v"(pfo), "s"(pfb), "n"(LDS_SZ - 256) : "memory", "scc");
      STAMP(1);
      const float er = d1 * lo2f(xr[0]), ph = d2 * lo2f(xr[1]);
      const v2f z = cpow(er, ph), E = cpow(er * 256.f, ph * 256.f);
      const v2f z2 = cmul(z, z), cb = cmul((v2f){lo2f(wv[0]), lo2f(wv[1])}, (v2f){lo2f(xr[2]), lo2f(xr[3])} * d16);
      const v2f z4 = cmul(z2, z2), z8 = cmul(z4, z4);
      v2f o = {z8.x, -z8.y};
      const v2f m = cmul(o, o);
      v2f e = m;
      put1<0>(0x3f80u); put1<16 * RP>(pk(o.x, o.y)); put1<RP>(pk(e.x, e.y));
      brows<2>(e, o, m);
      v2f x1, y, c0, c1;
      step2<31 * RP, 31 * RP>(x1, cb, E, o, o, m);
      asm volatile("s_mov_b32 m0, %0\n\ts_nop 0" :: "s"(20000 + ch * AC + half * AH) : "memory");
#define AL(r) (AOFF + (r) * RP)
#define AU(r) (AOFF + A_HI + (r) * RP)
      put1<AL(0)>(pk(cb.x, cb.y)); put1<AL(8)>(pk(x1.x, x1.y));
      step2<AL(1), AL(12)>(c1, cb, z, y, x1, z8);
      step2<AL(2), AU(2)>(c0, cb, z2, y, y, E);
      step2<AL(3), AU(3)>(c1, c1, z2, y, y, E);
      step2<AL(4), AU(4)>(c0, c0, z2, y, y, E);
      step2<AL(5), AU(5)>(c1, c1, z2, y, y, E);
      step2<AL(6), AU(6)>(c0, c0, z2, y, y, E);
      step2<AL(9), AL(10)>(c0, x1, z2, e, x1, z4);
      step2<AL(7), AU(7)>(c1, c1, z2, y, y, E);
      step2<AL(11), AU(0)>(c0, e, z2, e, e, E);
      e = cmul(e, E); put1<AU(1)>(pk(e.x, e.y));
    }
    lds_sig(F + F_X, 1u << (w - 8));
    STAMP(2);
    lds_wait<1>(F + F_X, 17u << ch);
    v4f e0 = {0.f, 0.f, 0.f, 0.f}, e1 = e0;
    {
      v8bf fa[4], f0[4], f1[4];
#pragma unroll
      for (int k = 0; k < 4; k++) { fa[k] = *(LDS v8bf*)(pa + k * 32); f0[k] = *(LDS v8bf*)(pb + k * 32); f1[k] = *(LDS v8bf*)(pb + 16 * RP + k * 32); }
      SB();
#pragma unroll
      for (int k = 0; k < 4; k++) {
        e0 = MF(fa[k], f0[k], e0); e1 = MF(fa[k], f1[k], e1);
        fa[k] = *(LDS v8bf*)(pa + 128 + k * 32); f0[k] = *(LDS v8bf*)(pb + 128 + k * 32); f1[k] = *(LDS v8bf*)(pb + 16 * RP + 128 + k * 32);
        SB();
      }
      lds_sig(F + F_U, 256u << (w - 8));
#pragma unroll
      for (int k = 0; k < 4; k++) { e0 = MF(fa[k], f0[k], e0); e1 = MF(fa[k], f1[k], e1); }
    }
    LDS char* const pw = Bc + half * 4096 + c16 * 64 + 16 * (kg ^ ((c16 >> 1) & 3));
    const u32 umask = (h ? 63 : 255) | 0x1100u << ch;
    if (!half) {
      LDS char* const K0 = Ac + 32;
      LDS char* const cpy = Ac + (kg >> 1) * (AH + 128) + (kg & 1) * 1344;
      const v2u w0 = {pk(e0[0], e0[1]), pk(e0[2], e0[3])}, w1 = {pk(e1[0], e1[1]), pk(e1[2], e1[3])};
      LDS char* q = K0 + (kg >> 1) * 512 + c16 * 32 + (kg & 1) * 8;
      *(LDS v2u*)q = w0;
      *(LDS v2u*)(q + 16) = w1;
      *(LDS u32*)(cpy + (c16 & 7) * 4) = 0;
      MEMB();
      LDS char* p = K0 + lane * 16;
      const v4u un = *(LDS v4u*)p; const v2u pv = *(LDS v2u*)(p - 8);
      {
        const int i0 = 2 * c16 - kg;
        const u32 sl = i0 > 2 ? 0x0c0cu : i0 == -3 ? 0x0302u : i0 == -2 ? 0x0504u : 0x0706u, sh = i0 > 1 ? 0x0c0cu : i0 == -3 ? 0x0504u : 0x0706u;
        *(LDS u32*)(cpy + 1056 + c16 * 4) = __builtin_amdgcn_perm(__builtin_amdgcn_readlane(w1[1], 63), __builtin_amdgcn_readlane(w1[0], 63), sl | sh << 16);
      }
      const u32 a0 = __builtin_amdgcn_alignbit(pv[1], pv[0], 16), a1 = __builtin_amdgcn_alignbit(un[0], pv[1], 16),
                a2 = __builtin_amdgcn_alignbit(un[1], un[0], 16), a3 = __builtin_amdgcn_alignbit(un[2], un[1], 16),
                a4 = __builtin_amdgcn_alignbit(un[3], un[2], 16);
      *(LDS v4u*)(p + 1344) = (v4u){a1, a2, a3, a4};
      *(LDS v4u*)(p + AH + 128) = (v4u){pv[1], un[0], un[1], un[2]};
      *(LDS v4u*)(p + AH + 128 + 1344) = (v4u){a0, a1, a2, a3};
      STAMP(3);
      u32 c0, c1, si = 0;
      asm volatile(
        LA8(40, 512) LA8(8, 0) LA(72, 0, 576)
        "v_mov_b32 v4, 0\n\tv_mov_b64 v[112:113], 0\n\tv_mov_b64 v[114:115], 0\n\ts_setprio 2\n\ts_mov_b32 %1, 0x20000\n" ST_(4)
        "1:\n\tds_read_b32 v5, v3\n\ts_waitcnt lgkmcnt(0)\n\tv_readfirstlane_b32 %0, v5\n\ts_andn2_b32 %0, %[msk], %0\n\t"
        "s_cbranch_scc0 2f\n\ts_sub_u32 %1, %1, 1\n\ts_cbranch_scc0 1b\n2:\n\t" ST_(5)
        LB8
        "s_cmp_eq_u32 %[h], 0\n\ts_cbranch_scc1 4f\n\ts_branch 6f\n"
        "3:\n\ts_waitcnt lgkmcnt(8)\n\tv_mfma_f32_16x16x32_bf16 " AX ", v[72:75], v[108:111], " AX "\n"
        "4:\n\t" H0(0, AX) H0(1, AX) H0(2, AX) H0(3, AX) H0(4, AX) H0(5, AX) H0(6, AX) H0(7, AY)
        "v_add_u32 v2, %0, v2\n\ts_movk_i32 %0, 0x400\n\tv_mov_b32 v4, %[bit]\n\ts_lshl_b32 %[bit], %[bit], 4\n\t"
        "s_add_u32 %2, %2, 1\n\ts_cmp_eq_u32 %2, 4\n\ts_cbranch_scc1 7f\n\t"
        "s_cmp_lg_u32 %2, %[jmp]\n\ts_cbranch_scc1 6f\n\t"
        "v_add_u32 v1, 0x800, v1\n\t" LB8
        "6:\n\tv_add_u32 v1, 0x200, v1\n\tds_read_b128 v[108:111], v1 offset:448\n\t"
        H1(0, "0") H1(1, AX) PUB H1(2, AX) H1(3, AX) H1(4, AX) H1(5, AX) H1(6, AX) H1(7, AX)
        "s_cmp_lg_u32 %2, %[one]\n\ts_cbranch_scc1 3b\n\ts_branch 4b\n"
        "7:\n\ts_nop 2\n\t" PUB ST_(6) FLUSH_
        "s_waitcnt lgkmcnt(0)\n\ts_endpgm"
        : "=&s"(c0), "=&s"(c1), "+s"(si)
        : "{v0}"(Ac + ((lane >> 3) & 1) * (AH + 128) + ((lane >> 2) & 1) * 1344 + 56 - kg * 16 + (lane & 3) * 8),
          "{v1}"(Uc + 32 - 1024 + h * 512 + c16 * 32 + kg * 16), "{v2}"(pw), "{v3}"(F + F_U), [msk] "s"(umask), [bit] "s"(1u << ch),
          [h] "s"(h), [jmp] "s"(h ? 9 : 2), [one] "s"(h ? 9 : 1)
          , STADDR
        : "memory", "scc", "v127");
    }
    {
      LDS char* const T = Ac + A_HI;
      {
        LDS u32* q = (LDS u32*)(T + kg * 256 + c16 * 4);
        q[0] = pk(e0[0], e1[0]); q[16] = pk(e0[1], e1[1]); q[32] = pk(e0[2], e1[2]); q[48] = pk(e0[3], e1[3]);
      }
      MEMB();
      LDS char* tp = T + (c16 - 4 * kg - 3) * 4;
      v8bf ta[3], tb4[3], t8[2];
      {
        v2u q[16]; u32 kk;
        asm volatile(TB(0, 1, 1) TB(2, 3, 2) TB(4, 5, 3) TB(6, 7, 4) TB(8, 9, 5) TB(10, 11, 6) TB(12, 13, 7) TB(14, 15, 8)
                     "ds_read_b32 %16, %[t] offset:380\n\ts_waitcnt lgkmcnt(0)"
                     : "=&v"(q[0]), "=&v"(q[1]), "=&v"(q[2]), "=&v"(q[3]), "=&v"(q[4]), "=&v"(q[5]), "=&v"(q[6]), "=&v"(q[7]),
                       "=&v"(q[8]), "=&v"(q[9]), "=&v"(q[10]), "=&v"(q[11]), "=&v"(q[12]), "=&v"(q[13]), "=&v"(q[14]), "=&v"(q[15]), "=&v"(kk)
                     : [p] "v"(tp), [t] "v"(T) : "memory");
#pragma unroll
        for (int i = 0; i < 3; i++) { ta[i] = cat2(q[2 * i], q[2 * i + 1]); tb4[i] = cat2(q[6 + 2 * i], q[7 + 2 * i]); }
        t8[0] = cat2(q[12], q[13]); t8[1] = cat2(q[14], q[15]);
        kk = __builtin_amdgcn_perm(kk, kk, 0x03020302u);
#pragma unroll
        for (int j = 0; j < 4; j++) b34[j] &= kk;
      }
      asm volatile("" : "+v"(ta[0]), "+v"(ta[1]), "+v"(ta[2]), "+v"(tb4[0]), "+v"(tb4[1]), "+v"(tb4[2]), "+v"(t8[0]), "+v"(t8[1]), "+v"(b34));
      LDS char* pu = Uc + 512 - 48 + (h ? 0 : 1024) + c16 * 32 + kg * 16;
      __builtin_amdgcn_s_setprio(2);
      STAMP(3);
      lds_wait<0>(F + F_U, umask);
      STAMP(4);
      const v4f zz = {0.f, 0.f, 0.f, 0.f};
      v8bf ua[4], ub[4];
#pragma unroll
      for (int a = 0; a < 4; a++) { ua[a] = *(LDS v8bf*)(pu + a * 512 + 16); ub[a] = *(LDS v8bf*)(pu + a * 512); }
      v4f r0 = zz, r1 = zz, r2 = zz, r3 = zz;
      v8bf wa0 = wop(ua[0], ub[0], b35, b35), wb0 = wop(ua[0], ub[0], b6a, b6a);
      v8bf wa1 = wop(ua[1], ub[1], b35, b35), wb1 = wop(ua[1], ub[1], b6a, b6a);
      if (h) {
        r0 = MF(wa0, ta[0], r0); r1 = MF(wa0, ta[1], r1); r0 = MF(wb0, tb4[0], r0); r1 = MF(wb0, tb4[1], r1);
        r1 = MF(wa1, ta[0], r1); r1 = MF(wb1, tb4[0], r1);
      }
      r2 = MF(wa0, ta[2], r2); r3 = MF(wa1, ta[2], r3); r2 = MF(wb0, tb4[2], r2); r3 = MF(wb1, tb4[2], r3);
      *(LDS v4f*)pw = r0;
      *(LDS v4f*)(pw + 1024) = r1;
      lds_sig(F + F_PH, 0x110000u << ch);
      r2 = MF(wa1, ta[1], r2); r2 = MF(wb1, tb4[1], r2);
      {
        v8bf wa2 = wop(ua[2], ub[2], b35, b35), wb2 = wop(ua[2], ub[2], b6a, b6a), w80 = wop(ua[0], ub[0], b5b, b4c);
        r2 = MF(BF8(b34), ua[0], r2); r3 = MF(BF8(b34), ua[1], r3);
        r2 = MF(wa2, ta[0], r2); r3 = MF(wa2, ta[1], r3); r2 = MF(wb2, tb4[0], r2); r3 = MF(wb2, tb4[1], r3);
        r2 = MF(w80, t8[0], r2); r3 = MF(w80, t8[1], r3);
      }
      if (!h) {
        v8bf va0 = *(LDS v8bf*)(pu - 1024 + 16), vb0 = *(LDS v8bf*)(pu - 1024), va1 = *(LDS v8bf*)(pu - 512 + 16), vb1 = *(LDS v8bf*)(pu - 512);
        v2u q[4];
        asm volatile(TB(0, 1, 9) TB(2, 3, 10) "s_waitcnt lgkmcnt(0)" : "=&v"(q[0]), "=&v"(q[1]), "=&v"(q[2]), "=&v"(q[3]) : [p] "v"(tp) : "memory");
        const v8bf t2 = cat2(q[0], q[1]), t3 = cat2(q[2], q[3]);
        v8bf x0 = wop(va0, vb0, b5b, b4c), x1 = wop(va1, vb1, b5b, b4c);
        r2 = MF(x0, t2, r2); r3 = MF(x0, t3, r3); r2 = MF(x1, t8[1], r2); r3 = MF(x1, t2, r3);
      }
      {
        v8bf wa3 = wop(ua[3], ub[3], b35, b35), wb3 = wop(ua[3], ub[3], b6a, b6a), w81 = wop(ua[1], ub[1], b5b, b4c);
        *(LDS v4f*)(pw + 2048) = r2;
        lds_sig(F + F_PH, 0x1000000u << ch);
        r3 = MF(wa3, ta[0], r3); r3 = MF(wb3, tb4[0], r3); r3 = MF(w81, t8[0], r3);
      }
      *(LDS v4f*)(pw + 3072) = r3;
      lds_sig(F + F_PH, 0x10000000u << ch);
      STAMP(5);
      if (ch == 0) {
        __builtin_amdgcn_s_setprio(3);
        lds_wait<1>(F + F_SP, 256);
        const v2u ts = *(LDS v2u*)(F + F_TS);
        const u32 tag = *(LDS u32*)(F + F_TAG);
        const u32 s0 = __builtin_amdgcn_readfirstlane(ts[0]), s1 = __builtin_amdgcn_readfirstlane(ts[1]);
        const int ts0 = 2047 - (s0 & 2047), ts1 = 2047 - (s1 & 2047);
        const u32 own = h ? 0x3Cu : 0xC3u;
        const u32 need = ((own >> (ts0 >> 8)) & 1) | ((own >> (ts1 >> 8)) & 1) << 1;
        if (need) {
          const u32 kk = (lane & 1) ? s1 : s0;
          const float r = __uint_as_float((kk ^ 0x80000000u ^ ((~(int)kk >> 31) & 0x7fff0000u)) & 0xffff0000u);
          float sgm = rbf(__builtin_amdgcn_rcpf(1.f + __builtin_amdgcn_exp2f(r * -1.44269504089f)));
          u32 c_lo = 0x3f80u, c_hi = 0x3f800000u;
          asm volatile("" : "+v"(sgm), "+s"(c_lo), "+s"(c_hi));
          __builtin_amdgcn_s_sleep(D_Q);
          const u32 o0 = (lane >> 1) * 4096 + (lane & 1) * 16, o3 = o0 + (lane < 32 ? 96 * 4096 : 0);
          v4u a[4];
          int i = SPIN;
          bool bad;
          _Pragma("unroll 1") do {
#pragma unroll
            for (int j = 0; j < 4; j++) a[j] = __builtin_amdgcn_raw_buffer_load_b128(rs, j < 3 ? o0 : o3, WS_P + cg * 32 + (j < 3 ? j * 32 * 4096 : 0), 16);
            bad = __any(((a[0][2] ^ tag) | (a[1][2] ^ tag) | (a[2][2] ^ tag) | (a[3][2] ^ tag)) != 0);
            MEMB();
          } while (bad && --i);
          STAMP(6);
          if (lane >= 32) { a[3][0] = 0; a[3][1] = 0; }
          float y[4] = {0.f, 0.f, 0.f, 0.f};
#pragma unroll
          for (int j = 0; j < 4; j++) {
            y[0] = dot2(a[j][0], c_lo, y[0]); y[1] = dot2(a[j][0], c_hi, y[1]);
            y[2] = dot2(a[j][1], c_lo, y[2]); y[3] = dot2(a[j][1], c_hi, y[3]);
          }
          float f4 = fold4(y[0], y[2], y[1], y[3]);
          f4 += dppf<0x4E>(f4); f4 += dppf<0x124>(f4); f4 += dppf<0x128>(f4);
          const float up = rbf(f4) * sgm;
          lds_wait<0>(F + F_SP, need);
          if (c16 < 2 && ((need >> c16) & 1))
            out[(c16 ? ts1 : ts0) * 512 + cg * 4 + kg] = (u16)pk(*(LDS float*)(F + F_SV + c16 * 16 + kg * 4) + up, 0.f);
        }
      }
      STAMP(7);
      FLUSH();
    }
  }
}

__global__ void __launch_bounds__(PT) khead(char* ws) {
  u64 ts; asm volatile("s_memrealtime %0" : "=s"(ts) :: "memory");
  __shared__ char sm[PL];
  if (PV == 128) asm volatile("" ::: "v127");
  sm[threadIdx.x] = 1;
  asm volatile("s_waitcnt lgkmcnt(0)" : "+s"(ts) :: "memory");
  if (threadIdx.x == 0) ((u64*)(ws + (17u << 20)))[blockIdx.x] = ts + sm[threadIdx.x] - 1;
}
__global__ void __launch_bounds__(64) kprobe(char* ws) {
  const int lane = threadIdx.x;
  const u64* st = (const u64*)(ws + (17u << 20));
  u64 t0 = ~0ull, mx = 0;
  for (int j = lane; j < PB; j += 64) { u64 v = st[j]; t0 = v < t0 ? v : t0; mx = v > mx ? v : mx; }
  for (int o = 32; o; o >>= 1) { u64 a = __shfl_xor(t0, o), c = __shfl_xor(mx, o); t0 = a < t0 ? a : t0; mx = c > mx ? c : mx; }
  u64 t1;
  u64 end = (PM ? mx : t0) + 3000;
  for (int i = 0; i < 2000000; i++) {
    asm volatile("s_memrealtime %0\n\ts_waitcnt lgkmcnt(0)" : "=s"(t1) :: "memory");
    if (t1 >= end) break;
  }
}

extern "C" void kernel_launch(void* const* d_in, const int* in_sizes, int n_in, void* d_out, int out_size, void* d_ws,
                              size_t ws_size, hipStream_t stream) {
  khead<<<PB, PT, 0, stream>>>((char*)d_ws);
  kf<<<256, 1024, 0, stream>>>((const u16*)d_in[0], (const u16*)d_in[10], (char*)d_ws, (const u16*)d_in[8], (u16*)d_out,
                               (const u16*)d_in[7], (const u16*)d_in[1], (const u16*)d_in[2], (const u16*)d_in[3], (const u16*)d_in[4],
                               (const u16*)d_in[5], (const u16*)d_in[6], (const u16*)d_in[11], (const u16*)d_in[12], (const u16*)d_in[13]);
  kprobe<<<1, 64, 0, stream>>>((char*)d_ws);
}
```

```cpp
#include <hip/hip_runtime.h>

typedef unsigned short u16;
typedef unsigned u32;
typedef unsigned long long u64;
typedef u32 v4u __attribute__((ext_vector_type(4)));
typedef u32 v2u __attribute__((ext_vector_type(2)));
typedef float v4f __attribute__((ext_vector_type(4)));
typedef float v2f __attribute__((ext_vector_type(2)));
typedef __bf16 v8bf __attribute__((ext_vector_type(8)));
typedef __bf16 v2bf __attribute__((ext_vector_type(2)));
typedef short v4s __attribute__((ext_vector_type(4)));
#define LDS __attribute__((address_space(3)))
#define GLB __attribute__((address_space(1)))
#define CST __attribute__((address_space(4)))
#define DEVI __device__ __forceinline__
#define SB() __builtin_amdgcn_sched_barrier(0)
#define MEMB() asm volatile("" ::: "memory")

#define WS_R 4096
#define WS_S 8192
#define WS_P 32768
#define WS_X (1u << 20)
#define WS_END (3u << 20)
#define SPIN 4000
#define PF_LATE 0
#define CODE_SZ 13824
#define D_A 26
#define D_W 2
#define D_K 4
#define D_Q 30

#define ST_(i)
#define STAMP(i)
#define STAMPD(i, v)
#define FLUSH_
#define FLUSH()

DEVI u32 pk(float a, float b) { return __builtin_bit_cast(u32, __builtin_convertvector((v2f){a, b}, v2bf)); }
DEVI float rbf(float a) { return __uint_as_float(pk(0.f, a)); }
DEVI float lo2f(u32 v) { return __uint_as_float(v << 16); }
DEVI float hi2f(u32 v) { return __uint_as_float(v & 0xffff0000u); }
DEVI float dot2(u32 a, u32 b, float c) { return __builtin_amdgcn_fdot2_f32_bf16(__builtin_bit_cast(v2bf, a), __builtin_bit_cast(v2bf, b), c, false); }
template <int CTRL> DEVI float dppf(float v) { return __int_as_float(__builtin_amdgcn_update_dpp(0, __float_as_int(v), CTRL, 0xf, 0xf, true)); }
template <int CTRL, int RM> DEVI u32 dppu(u32 v) { return (u32)__builtin_amdgcn_update_dpp(0, (int)v, CTRL, RM, 0xf, false); }
DEVI float rdl(float v, int l) { return __int_as_float(__builtin_amdgcn_readlane(__float_as_int(v), l)); }
DEVI float fold4(float a, float b, float c, float d) {
  v2u p = __builtin_amdgcn_permlane32_swap(__float_as_uint(a), __float_as_uint(b), false, false);
  v2u q = __builtin_amdgcn_permlane32_swap(__float_as_uint(c), __float_as_uint(d), false, false);
  float t = __uint_as_float(p[0]) + __uint_as_float(p[1]), s = __uint_as_float(q[0]) + __uint_as_float(q[1]);
  v2u r = __builtin_amdgcn_permlane16_swap(__float_as_uint(t), __float_as_uint(s), false, false);
  return __uint_as_float(r[0]) + __uint_as_float(r[1]);
}
DEVI u32 wave_max(u32 v) {
  v = max(v, dppu<0xB1, 0xf>(v)); v = max(v, dppu<0x4E, 0xf>(v)); v = max(v, dppu<0x141, 0xf>(v)); v = max(v, dppu<0x140, 0xf>(v));
  v = max(v, dppu<0x142, 0xa>(v)); v = max(v, dppu<0x143, 0xc>(v));
  return (u32)__builtin_amdgcn_readlane((int)v, 63);
}
DEVI v4s trd(LDS char* p) { return __builtin_amdgcn_ds_read_tr16_b64_v4i16((LDS v4s*)p); }
DEVI v8bf cat(v4s a, v4s b) { return __builtin_bit_cast(v8bf, __builtin_shufflevector(a, b, 0, 1, 2, 3, 4, 5, 6, 7)); }
template <int SL> DEVI void lds_wait(LDS char* p, u32 m) {
  u32 v, s, c;
  asm volatile("s_mov_b32 %2, 0x20000\n"
               "1:\n\tds_read_b32 %0, %3\n\ts_waitcnt lgkmcnt(0)\n\tv_readfirstlane_b32 %1, %0\n\ts_andn2_b32 %1, %4, %1\n\t"
               "s_cbranch_scc0 2f\n\ts_sleep %5\n\ts_sub_u32 %2, %2, 1\n\ts_cbranch_scc0 1b\n2:"
               : "=&v"(v), "=&s"(s), "=&s"(c) : "v"(p), "s"(m), "n"(SL) : "memory", "scc");
}
DEVI void lds_sig(LDS char* p, u32 bit) {
  asm volatile("s_mov_b64 exec, 1\n\tds_or_b32 %0, %1\n\ts_mov_b64 exec, -1" :: "v"(p), "v"(bit) : "memory");
}
DEVI void merge2(u32& a0, u32& a1, u32 b0, u32 b1) { u32 m = min(a0, b0); a0 = max(a0, b0); a1 = max(m, max(a1, b1)); }
DEVI v2f cpow(float er, float ph) {
  float e = __builtin_amdgcn_exp2f(er), f = __builtin_amdgcn_fractf(ph);
  return (v2f){__builtin_amdgcn_cosf(f), __builtin_amdgcn_sinf(f)} * e;
}

#define RP 272
#define BC 17408
#define O_A 69632
#define AC 11776
#define AH 5888
#define A_HI 3584
#define O_U (O_A + 4 * AC)
#define UP 4640
#define O_F (O_U + 4 * UP)
#define O_LN (O_F + 768)
#define O_W (O_LN + 8704)
#define F_KS 0
#define F_U 64
#define F_PH 68
#define F_EX 72
#define F_SP 76
#define F_TS 80
#define F_TAG 88
#define F_X 92
#define F_SV 128
#define F_E 256
#define LDS_SZ (O_W + 16384 + 256)
#define AOFF 49632

#define CM1(t, x, m) "v_pk_mul_f32 " t ", " x ", " m " op_sel_hi:[0,1]\n\t"
#define CM2(r, t, x, m) "v_pk_fma_f32 " r ", " x ", " m ", " t " op_sel:[1,1,0] op_sel_hi:[1,0,1] neg_lo:[0,1,0]\n\t"
DEVI v2f cmul(v2f x, v2f m) {
  v2f t, r;
  asm(CM1("%1", "%2", "%3") CM2("%0", "%1", "%2", "%3") : "=v"(r), "=&v"(t) : "v"(x), "v"(m));
  return r;
}
template <int O1, int O2> DEVI void step2(v2f& r1, v2f x1, v2f m1, v2f& r2, v2f x2, v2f m2) {
  v2f t1, t2;
  asm volatile(CM1("%2", "%4", "%5") CM1("%3", "%6", "%7") CM2("%0", "%2", "%4", "%5") CM2("%1", "%3", "%6", "%7")
               : "=&v"(r1), "=v"(r2), "=&v"(t1), "=&v"(t2) : "v"(x1), "v"(m1), "v"(x2), "v"(m2));
  u32 p1 = pk(r1.x, r1.y), p2 = pk(r2.x, r2.y);
  asm volatile("ds_write_addtid_b32 %0 offset:%2\n\tds_write_addtid_b32 %1 offset:%3" :: "v"(p1), "v"(p2), "n"(O1), "n"(O2) : "memory");
}
template <int O1> DEVI void put1(u32 p) { asm volatile("ds_write_addtid_b32 %0 offset:%1" :: "v"(p), "n"(O1) : "memory"); }
template <int J> DEVI void brows(v2f& e, v2f& o, v2f m) {
  if constexpr (J < 16) { step2<J * RP, (15 + J) * RP>(e, e, m, o, o, m); brows<J + 1>(e, o, m); }
}
#define MF(a, b, c) __builtin_amdgcn_mfma_f32_16x16x32_bf16(a, b, c, 0, 0, 0)
#define BF8(x) __builtin_bit_cast(v8bf, x)
template <int LO, int HI> DEVI v4u band(int e, u32 one) {
  const u32 m = (u32)(((((u64)1 << (HI - LO + 1)) - 1) << (e - HI + 24)) >> 24);
  v4u r;
#pragma unroll
  for (int j = 0; j < 4; j++) { u32 y = m >> (2 * j); r[j] = ((y & 1) | ((y & 2) << 15)) * one; }
  return r;
}
DEVI v8bf wop(v8bf a, v8bf b, v4u ba, v4u bb) {
  const v4f zz = {0.f, 0.f, 0.f, 0.f};
  v4f p = MF(a, BF8(ba), zz), q = MF(b, BF8(bb), zz);
  return BF8(((v4u){pk(p[0], q[0]), pk(p[1], q[1]), pk(p[2], q[2]), pk(p[3], q[3])}));
}
#define TB(a, b, s) "ds_read2_b32 %" #a ", %[p] offset0:16*" #s "+3 offset1:16*" #s "+2\n\tds_read2_b32 %" #b ", %[p] offset0:16*" #s "+1 offset1:16*" #s "\n\t"
DEVI v8bf cat2(v2u a, v2u b) { return BF8(__builtin_shufflevector(a, b, 0, 1, 2, 3)); }

#define VR(b, j) "v[" #b "+4*" #j ":" #b "+3+4*" #j "]"
#define LA(b, j, o) "ds_read_b64_tr_b16 v[" #b "+4*" #j ":" #b "+1+4*" #j "], v0 offset:" #o "+456-64*" #j "\n\t" \
                    "ds_read_b64_tr_b16 v[" #b "+2+4*" #j ":" #b "+3+4*" #j "], v0 offset:" #o "+448-64*" #j "\n\t"
#define LA8(b, o) LA(b, 0, o) LA(b, 1, o) LA(b, 2, o) LA(b, 3, o) LA(b, 4, o) LA(b, 5, o) LA(b, 6, o) LA(b, 7, o)
#define LB(j) "ds_read_b128 " VR(76, j) ", v1 offset:1024+64*" #j "\n\t"
#define LB8 LB(0) LB(1) LB(2) LB(3) LB(4) LB(5) LB(6) LB(7)
#define AX "v[112:115]"
#define AY "v[116:119]"
#define MA(d, a, j, c) "v_mfma_f32_16x16x32_bf16 " d ", " VR(a, j) ", " VR(76, j) ", " c "\n\t"
#define H0(j, d) "s_waitcnt lgkmcnt(7-" #j ")\n\t" MA(d, 8, j, AX)
#define H1(j, c) "s_waitcnt lgkmcnt(8)\n\t" MA(AX, 40, j, c) LB(j)
#define PUB "ds_write_b128 v2, " AY "\n\ts_mov_b64 exec, 1\n\tds_or_b32 v3, v4 offset:4\n\ts_mov_b64 exec, -1\n\t"
#define WG_ "global_load_lds_dwordx4 %[go], %[wg]\n\ts_add_u32 m0, m0, 0x400\n\tv_add_u32 %[go], 0x1c000, %[go]\n\t"
#define WU(j) "global_load_dwordx4 %" #j ", %[gu], %[wu]\n\tv_add_u32 %[gu], 0x1c000, %[gu]\n\t"
#define WD(j, o) "global_load_dwordx4 %" #j ", %[dd], %[wd] offset:" #o "\n\t"

__global__ void __launch_bounds__(1024, 4) kf(const u16* __restrict__ x, const u16* __restrict__ wr, char* __restrict__ ws,
    const u16* __restrict__ dskip, u16* __restrict__ out, const u16* __restrict__ logdt,
    const u16* __restrict__ Are, const u16* __restrict__ Aim, const u16* __restrict__ Bre, const u16* __restrict__ Bim,
    const u16* __restrict__ Cre, const u16* __restrict__ Cim,
    const u16* __restrict__ wg, const u16* __restrict__ wu, const u16* __restrict__ wd) {
  __shared__ __attribute__((aligned(16))) char smem[LDS_SZ];
  LDS char* L = (LDS char*)smem;
  LDS char* F = L + O_F;
  const int lane = threadIdx.x & 63, w = __builtin_amdgcn_readfirstlane(threadIdx.x >> 6);
  u64 pc;
  asm volatile("ds_write_b32 %1, %2\n\ts_getpc_b64 %0" : "=s"(pc) : "v"(O_U + min(w >> 1, 4) * UP + (threadIdx.x & 127) * 4), "v"(0) : "memory");
  const int b = blockIdx.x, sw = (b & 7) * 32 + (b >> 3), cg = sw >> 1, h = sw & 1;
  const int c16 = lane & 15, kg = lane >> 4;
  const int t = sw * 8 + w, ch = w & 3, half = (w >> 2) & 1;
  v4u xr, wv;
  u32 tag0; v2u dsk; char* wsb;
  {
    const int d = cg * 4 + ch;
    asm volatile("s_waitcnt lgkmcnt(0)\n\ts_cmp_lt_u32 %[w], 8\n\ts_cbranch_scc0 1f\n\t"
                 "global_load_dwordx4 %0, %[xo], %[xp]\n\tglobal_load_dwordx4 %1, %[lo], %[wr]\n\t"
                 "s_load_dword %2, %[ws], 0\n\ts_load_dwordx2 %3, %[dk], %[co]\n\ts_setprio 3\n\ts_branch 2f\n"
                 "1:\n\tglobal_load_ushort v12, %[ix], %[p0]\n\tglobal_load_ushort v13, %[ix], %[p1]\n\tglobal_load_ushort v14, %[ix], %[p2]\n\t"
                 "global_load_ushort v15, %[ix], %[p3]\n\tglobal_load_ushort v16, %[ix], %[p4]\n\tglobal_load_ushort v17, %[ix], %[p5]\n\t"
                 "s_load_dwordx2 %3, %[ld], %[co]\n\ts_setprio 1\n2:\n\ts_mov_b64 %4, %[ws]"
                 : "=&{v[12:15]}"(xr), "=&{v[16:19]}"(wv), "=&s"(tag0), "=&{s[40:41]}"(dsk), "=&s"(wsb)
                 : [w] "s"(w), [lo] "v"(lane * 16), [xo] "v"(t * 1024 + lane * 16), [xp] "s"(x), [wr] "s"(wr), [ws] "s"(ws), [dk] "s"(dskip), [co] "s"(cg * 8),
                   [ix] "v"((d * 128 + half * 64 + lane) * 2), [p0] "s"(Are), [p1] "s"(Aim), [p2] "s"(Bre), [p3] "s"(Bim), [p4] "s"(Cre), [p5] "s"(Cim),
                   [ld] "s"(logdt), "s"(out), "s"(wg), "s"(wu), "s"(wd)
                 : "scc");
  }
  STAMP(0);
  const __amdgpu_buffer_rsrc_t rs = __builtin_amdgcn_make_buffer_rsrc(wsb, 0, WS_END, 0x00020000);
  if (__builtin_expect(w < 8, 1)) {
    LDS char* a_xn = L + O_LN + w * 1088 + lane * 16;
    LDS char* a_ks = F + F_KS + w * 8;
    LDS char* a_f = F;
    LDS char* a_kr = F + F_KS + (lane & 7) * 8;
    LDS char* a_tr = L + O_LN + ((lane >> 2) & 3) * 1088 + w * 128 + kg * 32 + (lane & 3) * 8;
    u32 o_pub = (lane >> 3) * 32768 + (lane & 7) * 16, rsh = (lane & 3) * 4 + 1, o_r = WS_R + sw * 16, o_s = lane * 8;
    u32 so_pub = WS_X + w * 8 * 32768 + sw * 128, so_s = WS_S + sw * 64, kix = 0x80000000u | (u32)(2047 - t);
    const u32 fq = ((b >> 4) + ((~b & 8) << 1)) | ((w - 6) & 0x100);
    const bool ffn = fq < 14;
    const int fs = (b & 7) * 14 + (b >> 4), fw = w & 1;
    asm volatile("s_barrier\n\ts_waitcnt vmcnt(0)"
                 : "+v"(xr), "+v"(wv), "+v"(a_xn), "+v"(a_ks), "+v"(a_f), "+v"(a_kr), "+v"(a_tr), "+v"(o_pub), "+v"(rsh), "+v"(o_r), "+v"(o_s),
                   "+s"(so_pub), "+s"(so_s), "+s"(kix) :: "memory");
    STAMP(1);
    float s1 = 0.f, s2 = 0.f, sr = 0.f, sw_ = 0.f;
#pragma unroll
    for (int j = 0; j < 4; j++) { s1 = dot2(xr[j], 0x3f803f80u, s1); s2 = dot2(xr[j], xr[j], s2); sr = dot2(xr[j], wv[j], sr); sw_ = dot2(wv[j], 0x3f803f80u, sw_); }
    float f4 = fold4(s1, s2, sr, sw_);
    f4 += dppf<0xB1>(f4); f4 += dppf<0x4E>(f4); f4 += dppf<0x141>(f4); f4 += dppf<0x140>(f4);
    f4 *= 1.f / 512.f;
    const float dm = rdl(f4, 0), mb = rbf(dm);
    const float sd = rbf(__builtin_amdgcn_sqrtf(rbf(rbf(fmaxf(rdl(f4, 32) - dm * dm, 0.f)) + 1e-5f)));
    const float r16 = __builtin_amdgcn_rcpf(sd * 16.f), off = -mb * r16;
    v4u xn;
#pragma unroll
    for (int j = 0; j < 4; j++) { v2f y = __builtin_elementwise_fma((v2f){lo2f(xr[j]), hi2f(xr[j])}, (v2f){r16, r16}, (v2f){off, off}); xn[j] = pk(y.x, y.y); }
    asm volatile("s_waitcnt lgkmcnt(0)" : "+s"(tag0), "+s"(dsk), "+v"(xn) :: "memory");
    const u32 tag = tag0 + 0x11111111u;
    *(LDS v4u*)a_xn = xn;
    const u32 rb = pk(0.f, (rdl(f4, 16) - mb * rdl(f4, 48)) * 8192.f * r16);
    const u32 key = rb ^ (((int)rb >> 31) & 0x7fff0000u) ^ kix;
    asm volatile("s_mov_b64 exec, 1\n\tds_write_b32 %0, %2 offset:%4\n\tds_write_b64 %1, %3\n\ts_mov_b64 exec, -1"
                 :: "v"(a_f), "v"(a_ks), "v"(tag), "v"((v2u){key, (__float_as_uint(mb) >> 16) | __float_as_uint(sd)}), "n"(F_TAG) : "memory");
    u32 pt[4];
    pt[0] = __builtin_amdgcn_alignbit(tag, tag, rsh) ^ 0x3C5A96C3u;
    pt[1] = pt[0] << 1; pt[2] = pt[0] << 2; pt[3] = pt[0] << 3;
    asm volatile("" : "+v"(pt[0]), "+v"(pt[1]), "+v"(pt[2]), "+v"(pt[3]));
    {
      v2u ks, a0, a1; u32 cnt;
      asm volatile("s_mov_b32 %3, 0x20000\n"
                   "1:\n\tds_read_b64 %0, %4\n\tds_read_b64_tr_b16 %1, %5\n\tds_read_b64_tr_b16 %2, %5 offset:4352\n\ts_waitcnt lgkmcnt(2)\n\t"
                   "v_cmp_gt_u64 vcc, %6, %0\n\ts_cbranch_vccz 2f\n\ts_sub_u32 %3, %3, 1\n\ts_cbranch_scc0 1b\n2:\n\ts_waitcnt lgkmcnt(0)"
                   : "=&v"(ks), "=&v"(a0), "=&v"(a1), "=&s"(cnt)
                   : "v"(a_kr), "v"(a_tr), "s"(1ull << 32)
                   : "memory", "scc", "vcc");
      if (w == 0) {
        u32 o = dppu<0xB1, 0xf>(ks[0]), k0 = max(ks[0], o), k1 = min(ks[0], o);
        merge2(k0, k1, dppu<0x4E, 0xf>(k0), dppu<0x4E, 0xf>(k1));
        merge2(k0, k1, dppu<0x141, 0xf>(k0), dppu<0x141, 0xf>(k1));
        if (lane == 0) __builtin_amdgcn_raw_buffer_store_b128((v4u){k0, k1, tag, tag}, rs, o_r, 0, 16);
      }
      v4u d = {a0[0], a0[1], a1[0], a1[1]};
#pragma unroll
      for (int j = 0; j < 4; j++) d[j] = (d[j] & ~0x40004000u) | (pt[j] & 0x40004000u);
      __builtin_amdgcn_raw_buffer_store_b128(d, rs, o_pub, so_pub, 16);
      if (w == 1 && lane < 8) __builtin_amdgcn_raw_buffer_store_b64((v2u){ks[1], tag}, rs, o_s, so_s, 16);
    }
    STAMP(2);
    const int si = w >> 1, s = h ? 2 + si : (si < 2 ? si : si + 4), t0 = s * 256 + (w & 1) * 128 + lane * 2;
    v8bf fg[8], fu[8];
    v4f sg = {0.f, 0.f, 0.f, 0.f}, su = sg;
    LDS char* const wl = L + O_W + fw * 8192;
    if (ffn) {
      v4u wq[16];
      const int rho = lane >> 1, kk = 8 * ((rho >> 2) & 3) + 4 * (rho >> 4) + (rho & 3);
      u32 go = (fw * 256 + kk) * 3584 + fs * 32 + (lane & 1) * 16, gu = go;
      asm volatile("s_mov_b32 m0, %[m]\n\ts_sleep %[sl]\n\t"
                   WG_ WG_ WG_ WG_ WG_ WG_ WG_ WG_ WU(0) WU(1) WU(2) WU(3) WU(4) WU(5) WU(6) WU(7)
                   WD(8, 0) WD(9, 64) WD(10, 128) WD(11, 192) WD(12, 256) WD(13, 320) WD(14, 384) WD(15, 448)
                   "s_waitcnt vmcnt(16)"
                   : "=&v"(wq[0]), "=&v"(wq[1]), "=&v"(wq[2]), "=&v"(wq[3]), "=&v"(wq[4]), "=&v"(wq[5]), "=&v"(wq[6]), "=&v"(wq[7]),
                     "=&v"(wq[8]), "=&v"(wq[9]), "=&v"(wq[10]), "=&v"(wq[11]), "=&v"(wq[12]), "=&v"(wq[13]), "=&v"(wq[14]), "=&v"(wq[15]),
                     [go] "+v"(go), [gu] "+v"(gu)
                   : [m] "s"(O_W + fw * 8192), [sl] "n"(D_W), [wg] "s"(wg), [wu] "s"(wu), [wd] "s"(wd),
                     [dd] "v"((fs * 16 + (lane >> 2)) * 1024 + fw * 512 + (lane & 3) * 16)
                   : "memory", "scc");
#pragma unroll
      for (int ks = 0; ks < 8; ks++) fg[ks] = cat(trd(wl + lane * 8 + ks * 1024), trd(wl + lane * 8 + ks * 1024 + 512));
      asm volatile("s_waitcnt vmcnt(8)"
                   : "+v"(wq[0]), "+v"(wq[1]), "+v"(wq[2]), "+v"(wq[3]), "+v"(wq[4]), "+v"(wq[5]), "+v"(wq[6]), "+v"(wq[7]) :: "memory");
#pragma unroll
      for (int ks = 0; ks < 8; ks++) *(LDS v4u*)(wl + ks * 1024 + lane * 16) = wq[ks];
      MEMB();
#pragma unroll
      for (int ks = 0; ks < 8; ks++) fu[ks] = cat(trd(wl + lane * 8 + ks * 1024), trd(wl + lane * 8 + ks * 1024 + 512));
      asm volatile("s_waitcnt vmcnt(0)"
                   : "+v"(wq[8]), "+v"(wq[9]), "+v"(wq[10]), "+v"(wq[11]), "+v"(wq[12]), "+v"(wq[13]), "+v"(wq[14]), "+v"(wq[15]) :: "memory");
#pragma unroll
      for (int ks = 0; ks < 8; ks++) *(LDS v4u*)(wl + ks * 1024 + lane * 16) = wq[8 + ks];
      MEMB();
      const v8bf ones = BF8(((v4u){0x3f803f80u, 0x3f803f80u, 0x3f803f80u, 0x3f803f80u}));
#pragma unroll
      for (int ks = 0; ks < 8; ks++) { sg = MF(fg[ks], ones, sg); su = MF(fu[ks], ones, su); }
      asm volatile("" : "+v"(sg), "+v"(su));
      STAMP(3);
      __builtin_amdgcn_s_sleep(D_K);
    } else {
      __builtin_amdgcn_s_sleep(D_A);
      if (w < 6 || h == 0) {
        const u32 o = (lane >> 2) * 128 + (lane & 3) * 16, so = WS_X + (cg >> 1) * 32768 + w * 4096 + (cg & 1) * 64;
        v4u a, c;
        int i = SPIN;
        bool bad;
        _Pragma("unroll 1") do {
          a = __builtin_amdgcn_raw_buffer_load_b128(rs, o, so, 16);
          c = __builtin_amdgcn_raw_buffer_load_b128(rs, o + 2048, so, 16);
#pragma unroll
          for (int j = 0; j < 4; j++) { a[j] ^= pt[j] & 0x40004000u; c[j] ^= pt[j] & 0x40004000u; }
          u32 e = (a[0] | a[1] | a[2]) | (a[3] | c[0] | c[1]) | (c[2] | c[3]);
          bad = __any(e & 0x40004000u);
          MEMB();
        } while (bad && --i);
        LDS char* q = L + O_U + (lane & 3) * UP + 512 + (w * 32 + (lane >> 2)) * 16;
        *(LDS v4u*)q = a;
        *(LDS v4u*)(q + 256) = c;
        lds_sig(F + F_U, 1u << w);
      }
      STAMP(3);
    }
    if (w == 7 || ffn) {
      v4u k[4];
      {
        int i = SPIN;
        bool bad;
        _Pragma("unroll 1") do {
#pragma unroll
          for (int j = 0; j < 4; j++) k[j] = __builtin_amdgcn_raw_buffer_load_b128(rs, j * 1024 + lane * 16, WS_R, 16);
          bad = __any(((k[0][2] ^ tag) | (k[1][2] ^ tag) | (k[2][2] ^ tag) | (k[3][2] ^ tag)) != 0);
          MEMB();
        } while (bad && --i);
      }
      u32 k0 = k[0][0], k1 = k[0][1], k2 = k[2][0], k3 = k[2][1];
      merge2(k0, k1, k[1][0], k[1][1]); merge2(k2, k3, k[3][0], k[3][1]); merge2(k0, k1, k2, k3);
      const u32 s0 = wave_max(k0), s1 = wave_max(k0 == s0 ? k1 : k0);
      const int tk = 2047 - (((lane & 1) ? s1 : s0) & 2047);
      v4u xf[8]; v2u st;
      if (ffn) {
#pragma unroll
        for (int ks = 0; ks < 8; ks++) xf[ks] = *(const GLB v4u*)((const GLB char*)x + fw * 512 + ks * 64 + (u32)(tk * 1024 + kg * 16));
        st = __builtin_amdgcn_raw_buffer_load_b64(rs, tk * 8, WS_S, 16);
      }
      STAMP(4);
      asm volatile("s_mov_b64 exec, 1\n\tds_write_b64 %0, %1 offset:%3\n\tds_or_b32 %0, %2 offset:%4\n\ts_mov_b64 exec, -1"
                   :: "v"(F), "v"((v2u){s0, s1}), "v"(256u), "n"(F_TS), "n"(F_SP) : "memory");
      if (ffn) {
        v4f g = {0.f, 0.f, 0.f, 0.f}, u = g;
#pragma unroll
        for (int ks = 0; ks < 8; ks++) { g = MF(fg[ks], BF8(xf[ks]), g); u = MF(fu[ks], BF8(xf[ks]), u); }
        SB();
        v8bf wdf[8];
        LDS char* dl = wl + (lane >> 2) * 64 + (lane & 3) * 8;
#pragma unroll
        for (int j = 0; j < 8; j++) wdf[j] = cat(trd(dl + j * 1024), trd(dl + j * 1024 + 32));
        v2u hz[8];
#pragma unroll
        for (int j = 0; j < 8; j++) hz[j] = (v2u){0, 0};
        asm volatile("" : "+v"(hz[0]), "+v"(hz[1]), "+v"(hz[2]), "+v"(hz[3]), "+v"(hz[4]), "+v"(hz[5]), "+v"(hz[6]), "+v"(hz[7]));
        _Pragma("unroll 1") for (int i = 0; i < SPIN; i++) {
          if (!__any(st[1] != tag)) break;
          MEMB();
          st = __builtin_amdgcn_raw_buffer_load_b64(rs, tk * 8, WS_S, 16);
        }
        const float mm = lo2f(st[0]), rsd = __builtin_amdgcn_rcpf(hi2f(st[0]));
        g -= sg * mm; u -= su * mm;
        LDS char* ex = F + F_E + (kg * 2 + (lane & 1)) * 32;
        if (c16 < 2) { *(LDS v4f*)(ex + fw * 256) = g; *(LDS v4f*)(ex + fw * 256 + 16) = u; }
        lds_sig(F + F_EX, 1u << fw);
        STAMP(5);
        v4f eg, eu;
        {
          u32 f, sc, cnt;
          asm volatile("s_mov_b32 %4, 0x20000\n"
                       "1:\n\tds_read_b32 %2, %5\n\tds_read_b128 %0, %6\n\tds_read_b128 %1, %6 offset:16\n\t"
                       "s_waitcnt lgkmcnt(2)\n\tv_readfirstlane_b32 %3, %2\n\ts_cmp_eq_u32 %3, 3\n\t"
                       "s_cbranch_scc1 2f\n\ts_sub_u32 %4, %4, 1\n\ts_cbranch_scc0 1b\n2:\n\ts_waitcnt lgkmcnt(0)"
                       : "=&v"(eg), "=&v"(eu), "=&v"(f), "=&s"(sc), "=&s"(cnt) : "v"(F + F_EX), "v"(ex + (fw ^ 1) * 256) : "memory", "scc");
        }
        g = (g + eg) * rsd; u = (u + eu) * rsd;
        float hh[4];
#pragma unroll
        for (int i = 0; i < 4; i++) hh[i] = g[i] * __builtin_amdgcn_rcpf(1.f + __builtin_amdgcn_exp2f(g[i] * -1.44269504089f)) * u[i];
        const v2u hf = {pk(hh[0], hh[1]), pk(hh[2], hh[3])};
#define HM(j) "v_mov_b64 %" #j ", %8\n\ts_lshl_b64 exec, exec, 2\n\t"
        asm volatile("s_mov_b64 exec, %9\n\t" HM(0) HM(1) HM(2) HM(3) HM(4) HM(5) HM(6) "v_mov_b64 %7, %8\n\ts_mov_b64 exec, -1\n\ts_nop 0"
                     : "+v"(hz[0]), "+v"(hz[1]), "+v"(hz[2]), "+v"(hz[3]), "+v"(hz[4]), "+v"(hz[5]), "+v"(hz[6]), "+v"(hz[7])
                     : "v"(hf), "s"(0x0003000300030003ull) : "scc");
        v4f e0 = {0.f, 0.f, 0.f, 0.f}, e1 = e0;
#pragma unroll
        for (int j = 0; j < 4; j++) {
          v8bf hb = BF8(((v4u){hz[2 * j][0], hz[2 * j][1], hz[2 * j + 1][0], hz[2 * j + 1][1]}));
          e0 = MF(wdf[j], hb, e0); e1 = MF(wdf[4 + j], hb, e1);
        }
        const u32 po = (c16 >> 1) * 128 + kg * 32 + (lane & 1) * 16;
        __builtin_amdgcn_raw_buffer_store_b128((v4u){pk(e0[0], e0[1]), pk(e0[2], e0[3]), tag, tag}, rs, po, WS_P + fs * 4096 + fw * 2048, 16);
        __builtin_amdgcn_raw_buffer_store_b128((v4u){pk(e1[0], e1[1]), pk(e1[2], e1[3]), tag, tag}, rs, po + 1024, WS_P + fs * 4096 + fw * 2048, 16);
        STAMP(6);
      } else if (b == 0 && lane == 0) *(GLB u32*)ws = tag;
    }
    __builtin_amdgcn_s_setprio(0);
    v4u sv;
    {
      int i = SPIN;
      bool bad;
      _Pragma("unroll 1") do {
        sv = __builtin_amdgcn_raw_buffer_load_b128(rs, WS_S + t0 * 8, 0, 16);
        bad = __any(((sv[1] ^ tag) | (sv[3] ^ tag)) != 0);
        MEMB();
      } while (bad && --i);
    }
    lds_wait<1>(F + F_U, 1u << s);
    v2f base[4];
    {
      const v2f m2 = {lo2f(sv[0]), lo2f(sv[2])}, sd2 = (v2f){hi2f(sv[0]), hi2f(sv[2])} * 16.f;
#pragma unroll
      for (int c = 0; c < 4; c++) {
        u32 uu = *(LDS u32*)(L + O_U + c * UP + 512 + t0 * 2), dd = dsk[c >> 1];
        float dk = ((c & 1) ? hi2f(dd) : lo2f(dd)) * 16.f + 16.f;
        base[c] = __builtin_elementwise_fma((v2f){lo2f(uu), hi2f(uu)}, sd2 + dk, m2);
      }
    }
    lds_wait<1>(F + F_SP, 256);
    const v2u ts = *(LDS v2u*)(F + F_TS);
    const int ts0 = 2047 - (ts[0] & 2047), ts1 = 2047 - (ts[1] & 2047);
    const int n = (w & 1) * 8 + (lane >> 3);
    u64 m0 = __builtin_amdgcn_ballot_w64(t0 != ts0 && t0 != ts1), m1 = __builtin_amdgcn_ballot_w64(t0 + 1 != ts0 && t0 + 1 != ts1);
    GLB char* op = (GLB char*)out + cg * 8;
    u32 oo = t0 * 1024;
    asm volatile("" : "+v"(oo), "+s"(op), "+s"(m0), "+s"(m1), "+v"(base[0]), "+v"(base[1]), "+v"(base[2]), "+v"(base[3]));
    if (!ffn) STAMP(5);
    v2f pr[8];
    {
      u32 f, sc, cnt;
      asm volatile("s_mov_b32 %10, 0x20000\n"
                   "1:\n\tds_read_b32 %8, %11\n\ts_waitcnt lgkmcnt(0)\n\tv_readfirstlane_b32 %9, %8\n\ts_and_b32 %9, %13, %9\n\t"
                   "s_cmp_gt_u32 %9, %14\n\ts_cbranch_scc1 3f\n\ts_sleep 1\n\ts_sub_u32 %10, %10, 1\n\ts_cbranch_scc0 1b\n"
                   "3:\n\tds_read_b32 %8, %11\n\tds_read_b64 %0, %12\n\tds_read_b64 %1, %12 offset:%15\n\tds_read_b64 %2, %12 offset:%15*2\n\t"
                   "ds_read_b64 %3, %12 offset:%15*3\n\tds_read_b64 %4, %12 offset:4096\n\tds_read_b64 %5, %12 offset:%15+4096\n\t"
                   "ds_read_b64 %6, %12 offset:%15*2+4096\n\tds_read_b64 %7, %12 offset:%15*3+4096\n\t"
                   "s_waitcnt lgkmcnt(8)\n\tv_readfirstlane_b32 %9, %8\n\ts_andn2_b32 %9, %13, %9\n\t"
                   "s_cbranch_scc0 2f\n\ts_waitcnt lgkmcnt(0)\n\ts_sub_u32 %10, %10, 1\n\ts_cbranch_scc0 3b\n2:\n\ts_setprio 3\n\ts_waitcnt lgkmcnt(0)"
                   : "=&v"(pr[0]), "=&v"(pr[1]), "=&v"(pr[2]), "=&v"(pr[3]), "=&v"(pr[4]), "=&v"(pr[5]), "=&v"(pr[6]), "=&v"(pr[7]),
                     "=&v"(f), "=&s"(sc), "=&s"(cnt)
                   : "v"(F + F_PH), "v"(L + si * 1024 + n * 64 + 16 * (((lane >> 1) & 3) ^ ((n >> 1) & 3)) + (lane & 1) * 8),
                     "s"(0xf000fu << (4 * si)), "s"(0xf0000u << (4 * si)), "n"(BC) : "memory", "scc");
    }
    if (!ffn) STAMP(6);
#pragma unroll
    for (int c = 0; c < 4; c++) base[c] += pr[c] + pr[4 + c];
    asm volatile("s_mov_b64 exec, %4\n\tglobal_store_dwordx2 %0, %1, %3\n\ts_mov_b64 exec, %5\n\tglobal_store_dwordx2 %0, %2, %3 offset:1024\n\ts_mov_b64 exec, -1"
                 :: "v"(oo), "v"((v2u){pk(base[0][0], base[1][0]), pk(base[2][0], base[3][0])}),
                    "v"((v2u){pk(base[0][1], base[1][1]), pk(base[2][1], base[3][1])}), "s"(op), "s"(m0), "s"(m1) : "memory");
    if (__builtin_expect((m0 & m1) != ~0ull, 0)) {
#pragma unroll
      for (int i = 0; i < 2; i++) {
        if (t0 + i == ts0 || t0 + i == ts1) {
          *(LDS v4f*)(F + F_SV + (t0 + i == ts0 ? 0 : 16)) = (v4f){base[0][i], base[1][i], base[2][i], base[3][i]};
          asm volatile("ds_or_b32 %0, %1" :: "v"(F + F_SP), "v"(t0 + i == ts0 ? 1u : 2u) : "memory");
        }
      }
    }
    STAMP(7);
    FLUSH();
    asm volatile("s_endpgm" ::: "memory");
  }
  {
    asm volatile("" : "={v[12:15]}"(xr), "={v[16:19]}"(wv), "={s[40:41]}"(dsk));
    SB();
    asm volatile("s_barrier" ::: "memory");
    u64 pfb; u32 pfo;
    {
      const int g = b & 7, i = b >> 3;
      pfb = w == 8 ? (u64)wg : w == 9 ? (u64)wu : w == 10 ? (u64)wd : pc;
      const u32 o = w < 10 ? i * 16 * 3584 + ((448 * g) & ~127) : w == 10 ? (224 * g + 7 * i) * 1024 : (i & 3) * 4096;
      pfo = min(o + (lane >> 2) * (w < 10 ? 3584 : w == 10 ? 512 : 256) + (lane & 3) * (w < 11 ? 128 : 64), w < 10 ? ~0u : w == 10 ? o + 55 * 128 : CODE_SZ);
      asm volatile("s_cmp_ge_u32 %3, %4\n\ts_cbranch_scc1 1f\n\ts_mov_b32 m0, %2\n\ts_nop 0\n\tglobal_load_lds_dword %0, %1\n1:"
                   :: "v"(pfo), "s"(pfb), "n"(LDS_SZ - 256), "s"(PF_LATE ? w - 11 : w - 8), "n"(PF_LATE ? 1 : 4) : "memory", "scc");
    }
    LDS char* const Bc = L + ch * BC;
    LDS char* const Ac = L + O_A + ch * AC;
    LDS char* const Uc = L + O_U + ch * UP;
    const u32 tb = half ? (c16 < 8 ? 0x4f32f10fu : 0xfffff765u) : (c16 < 8 ? 0x76543210u : 0xcbaaa988u);
    const u32 ar = (tb >> ((c16 & 7) * 4)) & 15;
    LDS char* pa = (ar == 15 ? Uc : Ac + half * A_HI + ar * RP + (kg & 1) * AH) + (kg >> 1) * 16;
    LDS char* pb = Bc + c16 * RP + (kg & 1) * (BC / 2) + (kg >> 1) * 16;
    const int be = c16 + 16 - 8 * kg;
    v4u b35 = band<3, 5>(be, 0x3f80), b6a = band<6, 10>(be, 0x3f80), b5b = band<5, 11>(be, 0x3f80), b4c = band<4, 12>(be, 0x3f80), b34 = band<3, 4>(be, 0xffff);
    asm volatile("" : "+v"(pa), "+v"(pb), "+v"(b35), "+v"(b6a), "+v"(b5b), "+v"(b4c), "+v"(b34));
    {
      asm volatile("s_waitcnt lgkmcnt(0)" : "+s"(dsk) :: "memory");
      const u32 ldt = dsk[ch >> 1];
      const float dt = rbf(__builtin_amdgcn_exp2f(((ch & 1) ? hi2f(ldt) : lo2f(ldt)) * 1.44269504089f));
      float d1 = dt * 1.44269504089f, d2 = dt * 0.15915494309f, d16 = dt * 16.f;
      asm volatile("s_cmp_lt_u32 %6, %7\n\ts_cbranch_scc1 1f\n\ts_waitcnt vmcnt(0)\n1:\n\ts_waitcnt vmcnt(1)\n\t"
                   "s_mov_b32 m0, %5\n\ts_nop 0"
                   : "+v"(xr), "+v"(wv), "+v"(d1), "+v"(d2), "+v"(d16)
                   : "s"(ch * BC + half * (BC / 2)), "s"(PF_LATE ? w - 11 : w - 8), "n"(PF_LATE ? 1 : 4), "s"(w), "v"(pfo), "s"(pfb), "n"(LDS_SZ - 256) : "memory", "scc");
      STAMP(1);
      const float er = d1 * lo2f(xr[0]), ph = d2 * lo2f(xr[1]);
      const v2f z = cpow(er, ph), E = cpow(er * 256.f, ph * 256.f);
      const v2f z2 = cmul(z, z), cb = cmul((v2f){lo2f(wv[0]), lo2f(wv[1])}, (v2f){lo2f(xr[2]), lo2f(xr[3])} * d16);
      const v2f z4 = cmul(z2, z2), z8 = cmul(z4, z4);
      v2f o = {z8.x, -z8.y};
      const v2f m = cmul(o, o);
      v2f e = m;
      put1<0>(0x3f80u); put1<16 * RP>(pk(o.x, o.y)); put1<RP>(pk(e.x, e.y));
      brows<2>(e, o, m);
      v2f x1, y, c0, c1;
      step2<31 * RP, 31 * RP>(x1, cb, E, o, o, m);
      asm volatile("s_mov_b32 m0, %0\n\ts_nop 0" :: "s"(20000 + ch * AC + half * AH) : "memory");
#define AL(r) (AOFF + (r) * RP)
#define AU(r) (AOFF + A_HI + (r) * RP)
      put1<AL(0)>(pk(cb.x, cb.y)); put1<AL(8)>(pk(x1.x, x1.y));
      step2<AL(1), AL(12)>(c1, cb, z, y, x1, z8);
      step2<AL(2), AU(2)>(c0, cb, z2, y, y, E);
      step2<AL(3), AU(3)>(c1, c1, z2, y, y, E);
      step2<AL(4), AU(4)>(c0, c0, z2, y, y, E);
      step2<AL(5), AU(5)>(c1, c1, z2, y, y, E);
      step2<AL(6), AU(6)>(c0, c0, z2, y, y, E);
      step2<AL(9), AL(10)>(c0, x1, z2, e, x1, z4);
      step2<AL(7), AU(7)>(c1, c1, z2, y, y, E);
      step2<AL(11), AU(0)>(c0, e, z2, e, e, E);
      e = cmul(e, E); put1<AU(1)>(pk(e.x, e.y));
    }
    lds_sig(F + F_X, 1u << (w - 8));
    STAMP(2);
    lds_wait<1>(F + F_X, 17u << ch);
    v4f e0 = {0.f, 0.f, 0.f, 0.f}, e1 = e0;
    {
      v8bf fa[4], f0[4], f1[4];
#pragma unroll
      for (int k = 0; k < 4; k++) { fa[k] = *(LDS v8bf*)(pa + k * 32); f0[k] = *(LDS v8bf*)(pb + k * 32); f1[k] = *(LDS v8bf*)(pb + 16 * RP + k * 32); }
      SB();
#pragma unroll
      for (int k = 0; k < 4; k++) {
        e0 = MF(fa[k], f0[k], e0); e1 = MF(fa[k], f1[k], e1);
        fa[k] = *(LDS v8bf*)(pa + 128 + k * 32); f0[k] = *(LDS v8bf*)(pb + 128 + k * 32); f1[k] = *(LDS v8bf*)(pb + 16 * RP + 128 + k * 32);
        SB();
      }
      lds_sig(F + F_U, 256u << (w - 8));
#pragma unroll
      for (int k = 0; k < 4; k++) { e0 = MF(fa[k], f0[k], e0); e1 = MF(fa[k], f1[k], e1); }
    }
    LDS char* const pw = Bc + half * 4096 + c16 * 64 + 16 * (kg ^ ((c16 >> 1) & 3));
    const u32 umask = (h ? 63 : 255) | 0x1100u << ch;
    if (!half) {
      LDS char* const K0 = Ac + 32;
      LDS char* const cpy = Ac + (kg >> 1) * (AH + 128) + (kg & 1) * 1344;
      const v2u w0 = {pk(e0[0], e0[1]), pk(e0[2], e0[3])}, w1 = {pk(e1[0], e1[1]), pk(e1[2], e1[3])};
      LDS char* q = K0 + (kg >> 1) * 512 + c16 * 32 + (kg & 1) * 8;
      *(LDS v2u*)q = w0;
      *(LDS v2u*)(q + 16) = w1;
      *(LDS u32*)(cpy + (c16 & 7) * 4) = 0;
      MEMB();
      LDS char* p = K0 + lane * 16;
      const v4u un = *(LDS v4u*)p; const v2u pv = *(LDS v2u*)(p - 8);
      {
        const int i0 = 2 * c16 - kg;
        const u32 sl = i0 > 2 ? 0x0c0cu : i0 == -3 ? 0x0302u : i0 == -2 ? 0x0504u : 0x0706u, sh = i0 > 1 ? 0x0c0cu : i0 == -3 ? 0x0504u : 0x0706u;
        *(LDS u32*)(cpy + 1056 + c16 * 4) = __builtin_amdgcn_perm(__builtin_amdgcn_readlane(w1[1], 63), __builtin_amdgcn_readlane(w1[0], 63), sl | sh << 16);
      }
      const u32 a0 = __builtin_amdgcn_alignbit(pv[1], pv[0], 16), a1 = __builtin_amdgcn_alignbit(un[0], pv[1], 16),
                a2 = __builtin_amdgcn_alignbit(un[1], un[0], 16), a3 = __builtin_amdgcn_alignbit(un[2], un[1], 16),
                a4 = __builtin_amdgcn_alignbit(un[3], un[2], 16);
      *(LDS v4u*)(p + 1344) = (v4u){a1, a2, a3, a4};
      *(LDS v4u*)(p + AH + 128) = (v4u){pv[1], un[0], un[1], un[2]};
      *(LDS v4u*)(p + AH + 128 + 1344) = (v4u){a0, a1, a2, a3};
      STAMP(3);
      u32 c0, c1, si = 0;
      asm volatile(
        LA8(40, 512) LA8(8, 0) LA(72, 0, 576)
        "v_mov_b32 v4, 0\n\tv_mov_b64 v[112:113], 0\n\tv_mov_b64 v[114:115], 0\n\ts_setprio 2\n\ts_mov_b32 %1, 0x20000\n" ST_(4)
        "1:\n\tds_read_b32 v5, v3\n\ts_waitcnt lgkmcnt(0)\n\tv_readfirstlane_b32 %0, v5\n\ts_andn2_b32 %0, %[msk], %0\n\t"
        "s_cbranch_scc0 2f\n\ts_sub_u32 %1, %1, 1\n\ts_cbranch_scc0 1b\n2:\n\t" ST_(5)
        LB8
        "s_cmp_eq_u32 %[h], 0\n\ts_cbranch_scc1 4f\n\ts_branch 6f\n"
        "3:\n\ts_waitcnt lgkmcnt(8)\n\tv_mfma_f32_16x16x32_bf16 " AX ", v[72:75], v[108:111], " AX "\n"
        "4:\n\t" H0(0, AX) H0(1, AX) H0(2, AX) H0(3, AX) H0(4, AX) H0(5, AX) H0(6, AX) H0(7, AY)
        "v_add_u32 v2, %0, v2\n\ts_movk_i32 %0, 0x400\n\tv_mov_b32 v4, %[bit]\n\ts_lshl_b32 %[bit], %[bit], 4\n\t"
        "s_add_u32 %2, %2, 1\n\ts_cmp_eq_u32 %2, 4\n\ts_cbranch_scc1 7f\n\t"
        "s_cmp_lg_u32 %2, %[jmp]\n\ts_cbranch_scc1 6f\n\t"
        "v_add_u32 v1, 0x800, v1\n\t" LB8
        "6:\n\tv_add_u32 v1, 0x200, v1\n\tds_read_b128 v[108:111], v1 offset:448\n\t"
        H1(0, "0") H1(1, AX) PUB H1(2, AX) H1(3, AX) H1(4, AX) H1(5, AX) H1(6, AX) H1(7, AX)
        "s_cmp_lg_u32 %2, %[one]\n\ts_cbranch_scc1 3b\n\ts_branch 4b\n"
        "7:\n\ts_nop 2\n\t" PUB ST_(6) FLUSH_
        "s_waitcnt lgkmcnt(0)\n\ts_endpgm"
        : "=&s"(c0), "=&s"(c1), "+s"(si)
        : "{v0}"(Ac + ((lane >> 3) & 1) * (AH + 128) + ((lane >> 2) & 1) * 1344 + 56 - kg * 16 + (lane & 3) * 8),
          "{v1}"(Uc + 32 - 1024 + h * 512 + c16 * 32 + kg * 16), "{v2}"(pw), "{v3}"(F + F_U), [msk] "s"(umask), [bit] "s"(1u << ch),
          [h] "s"(h), [jmp] "s"(h ? 9 : 2), [one] "s"(h ? 9 : 1)
        : "memory", "scc", "v127");
    }
    {
      LDS char* const T = Ac + A_HI;
      {
        LDS u32* q = (LDS u32*)(T + kg * 256 + c16 * 4);
        q[0] = pk(e0[0], e1[0]); q[16] = pk(e0[1], e1[1]); q[32] = pk(e0[2], e1[2]); q[48] = pk(e0[3], e1[3]);
      }
      MEMB();
      LDS char* tp = T + (c16 - 4 * kg - 3) * 4;
      v8bf ta[3], tb4[3], t8[2];
      {
        v2u q[16]; u32 kk;
        asm volatile(TB(0, 1, 1) TB(2, 3, 2) TB(4, 5, 3) TB(6, 7, 4) TB(8, 9, 5) TB(10, 11, 6) TB(12, 13, 7) TB(14, 15, 8)
                     "ds_read_b32 %16, %[t] offset:380\n\ts_waitcnt lgkmcnt(0)"
                     : "=&v"(q[0]), "=&v"(q[1]), "=&v"(q[2]), "=&v"(q[3]), "=&v"(q[4]), "=&v"(q[5]), "=&v"(q[6]), "=&v"(q[7]),
                       "=&v"(q[8]), "=&v"(q[9]), "=&v"(q[10]), "=&v"(q[11]), "=&v"(q[12]), "=&v"(q[13]), "=&v"(q[14]), "=&v"(q[15]), "=&v"(kk)
                     : [p] "v"(tp), [t] "v"(T) : "memory");
#pragma unroll
        for (int i = 0; i < 3; i++) { ta[i] = cat2(q[2 * i], q[2 * i + 1]); tb4[i] = cat2(q[6 + 2 * i], q[7 + 2 * i]); }
        t8[0] = cat2(q[12], q[13]); t8[1] = cat2(q[14], q[15]);
        kk = __builtin_amdgcn_perm(kk, kk, 0x03020302u);
#pragma unroll
        for (int j = 0; j < 4; j++) b34[j] &= kk;
      }
      asm volatile("" : "+v"(ta[0]), "+v"(ta[1]), "+v"(ta[2]), "+v"(tb4[0]), "+v"(tb4[1]), "+v"(tb4[2]), "+v"(t8[0]), "+v"(t8[1]), "+v"(b34));
      LDS char* pu = Uc + 512 - 48 + (h ? 0 : 1024) + c16 * 32 + kg * 16;
      __builtin_amdgcn_s_setprio(2);
      STAMP(3);
      lds_wait<0>(F + F_U, umask);
      STAMP(4);
      const v4f zz = {0.f, 0.f, 0.f, 0.f};
      v8bf ua[4], ub[4];
#pragma unroll
      for (int a = 0; a < 4; a++) { ua[a] = *(LDS v8bf*)(pu + a * 512 + 16); ub[a] = *(LDS v8bf*)(pu + a * 512); }
      v4f r0 = zz, r1 = zz, r2 = zz, r3 = zz;
      v8bf wa0 = wop(ua[0], ub[0], b35, b35), wb0 = wop(ua[0], ub[0], b6a, b6a);
      v8bf wa1 = wop(ua[1], ub[1], b35, b35), wb1 = wop(ua[1], ub[1], b6a, b6a);
      if (h) {
        r0 = MF(wa0, ta[0], r0); r1 = MF(wa0, ta[1], r1); r0 = MF(wb0, tb4[0], r0); r1 = MF(wb0, tb4[1], r1);
        r1 = MF(wa1, ta[0], r1); r1 = MF(wb1, tb4[0], r1);
      }
      r2 = MF(wa0, ta[2], r2); r3 = MF(wa1, ta[2], r3); r2 = MF(wb0, tb4[2], r2); r3 = MF(wb1, tb4[2], r3);
      *(LDS v4f*)pw = r0;
      *(LDS v4f*)(pw + 1024) = r1;
      lds_sig(F + F_PH, 0x110000u << ch);
      r2 = MF(wa1, ta[1], r2); r2 = MF(wb1, tb4[1], r2);
      {
        v8bf wa2 = wop(ua[2], ub[2], b35, b35), wb2 = wop(ua[2], ub[2], b6a, b6a), w80 = wop(ua[0], ub[0], b5b, b4c);
        r2 = MF(BF8(b34), ua[0], r2); r3 = MF(BF8(b34), ua[1], r3);
        r2 = MF(wa2, ta[0], r2); r3 = MF(wa2, ta[1], r3); r2 = MF(wb2, tb4[0], r2); r3 = MF(wb2, tb4[1], r3);
        r2 = MF(w80, t8[0], r2); r3 = MF(w80, t8[1], r3);
      }
      if (!h) {
        v8bf va0 = *(LDS v8bf*)(pu - 1024 + 16), vb0 = *(LDS v8bf*)(pu - 1024), va1 = *(LDS v8bf*)(pu - 512 + 16), vb1 = *(LDS v8bf*)(pu - 512);
        v2u q[4];
        asm volatile(TB(0, 1, 9) TB(2, 3, 10) "s_waitcnt lgkmcnt(0)" : "=&v"(q[0]), "=&v"(q[1]), "=&v"(q[2]), "=&v"(q[3]) : [p] "v"(tp) : "memory");
        const v8bf t2 = cat2(q[0], q[1]), t3 = cat2(q[2], q[3]);
        v8bf x0 = wop(va0, vb0, b5b, b4c), x1 = wop(va1, vb1, b5b, b4c);
        r2 = MF(x0, t2, r2); r3 = MF(x0, t3, r3); r2 = MF(x1, t8[1], r2); r3 = MF(x1, t2, r3);
      }
      {
        v8bf wa3 = wop(ua[3], ub[3], b35, b35), wb3 = wop(ua[3], ub[3], b6a, b6a), w81 = wop(ua[1], ub[1], b5b, b4c);
        *(LDS v4f*)(pw + 2048) = r2;
        lds_sig(F + F_PH, 0x1000000u << ch);
        r3 = MF(wa3, ta[0], r3); r3 = MF(wb3, tb4[0], r3); r3 = MF(w81, t8[0], r3);
      }
      *(LDS v4f*)(pw + 3072) = r3;
      lds_sig(F + F_PH, 0x10000000u << ch);
      STAMP(5);
      if (ch == 0) {
        __builtin_amdgcn_s_setprio(3);
        lds_wait<1>(F + F_SP, 256);
        const v2u ts = *(LDS v2u*)(F + F_TS);
        const u32 tag = *(LDS u32*)(F + F_TAG);
        const u32 s0 = __builtin_amdgcn_readfirstlane(ts[0]), s1 = __builtin_amdgcn_readfirstlane(ts[1]);
        const int ts0 = 2047 - (s0 & 2047), ts1 = 2047 - (s1 & 2047);
        const u32 own = h ? 0x3Cu : 0xC3u;
        const u32 need = ((own >> (ts0 >> 8)) & 1) | ((own >> (ts1 >> 8)) & 1) << 1;
        if (need) {
          const u32 kk = (lane & 1) ? s1 : s0;
          const float r = __uint_as_float((kk ^ 0x80000000u ^ ((~(int)kk >> 31) & 0x7fff0000u)) & 0xffff0000u);
          float sgm = rbf(__builtin_amdgcn_rcpf(1.f + __builtin_amdgcn_exp2f(r * -1.44269504089f)));
          u32 c_lo = 0x3f80u, c_hi = 0x3f800000u;
          asm volatile("" : "+v"(sgm), "+s"(c_lo), "+s"(c_hi));
          __builtin_amdgcn_s_sleep(D_Q);
          const u32 o0 = (lane >> 1) * 4096 + (lane & 1) * 16, o3 = o0 + (lane < 32 ? 96 * 4096 : 0);
          v4u a[4];
          int i = SPIN;
          bool bad;
          _Pragma("unroll 1") do {
#pragma unroll
            for (int j = 0; j < 4; j++) a[j] = __builtin_amdgcn_raw_buffer_load_b128(rs, j < 3 ? o0 : o3, WS_P + cg * 32 + (j < 3 ? j * 32 * 4096 : 0), 16);
            bad = __any(((a[0][2] ^ tag) | (a[1][2] ^ tag) | (a[2][2] ^ tag) | (a[3][2] ^ tag)) != 0);
            MEMB();
          } while (bad && --i);
          STAMP(6);
          if (lane >= 32) { a[3][0] = 0; a[3][1] = 0; }
          float y[4] = {0.f, 0.f, 0.f, 0.f};
#pragma unroll
          for (int j = 0; j < 4; j++) {
            y[0] = dot2(a[j][0], c_lo, y[0]); y[1] = dot2(a[j][0], c_hi, y[1]);
            y[2] = dot2(a[j][1], c_lo, y[2]); y[3] = dot2(a[j][1], c_hi, y[3]);
          }
          float f4 = fold4(y[0], y[2], y[1], y[3]);
          f4 += dppf<0x4E>(f4); f4 += dppf<0x124>(f4); f4 += dppf<0x128>(f4);
          const float up = rbf(f4) * sgm;
          lds_wait<0>(F + F_SP, need);
          if (c16 < 2 && ((need >> c16) & 1))
            out[(c16 ? ts1 : ts0) * 512 + cg * 4 + kg] = (u16)pk(*(LDS float*)(F + F_SV + c16 * 16 + kg * 4) + up, 0.f);
        }
      }
      STAMP(7);
      FLUSH();
    }
  }
}

extern "C" void kernel_launch(void* const* d_in, const int* in_sizes, int n_in, void* d_out, int out_size, void* d_ws,
                              size_t ws_size, hipStream_t stream) {
  kf<<<256, 1024, 0, stream>>>((const u16*)d_in[0], (const u16*)d_in[10], (char*)d_ws, (const u16*)d_in[8], (u16*)d_out,
                               (const u16*)d_in[7], (const u16*)d_in[1], (const u16*)d_in[2], (const u16*)d_in[3], (const u16*)d_in[4],
                               (const u16*)d_in[5], (const u16*)d_in[6], (const u16*)d_in[11], (const u16*)d_in[12], (const u16*)d_in[13]);
}
```

```cpp
#define DBG_STAMP
#define PI 1
#define PMODE 2
#define PX 255
#define PWO 4
#define PW 1
#include <hip/hip_runtime.h>

typedef unsigned short u16;
typedef unsigned u32;
typedef unsigned long long u64;
typedef u32 v4u __attribute__((ext_vector_type(4)));
typedef u32 v2u __attribute__((ext_vector_type(2)));
typedef float v4f __attribute__((ext_vector_type(4)));
typedef float v2f __attribute__((ext_vector_type(2)));
typedef __bf16 v8bf __attribute__((ext_vector_type(8)));
typedef __bf16 v2bf __attribute__((ext_vector_type(2)));
typedef float v16f __attribute__((ext_vector_type(16)));
typedef short v2s __attribute__((ext_vector_type(2)));
typedef short v4s __attribute__((ext_vector_type(4)));
#define LDS __attribute__((address_space(3)))
#define GLB __attribute__((address_space(1)))
#define CST __attribute__((address_space(4)))
#define DEVI __device__ __forceinline__
#define SB() __builtin_amdgcn_sched_barrier(0)
#define MEMB() asm volatile("" ::: "memory")

#define WS_R 4096
#define WS_S 8192
#define WS_P 32768
#define WS_X (1u << 20)
#define WS_END (4u << 20)
#define PS 4096
#define PT 1
#define PG 3712
#define BS 1
#define GL 14
#define HW 5
#define EB 7
#define PM 2
#define PM2 2
#define PF 1
#define XS 32896
#define NQ 1
#define SPIN 4000
#define GS 0
#define D_G 2
#define XL 0
#define D_AG 30
#define D_KG 30
#define D_P 8
#define D_R 6
#define CODE_SZ 15788
#define D_K2 6
#define D_D 10
#define D_QG 0
#define D_A 20
#define D_W 4
#define D_K 30
#define D_Q 34
#define D_Q2 6
#define W_Q 256
#define D_M 8

#define WS_STAMP (16u << 20)
#define ISG(b) (((b) & 8) && ((b) >> 4) < 14)
#define GROUP(b, w) ((w) >= 12 ? 2 : (w) >= 8 ? 1 : ((w) >= 6 && ((b) & 8) && ((b) >> 4) < 14) ? 3 : (w) == 4 ? (((b) & 8) && ((b) >> 4) < 14 ? 4 : 5) : (w) == 5 && ((b) & 8) && ((b) >> 4) < 14 ? 6 : (w) > 4 ? 9 : ((b) & 8) && ((b) >> 4) < 14 ? 7 : 0)
#define ST_(i) "s_memrealtime s[84+2*" #i ":85+2*" #i "]\n\t"
#define STAMP(i) asm volatile(ST_(i) ::: "memory", "s99")
#define STAMPD(i, v) asm volatile(ST_(i) :: "v"(v) : "memory", "s99")
#define LG12 "12"
#define STAMPQ(i) asm volatile("s_memrealtime s[84+2*(%0):85+2*(%0)]" :: "n"(W_Q == 256 ? 2 * (i) - 4 : (i)) : "memory", "s99")
#define FL_(i) "v_mov_b32 v8, s[84+4*" #i "]\n\tv_mov_b32 v9, s[85+4*" #i "]\n\tv_mov_b32 v10, s[86+4*" #i "]\n\tv_mov_b32 v11, s[87+4*" #i "]\n\t" \
               "global_store_dwordx4 v[6:7], v[8:11], off offset:16*" #i "\n\ts_nop 1\n\t"
#define FLUSH_ "s_mov_b64 exec, 1\n\ts_waitcnt lgkmcnt(0)\n\t" FL_(0) FL_(1) FL_(2) FL_(3)
#define STADDR "{v[6:7]}"((u64*)(ws + WS_STAMP) + (blockIdx.x * 16 + w) * 8)
#define FLUSH() asm volatile(FLUSH_ "s_endpgm" :: STADDR : "memory", "v8", "v9", "v10", "v11")

DEVI u32 pk(float a, float b) { return __builtin_bit_cast(u32, __builtin_convertvector((v2f){a, b}, v2bf)); }
DEVI float opq(float a) { asm("" : "+v"(a)); return a; }
DEVI u32 pks(float a, float b) { return pk(opq(a), opq(b)); }
DEVI float rbf(float a) { return __uint_as_float(pk(0.f, a)); }
DEVI float lo2f(u32 v) { return __uint_as_float(v << 16); }
DEVI float hi2f(u32 v) { return __uint_as_float(v & 0xffff0000u); }
DEVI float dot2(u32 a, u32 b, float c) { return __builtin_amdgcn_fdot2_f32_bf16(__builtin_bit_cast(v2bf, a), __builtin_bit_cast(v2bf, b), c, false); }
template <int CTRL> DEVI float dppf(float v) { return __int_as_float(__builtin_amdgcn_update_dpp(0, __float_as_int(v), CTRL, 0xf, 0xf, true)); }
template <int CTRL, int RM> DEVI u32 dppu(u32 v) { return (u32)__builtin_amdgcn_update_dpp(0, (int)v, CTRL, RM, 0xf, false); }
DEVI float rdl(float v, int l) { return __int_as_float(__builtin_amdgcn_readlane(__float_as_int(v), l)); }
DEVI float fold4(float a, float b, float c, float d) {
  v2u p = __builtin_amdgcn_permlane32_swap(__float_as_uint(a), __float_as_uint(b), false, false);
  v2u q = __builtin_amdgcn_permlane32_swap(__float_as_uint(c), __float_as_uint(d), false, false);
  float t = __uint_as_float(p[0]) + __uint_as_float(p[1]), s = __uint_as_float(q[0]) + __uint_as_float(q[1]);
  v2u r = __builtin_amdgcn_permlane16_swap(__float_as_uint(t), __float_as_uint(s), false, false);
  return __uint_as_float(r[0]) + __uint_as_float(r[1]);
}
DEVI u32 wave_max(u32 v) {
  v = max(v, dppu<0xB1, 0xf>(v)); v = max(v, dppu<0x4E, 0xf>(v)); v = max(v, dppu<0x141, 0xf>(v)); v = max(v, dppu<0x140, 0xf>(v));
  v = max(v, dppu<0x142, 0xa>(v)); v = max(v, dppu<0x143, 0xc>(v));
  return (u32)__builtin_amdgcn_readlane((int)v, 63);
}
DEVI v4s trd(LDS char* p) { return __builtin_amdgcn_ds_read_tr16_b64_v4i16((LDS v4s*)p); }
DEVI v8bf cat(v4s a, v4s b) { return __builtin_bit_cast(v8bf, __builtin_shufflevector(a, b, 0, 1, 2, 3, 4, 5, 6, 7)); }
template <int SL> DEVI void lds_wait(LDS char* p, u32 m) {
  u32 v, s, c;
  asm volatile("s_mov_b32 %2, 0x20000\n"
               "1:\n\tds_read_b32 %0, %3\n\ts_waitcnt lgkmcnt(0)\n\tv_readfirstlane_b32 %1, %0\n\ts_andn2_b32 %1, %4, %1\n\t"
               "s_cbranch_scc0 2f\n\ts_sleep %5\n\ts_sub_u32 %2, %2, 1\n\ts_cbranch_scc0 1b\n2:"
               : "=&v"(v), "=&s"(s), "=&s"(c) : "v"(p), "s"(m), "n"(SL) : "memory", "scc");
}
DEVI void lds_sig(LDS char* p, u32 bit) {
  asm volatile("s_mov_b64 exec, 1\n\tds_or_b32 %0, %1\n\ts_mov_b64 exec, -1" :: "v"(p), "v"(bit) : "memory");
}
DEVI void merge2(u32& a0, u32& a1, u32 b0, u32 b1) { u32 m = min(a0, b0); a0 = max(a0, b0); a1 = max(m, max(a1, b1)); }
DEVI v2f cpow(float er, float ph) {
  float e = __builtin_amdgcn_exp2f(er), f = __builtin_amdgcn_fractf(ph);
  return (v2f){__builtin_amdgcn_cosf(f), __builtin_amdgcn_sinf(f)} * e;
}

#define RP 272
#define BC 17408
#define O_A 69632
#define AC 11776
#define AH 5888
#define A_HI 3584
#define A_UP 3616
#define O_U (O_A + 4 * AC)
#define UP 4640
#define O_F (O_U + 4 * UP)
#define O_LN (O_F + 768)
#define O_W (O_LN + 8704)
#define F_KS 0
#define F_U 64
#define F_PH 68
#define F_EX 72
#define F_SP 76
#define F_TS 80
#define F_PB 88
#define F_X 92
#define F_LN 96
#define F_ST 104
#define F_SV 128
#define F_UP 160
#define F_E 256
#define O_XR (O_W + 16384)
#define LDS_SZ (O_XR + 2688)
#define AOFF 49632

#define CM1(t, x, m) "v_pk_mul_f32 " t ", " x ", " m " op_sel_hi:[0,1]\n\t"
#define CM2(r, t, x, m) "v_pk_fma_f32 " r ", " x ", " m ", " t " op_sel:[1,1,0] op_sel_hi:[1,0,1] neg_lo:[0,1,0]\n\t"
DEVI v2f cmul(v2f x, v2f m) {
  v2f t, r;
  asm(CM1("%1", "%2", "%3") CM2("%0", "%1", "%2", "%3") : "=v"(r), "=&v"(t) : "v"(x), "v"(m));
  return r;
}
template <int O1, int O2> DEVI void step2(v2f& r1, v2f x1, v2f m1, v2f& r2, v2f x2, v2f m2) {
  v2f t1, t2;
  asm volatile(CM1("%2", "%4", "%5") CM1("%3", "%6", "%7") CM2("%0", "%2", "%4", "%5") CM2("%1", "%3", "%6", "%7")
               : "=&v"(r1), "=v"(r2), "=&v"(t1), "=&v"(t2) : "v"(x1), "v"(m1), "v"(x2), "v"(m2));
  u32 p1 = pk(r1.x, r1.y), p2 = pk(r2.x, r2.y);
  asm volatile("ds_write_addtid_b32 %0 offset:%2\n\tds_write_addtid_b32 %1 offset:%3" :: "v"(p1), "v"(p2), "n"(O1), "n"(O2) : "memory");
}
template <int O1> DEVI void put1(u32 p) { asm volatile("ds_write_addtid_b32 %0 offset:%1" :: "v"(p), "n"(O1) : "memory"); }
template <int J> DEVI void brows(v2f& e, v2f& o, v2f m) {
  if constexpr (J < 16) { step2<2 * J * RP, (2 * J - 1) * RP>(e, e, m, o, o, m); brows<J + 1>(e, o, m); }
}
#define MF(a, b, c) __builtin_amdgcn_mfma_f32_16x16x32_bf16(a, b, c, 0, 0, 0)
#define BF8(x) __builtin_bit_cast(v8bf, x)
template <int LO, int HI> DEVI v4u band(const u32* n, u32 one) {
  v4u r;
#pragma unroll
  for (int j = 0; j < 4; j++) r[j] = __builtin_amdgcn_bitop3_b32(n[LO - 3 + 2 * j], n[HI - 2 + 2 * j], one, 0x08);
  return r;
}
DEVI v8bf wop(v8bf a, v8bf b, v4u ba, v4u bb) {
  const v4f zz = {0.f, 0.f, 0.f, 0.f};
  v4f p = MF(a, BF8(ba), zz), q = MF(b, BF8(bb), zz);
  return BF8(((v4u){pk(p[0], q[0]), pk(p[1], q[1]), pk(p[2], q[2]), pk(p[3], q[3])}));
}
#define TB(a, b, s) "ds_read2_b32 %" #a ", %[p] offset0:16*" #s "+3 offset1:16*" #s "+2\n\tds_read2_b32 %" #b ", %[p] offset0:16*" #s "+1 offset1:16*" #s "\n\t"
DEVI v8bf cat2(v2u a, v2u b) { return BF8(__builtin_shufflevector(a, b, 0, 1, 2, 3)); }

#define VR(b, j) "v[" #b "+4*" #j ":" #b "+3+4*" #j "]"
#define LA(b, j, o) "ds_read_b64_tr_b16 v[" #b "+4*" #j ":" #b "+1+4*" #j "], v0 offset:" #o "+456-64*" #j "\n\t" \
                    "ds_read_b64_tr_b16 v[" #b "+2+4*" #j ":" #b "+3+4*" #j "], v0 offset:" #o "+448-64*" #j "\n\t"
#define LA8(b, o) LA(b, 0, o) LA(b, 1, o) LA(b, 2, o) LA(b, 3, o) LA(b, 4, o) LA(b, 5, o) LA(b, 6, o) LA(b, 7, o)
#define LB(j) "ds_read_b128 " VR(76, j) ", v1 offset:1024+64*" #j "\n\t"
#define LB8 LB(0) LB(1) LB(2) LB(3) LB(4) LB(5) LB(6) LB(7)
#define AX "v[112:115]"
#define AY "v[116:119]"
#define MA(d, a, j, c) "v_mfma_f32_16x16x32_bf16 " d ", " VR(a, j) ", " VR(76, j) ", " c "\n\t"
#define H0(j, d) "s_waitcnt lgkmcnt(7-" #j ")\n\t" MA(d, 8, j, AX)
#define H1(j, c) "s_waitcnt lgkmcnt(8)\n\t" MA(AX, 40, j, c) LB(j)
#define PUB "ds_write_b128 v2, " AY "\n\ts_mov_b64 exec, 1\n\tds_or_b32 v3, v4 offset:4\n\ts_mov_b64 exec, -1\n\t"
#define WG_ "global_load_lds_dwordx4 %[go], %[wg]\n\ts_add_u32 m0, m0, 0x400\n\tv_add_u32 %[go], 0x1c000, %[go]\n\t"
#define WU(j) "global_load_dwordx4 %" #j ", %[gu], %[wu]\n\tv_add_u32 %[gu], 0x1c000, %[gu]\n\t"
#define WD(j) "global_load_lds_dwordx4 %[dd], %[wd] offset:64*" #j "\n\ts_add_u32 m0, m0, 960\n\tglobal_load_dwordx4 %" #j ", %[dd], %[wd] offset:512+64*" #j "\n\t"
#define XR(j) "ds_read_b128 v[32+4*" #j ":35+4*" #j "], %[x] offset:64*" #j "\n\t"
#define XM(j, a, b, c, d) "s_waitcnt lgkmcnt(7-" #j ")\n\tv_mfma_f32_16x16x32_bf16 v[64:67], %" #a ", v[32+4*" #j ":35+4*" #j "], " c "\n\t" \
                          "v_mfma_f32_16x16x32_bf16 v[68:71], %" #b ", v[32+4*" #j ":35+4*" #j "], " d "\n\t"
#define XA(j, a, b) XM(j, a, b, "v[64:67]", "v[68:71]")
#define XN(r) "v_fmac_f32_dpp v" #r ", v" #r ", v73 quad_perm:[2,2,2,2] row_mask:0xf bank_mask:0xf\n\t"

#define TM 0x40004000u
#define PL(j) "buffer_load_dwordx4 v[32+4*" #j ":35+4*" #j "], %[o], %[rs], %[s" #j "] offen sc1\n\t"
#define PE(j) "v_bitop3_b32 v[48+" #j "], v[33+4*" #j "], v[35+4*" #j "], %[tag] bitop3:0x7e\n\t"
#define PR(j) "v_cmp_ne_u32 vcc, 0, v[48+" #j "]\n\ts_mov_b64 exec, vcc\n\t" PL(j) "s_mov_b64 exec, -1\n\t"
#define KL1(b, j) "buffer_load_dwordx4 v[" #b "+4*" #j ":" #b "+3+4*" #j "], %[o], %[rs], %[s" #j "] offen sc1\n\t"
#define KL(b) KL1(b, 0) KL1(b, 1) KL1(b, 2) KL1(b, 3)
#define KE1(b, j) "v_bitop3_b32 v[48+" #j "], v[" #b "+1+4*" #j "], v[" #b "+3+4*" #j "], %[tag] bitop3:0x7e\n\t"
#define KE(b) KE1(b, 0) KE1(b, 1) KE1(b, 2) KE1(b, 3) "v_or3_b32 v52, v48, v49, v50\n\tv_or_b32 v52, v52, v51\n\tv_cmp_ne_u32 vcc, 0, v52\n\t"
#define KM(b) "v_min_u32 v48, v[" #b "], v[" #b "+4]\n\tv_max_u32 v49, v[" #b "], v[" #b "+4]\n\tv_min_u32 v50, v[" #b "+8], v[" #b "+12]\n\t" \
              "v_max_u32 v51, v[" #b "+8], v[" #b "+12]\n\tv_max_u32 v53, v49, v51\n\tv_min_u32 v52, v49, v51\n\t" \
              "v_max3_u32 v55, v48, v[" #b "+2], v[" #b "+6]\n\tv_max3_u32 v56, v50, v[" #b "+10], v[" #b "+14]\n\tv_max3_u32 v54, v52, v55, v56\n\t"
#define GX(j, c) "s_waitcnt vmcnt(" #c ")\n\tv_xor_b32 v[48+4*" #j "], v[32+4*" #j "], v28\n\tv_xor_b32 v[49+4*" #j "], v[33+4*" #j "], v29\n\t" \
                 "v_xor_b32 v[50+4*" #j "], v[34+4*" #j "], v30\n\tv_xor_b32 v[51+4*" #j "], v[35+4*" #j "], v31\n\t" \
                 "ds_write_b128 %[u], v[48+4*" #j ":51+4*" #j "] offset:1024*" #j "\n\t" \
                 "v_or3_b32 v[64+" #j "], v[48+4*" #j "], v[49+4*" #j "], v[50+4*" #j "]\n\tv_bitop3_b32 v[64+" #j "], v[64+" #j "], v[51+4*" #j "], %[tm] bitop3:0xa8\n\t"
#define GQ(q) "v_or_b32_dpp v64, v64, v64 " q "\n\tv_or_b32_dpp v65, v65, v65 " q "\n\tv_or_b32_dpp v66, v66, v66 " q "\n\tv_or_b32_dpp v67, v67, v67 " q "\n\t"
#define GR(j) "v_cmp_ne_u32 vcc, 0, v[64+" #j "]\n\ts_mov_b64 exec, vcc\n\t" PL(j) "s_mov_b64 exec, -1\n\t"
#define GETTAG() u32 tag; asm volatile("s_waitcnt lgkmcnt(0)\n\ts_add_u32 %0, %1, 0x11111111" : "=s"(tag) : "{s4}"(tag0) : "scc")

__global__ void __launch_bounds__(1024, 4) kf(const void* p0, const void* p1, void* p2, const void* p3, void* p4, const void* p5, const void* p6,
    const void* p7, const void* p8, const void* p9, const void* p10, const void* p11, const void* p12, const void* p13, const void* p14, void* p15) {
  __shared__ __attribute__((aligned(16))) char smem[LDS_SZ];
  LDS char* L = (LDS char*)smem;
  LDS char* F = L + O_F;
  const CST char* kp = (const CST char*)__builtin_amdgcn_kernarg_segment_ptr();
  const int b = blockIdx.x;
  u64 k0_, k2_, k4_, k5_, k6_, k7_; v4u xa, xb, wa, wb; u32 w64, tag0, cg8; u64 dsk;
  asm volatile(
    "v_readfirstlane_b32 s3, %[tid]\n\ts_lshr_b32 s4, s3, 3\n\ts_and_b32 s4, s4, 64\n\ts_load_dwordx16 s[8:23], %[kp], s4\n\t"
    "v_lshlrev_b32 v1, 4, %[tid]\n\ts_cmpk_lt_u32 s3, 0x100\n\ts_cbranch_scc0 1f\n\t"
    "v_and_b32 v2, 0x1f0, v1\n\ts_lshl_b32 s4, %[b], 13\n\tv_sub_u32 v3, v1, v2\n\tv_add3_u32 v3, v3, v1, s4\n\t"
    "s_waitcnt lgkmcnt(0)\n\t"
    "global_load_dwordx4 v[12:15], v3, s[8:9]\n\tglobal_load_dwordx4 v[16:19], v3, s[8:9] offset:512\n\t"
    "global_load_dwordx4 v[20:23], v2, s[10:11]\n\tglobal_load_dwordx4 v[24:27], v2, s[10:11] offset:512\n\ts_setprio 3\n"
    "1:\n\ts_and_b32 s5, %[b], 7\n\ts_lshl_b32 s5, s5, 7\n\ts_lshr_b32 s4, %[b], 4\n\ts_lshl3_add_u32 s5, s4, s5\n\t"
    "s_cmpk_lt_u32 s3, 0x200\n\ts_cbranch_scc1 3f\n\t"
    "s_cmpk_lt_u32 s3, 0x300\n\ts_cbranch_scc1 2f\n\t"
    "s_getpc_b64 s[6:7]\n\tv_lshlrev_b32 v2, 2, v1\n\tv_and_b32 v2, 0x3fc0, v2\n\tv_min_u32 v2, %[cs], v2\n\tglobal_load_dword v18, v2, s[6:7]\n"
    "2:\n\ts_lshl_b32 s4, s5, 7\n\ts_and_b32 s6, s3, 0xc0\n\ts_lshl2_add_u32 s4, s6, s4\n\ts_and_b32 s6, s3, 0x100\n\ts_lshr_b32 s6, s6, 1\n\ts_add_u32 s4, s4, s6\n\t"
    "v_and_b32 v2, 63, %[tid]\n\tv_lshl_add_u32 v2, v2, 1, s4\n\ts_waitcnt lgkmcnt(0)\n\t"
    "global_load_ushort v12, v2, s[8:9]\n\tglobal_load_ushort v13, v2, s[10:11]\n\tglobal_load_ushort v14, v2, s[12:13]\n\t"
    "global_load_ushort v15, v2, s[14:15]\n\tglobal_load_ushort v16, v2, s[16:17]\n\tglobal_load_ushort v17, v2, s[18:19]\n\t"
    "s_load_dwordx2 s[6:7], s[20:21], s5\n\ts_setprio 1\n\ts_branch 5f\n"
    "3:\n\ts_cmpk_lt_u32 s3, 0x100\n\ts_cbranch_scc1 4f\n\t"
    "s_setprio 2\n\ts_bfe_u32 s4, s3, 0x20006\n\ts_mulk_i32 s4, %[up]\n\tv_mov_b64 v[12:13], 0\n\tv_and_b32 v1, 63, %[tid]\n\tv_lshlrev_b32 v2, 3, v1\n\t"
    "v_add_u32 v2, %[ou], v2\n\tv_add_u32 v3, s4, v2\n\tds_write_b64 v3, v[12:13]\n\tds_write_b64 v2, v[12:13] offset:%[up]*4\n\t"
    "s_and_b32 s4, %[b], 7\n\ts_lshr_b32 s6, %[b], 3\n\ts_cmpk_eq_u32 s3, 0x1c0\n\ts_cbranch_scc1 6f\n\t"
    "s_mulk_i32 s4, 448\n\ts_andn2_b32 s4, s4, 127\n\ts_mul_i32 s6, s6, 16*3584\n\ts_add_u32 s4, s4, s6\n\t"
    "v_lshrrev_b32 v2, 2, v1\n\tv_mul_u32_u24 v2, 3584-512, v2\n\tv_lshl_add_u32 v2, v1, 7, v2\n\ts_branch 7f\n"
    "6:\n\ts_lshl_b32 s4, s4, 5\n\ts_add_u32 s4, s4, s6\n\ts_mulk_i32 s4, 7*1024\n\tv_min_u32 v2, 55, v1\n\tv_lshlrev_b32 v2, 7, v2\n"
    "7:\n\tv_add_u32 v2, s4, v2\n\ts_waitcnt lgkmcnt(0)\n\t"
    "s_cmpk_eq_u32 s3, 0x140\n\ts_cselect_b64 s[10:11], s[18:19], s[20:21]\n\t"
    "s_cmpk_eq_u32 s3, 0x1c0\n\ts_cselect_b64 s[10:11], s[22:23], s[10:11]\n"
    "4:\n\ts_load_dword s4, s[12:13], 0\n\ts_load_dwordx2 s[6:7], s[14:15], s5\n"
    "5:\n\ts_barrier\n\ts_sub_u32 s14, s3, 0x140\n\ts_cmpk_lt_u32 s14, 0xc0\n\ts_cbranch_scc0 8f\n\ts_sleep %[dp]\n\tglobal_load_dword v14, v2, s[10:11]\n8:"
    : "={v[12:15]}"(xa), "={v[16:19]}"(xb), "={v[20:23]}"(wa), "={v[24:27]}"(wb)
    : [tid] "{v0}"(threadIdx.x), [kp] "{s[0:1]}"(kp), [b] "{s2}"(b), [cs] "n"(CODE_SZ), [up] "n"(UP), [ou] "n"(O_U), [dp] "n"(D_P)
    : "scc", "memory", "v1", "v2", "v3", "s3", "s4", "s5", "s6", "s7", "s8", "s9", "s10", "s11", "s12", "s13", "s14", "s15", "s16", "s17",
      "s18", "s19", "s20", "s21", "s22", "s23");
  asm volatile("" : "={s[8:9]}"(k0_), "={s[12:13]}"(k2_), "={s[16:17]}"(k4_), "={s[18:19]}"(k5_), "={s[20:21]}"(k6_), "={s[22:23]}"(k7_),
               "={s3}"(w64), "={s4}"(tag0), "={s5}"(cg8), "={s[6:7]}"(dsk));
  asm volatile("s_mov_b64 s[84:85], 0\n\ts_mov_b64 s[86:87], 0\n\ts_mov_b64 s[88:89], 0\n\ts_mov_b64 s[90:91], 0\n\ts_mov_b64 s[92:93], 0\n\t"
               "s_mov_b64 s[94:95], 0\n\ts_mov_b64 s[96:97], 0\n\ts_mov_b64 s[98:99], 0" ::: "s99");
  STAMP(0);
  const int lane = threadIdx.x & 63, w = w64 >> 6, cg = cg8 >> 3, h = (b >> 3) & 1;
  const int c16 = lane & 15, kg = lane >> 4;
  if (__builtin_expect(w64 < 512, 1)) {
    char* const ws = (char*)k2_;
    const __amdgpu_buffer_rsrc_t rs = __builtin_amdgcn_make_buffer_rsrc(ws, 0, WS_END, 0x00020000);
    const bool gwg = ((b >> 4) + ((~b & 8) << 1)) < 14;
    const int si = w >> 1, s = h ? 2 + si : (si < 2 ? si : si + 4), t0 = s * 256 + (w & 1) * 128 + lane * 2;
    v4u sv;
    asm volatile("" : "={v[120:123]}"(sv));
#define SVL() asm volatile("buffer_load_dwordx4 %0, %1, %2, %3 offen sc1" : "={v[120:123]}"(sv) : "v"(t0 * 8), "s"(rs), "s"(WS_S) : "memory")
#define SVW() asm volatile("s_waitcnt vmcnt(0)" : "+{v[120:123]}"(sv) :: "memory")
    if (__builtin_expect(w64 < 256, 1)) {
      LDS char* a_xn = L + O_LN + (threadIdx.x >> 5) * 1088 + (lane & 31) * 16;
      LDS char* a_ks = F + F_KS + (threadIdx.x >> 5) * 8;
      LDS char* a_f = F;
      LDS char* a_tr = L + O_LN + ((lane >> 2) & 3) * 1088 + w * 256 + kg * 32 + (lane & 3) * 8;
      u32 o_pub = (lane >> 3) * XS + (lane & 7) * 16;
      u32 o_g = (lane >> 2) * 128 + (lane & 3) * 16, so_g = WS_X + (cg >> 1) * XS + (cg & 1) * 64 + w * 2048;
      u32 rsh = (lane & 3) * 8, bit = 1u << w;
      u32 so_pub = WS_X + w * 16 * XS + b * (XL ? 64 : 128), so_pu2 = so_pub + 8 * XS, kix = 0x80000000u | (u32)(2047 - b * 8 - (threadIdx.x >> 5));
      LDS char* a_u = L + O_U + (lane & 3) * UP + 512 + w * 256 + (lane >> 2) * 16;
      asm volatile("s_waitcnt vmcnt(2)"
                   : "+{v[12:15]}"(xa), "+{v[16:19]}"(xb), "+v"(a_xn), "+v"(a_ks), "+v"(a_f), "+v"(a_tr), "+v"(o_pub), "+v"(rsh), "+v"(bit), "+v"(kix),
                     "+v"(o_g), "+v"(a_u), "+s"(so_pub), "+s"(so_pu2), "+s"(so_g) :: "memory");
      STAMP(1);
      float s1 = 0.f, s2 = 0.f;
#pragma unroll
      for (int j = 0; j < 4; j++) { s1 = dot2(xa[j], 0x3f803f80u, s1); s2 = dot2(xa[j], xa[j], s2); }
#pragma unroll
      for (int j = 0; j < 4; j++) { s1 = dot2(xb[j], 0x3f803f80u, s1); s2 = dot2(xb[j], xb[j], s2); }
      v2u p = __builtin_amdgcn_permlane16_swap(__float_as_uint(s1), __float_as_uint(s2), false, false);
      float f = __uint_as_float(p[0]) + __uint_as_float(p[1]);
      f += dppf<0xB1>(f); f += dppf<0x4E>(f); f += dppf<0x141>(f); f += dppf<0x140>(f);
      f *= 1.f / 512.f;
      p = __builtin_amdgcn_permlane16_swap(__float_as_uint(f), __float_as_uint(f), false, false);
      const float dm = __uint_as_float(p[0]), mb = rbf(dm);
      const float sd = rbf(__builtin_amdgcn_sqrtf(rbf(rbf(fmaxf(__uint_as_float(p[1]) - dm * dm, 0.f)) + 1e-5f)));
      const float r16 = __builtin_amdgcn_rcpf(sd * 16.f), off = -mb * r16;
      v4u na, nb;
#pragma unroll
      for (int j = 0; j < 4; j++) { v2f y = __builtin_elementwise_fma((v2f){lo2f(xa[j]), hi2f(xa[j])}, (v2f){r16, r16}, (v2f){off, off}); na[j] = pk(y.x, y.y); }
      asm volatile("ds_write_b128 %0, %1" :: "v"(a_xn), "v"(na) : "memory");
#pragma unroll
      for (int j = 0; j < 4; j++) { v2f y = __builtin_elementwise_fma((v2f){lo2f(xb[j]), hi2f(xb[j])}, (v2f){r16, r16}, (v2f){off, off}); nb[j] = pk(y.x, y.y); }
      asm volatile("ds_write_b128 %2, %3 offset:512\n\ts_mov_b64 exec, 1\n\tds_or_b32 %4, %5 offset:%6\n\ts_mov_b64 exec, -1\n\ts_waitcnt vmcnt(0)"
                   : "+{v[20:23]}"(wa), "+{v[24:27]}"(wb) : "v"(a_xn), "v"(nb), "v"(a_f), "v"(bit), "n"(F_LN) : "memory");
      float r0 = 0.f, r1 = 0.f;
#pragma unroll
      for (int j = 0; j < 4; j++) { r0 = dot2(na[j], wa[j], r0); r1 = dot2(nb[j], wb[j], r1); }
      r0 += r1;
      u32 fl; v2u a0, a1, a2, a3;
#define TRR "ds_read_b32 %0, %[f] offset:%[o]\n\tds_read_b64_tr_b16 %1, %[t]\n\tds_read_b64_tr_b16 %2, %[t] offset:4352\n\t" \
            "ds_read_b64_tr_b16 %3, %[t] offset:128\n\tds_read_b64_tr_b16 %4, %[t] offset:4480\n\t"
      asm volatile(TRR : "=&v"(fl), "=&v"(a0), "=&v"(a1), "=&v"(a2), "=&v"(a3), "+v"(r0) : [f] "v"(a_f), [t] "v"(a_tr), [o] "n"(F_LN) : "memory");
      r0 += dppf<0xB1>(r0); r0 += dppf<0x4E>(r0); r0 += dppf<0x141>(r0); r0 += dppf<0x140>(r0);
      p = __builtin_amdgcn_permlane16_swap(__float_as_uint(r0), __float_as_uint(r0), false, false);
      const u32 rb = pk(0.f, (__uint_as_float(p[0]) + __uint_as_float(p[1])) * 16.f);
      const u32 key = rb ^ (((int)rb >> 31) & 0x7fff0000u) ^ kix;
      {
        u32 sc, cnt;
        asm volatile("s_mov_b64 exec, %[em]\n\tds_write_b64 %[ka], %[kv]\n\ts_mov_b64 exec, -1\n\ts_mov_b32 %6, 0x20000\n\ts_waitcnt lgkmcnt(0)\n"
                     "1:\n\tv_readfirstlane_b32 %5, %0\n\ts_cmp_eq_u32 %5, 15\n\ts_cbranch_scc1 2f\n\t" TRR
                     "s_waitcnt lgkmcnt(0)\n\ts_sub_u32 %6, %6, 1\n\ts_cbranch_scc0 1b\n2:"
                     : "+v"(fl), "+v"(a0), "+v"(a1), "+v"(a2), "+v"(a3), "=&s"(sc), "=&s"(cnt)
                     : [f] "v"(a_f), [t] "v"(a_tr), [o] "n"(F_LN), [em] "s"(0x100000001ull), [ka] "v"(a_ks),
                       [kv] "v"((v2u){(__float_as_uint(mb) >> 16) | __float_as_uint(sd), key})
                     : "memory", "scc");
      }
      GETTAG();
      u32 pt[4];
      {
        const u32 q = __builtin_amdgcn_ubfe(tag ^ 0x3C5A96C3u, rsh, 8), y = q | q << 15;
#pragma unroll
        for (int j = 0; j < 4; j++) pt[j] = (y << (14 - 2 * j)) & TM;
      }
      __builtin_amdgcn_raw_buffer_store_b128((v4u){(a0[0] & ~TM) | pt[0], (a0[1] & ~TM) | pt[1], (a1[0] & ~TM) | pt[2], (a1[1] & ~TM) | pt[3]}, rs, o_pub, so_pub, 16);
      __builtin_amdgcn_raw_buffer_store_b128((v4u){(a2[0] & ~TM) | pt[0], (a2[1] & ~TM) | pt[1], (a3[0] & ~TM) | pt[2], (a3[1] & ~TM) | pt[3]}, rs, o_pub, so_pu2, 16);
      lds_sig(F + F_PB, bit);
      STAMP(2);
      {
        u32 cnt;
        asm volatile("s_cmp_lt_u32 %[g], 14\n\ts_cbranch_scc0 5f\n\ts_sleep %[sg]\n5:\n\tv_mov_b32 v67, 0\n\ts_movk_i32 %0, %[n]\n\ts_cmp_lg_u32 %[h], 0\n\ts_sleep %[sl]\n\ts_cbranch_scc1 1f\n\t" PL(3)
                     "1:\n\t" PL(0) PL(1) PL(2)
                     "2:\n\ts_cmp_lg_u32 %[h], 0\n\ts_cbranch_scc1 3f\n\t" GX(3, 3)
                     "3:\n\t" GX(0, 2) GX(1, 1) GX(2, 0)
                     "v_or3_b32 v68, v64, v65, v67\n\tv_or_b32 v68, v68, v66\n\tv_cmp_ne_u32 vcc, 0, v68\n\ts_cbranch_vccz 4f\n\t"
                     GQ("quad_perm:[1,0,3,2] row_mask:0xf bank_mask:0xf") GQ("quad_perm:[2,3,0,1] row_mask:0xf bank_mask:0xf")
                     GR(0) GR(1) GR(2) GR(3) "s_waitcnt vmcnt(0)\n\ts_sub_u32 %0, %0, 1\n\ts_cbranch_scc0 2b\n4:"
                     : "=&s"(cnt)
                     : [o] "v"(o_g), [rs] "s"(rs), [s0] "s"(so_g), [s1] "s"(so_g + (8192 >> XL)), [s2] "s"(so_g + (16384 >> XL)), [s3] "s"(so_g + (24576 >> XL)), [u] "v"(a_u),
                       "{v[28:31]}"((v4u){pt[0], pt[1], pt[2], pt[3]}), [tm] "s"(TM), [h] "s"(h), [n] "n"(SPIN), [sl] "n"(D_A), [g] "s"((b >> 4) + ((~b & 8) << 1)), [sg] "n"(D_AG)
                     : "memory", "scc", "vcc", "v32", "v33", "v34", "v35", "v36", "v37", "v38", "v39", "v40", "v41", "v42", "v43", "v44", "v45", "v46", "v47",
                       "v48", "v49", "v50", "v51", "v52", "v53", "v54", "v55", "v56", "v57", "v58", "v59", "v60", "v61", "v62", "v63", "v64", "v65", "v66",
                       "v67", "v68");
      }
      lds_sig(F + F_U, bit);
      SVL();
      STAMP(3);
      SVW();
    }
    asm volatile("" : "+s"(w64));
    {
      if (w64 == 256) {
        u32 o_s = lane * 8, o_k = lane * 16;
        LDS char* a_kr = F + F_KS + (lane & 7) * 8;
        asm volatile("s_setprio 3\n\ts_sleep %3" : "+v"(o_s), "+v"(o_k), "+v"(a_kr) : "n"(D_R) : "memory");
        GETTAG();
        u32 cnt;
#define DQ(q) " " q " row_mask:0xf bank_mask:0xf\n\t"
        asm volatile("v_mov_b32 v33, %[tag]\n\tv_mov_b32 v35, %[tag]\n\tv_mov_b32 v38, 16\n\ts_mov_b32 %0, 0x20000\n"
                     "1:\n\tds_read_b64 v[36:37], %[a]\n\ts_waitcnt lgkmcnt(0)\n\t"
                     "v_cmp_eq_u32 vcc, 0, v36\n\ts_cbranch_vccz 2f\n\ts_sub_u32 %0, %0, 1\n\ts_cbranch_scc0 1b\n"
                     "2:\n\tv_max_u32_dpp v32, v37, v37" DQ("quad_perm:[1,0,3,2]") "v_min_u32_dpp v34, v37, v37" DQ("quad_perm:[1,0,3,2]")
                     "v_mov_b32 v37, %[tag]\n\t"
                     "v_min_u32_dpp v39, v32, v32" DQ("quad_perm:[2,3,0,1]") "v_max_u32_dpp v34, v34, v34" DQ("quad_perm:[2,3,0,1]")
                     "v_max_u32_dpp v32, v32, v32" DQ("quad_perm:[2,3,0,1]") "v_max_u32 v34, v34, v39\n\ts_nop 0\n\t"
                     "v_min_u32_dpp v39, v32, v32" DQ("row_half_mirror") "v_max_u32_dpp v34, v34, v34" DQ("row_half_mirror")
                     "v_max_u32_dpp v32, v32, v32" DQ("row_half_mirror") "v_max_u32 v34, v34, v39\n\ts_mov_b64 exec, 1\n\t"
                     "buffer_store_dwordx4 v[32:35], %[ok], %[rs], %[sk] offen sc1\n\ts_mov_b64 exec, 0xff\n\t"
                     "buffer_store_dwordx2 v[36:37], %[os], %[rs], %[ss] offen sc1\n\ts_mov_b64 exec, 1\n\t"
                     "ds_or_b32 %[a], v38 offset:%[pb]\n\ts_mov_b64 exec, -1"
                     : "=&s"(cnt)
                     : [a] "v"(a_kr), [tag] "s"(tag), [ok] "v"(o_k), [os] "v"(o_s), [rs] "s"(rs), [sk] "s"(WS_R + b * 16), [ss] "s"(WS_S + b * 64),
                       [pb] "n"(F_PB - F_KS)
                     : "memory", "scc", "vcc", "v32", "v33", "v34", "v35", "v36", "v37", "v38", "v39");
        u32 k0, k1; v4u kb[4], kc[4];
        asm volatile("s_cmp_lt_u32 %[g], 14\n\ts_cbranch_scc1 3f\n\ts_sleep %[dk]\n\t" KL(32) "s_branch 4f\n"
                     "3:\n\ts_sleep %[dg]\n\t" KL(60) "s_sleep %[d2]\n\t" KL(76) "s_sleep %[d2]\n\t" KL(32)
                     "s_waitcnt vmcnt(8)\n\t" KE(60) "s_cbranch_vccnz 5f\n\t" KM(60) "s_branch 6f\n"
                     "5:\n\ts_waitcnt vmcnt(4)\n\t" KE(76) "s_cbranch_vccnz 4f\n\t" KM(76) "s_branch 6f\n"
                     "4:\n\ts_movk_i32 %[c], %[n]\n"
                     "1:\n\ts_waitcnt vmcnt(0)\n\t" KE(32) "s_cbranch_vccz 2f\n\t"
                     PR(0) PR(1) PR(2) PR(3) "s_sub_u32 %[c], %[c], 1\n\ts_cbranch_scc0 1b\n\ts_waitcnt vmcnt(0)\n2:\n\t" KM(32) "6:"
                     : "={v53}"(k0), "={v54}"(k1), "={v[32:35]}"(kc[0]), "={v[36:39]}"(kc[1]), "={v[40:43]}"(kc[2]), "={v[44:47]}"(kc[3]),
                       "={v[76:79]}"(kb[0]), "={v[80:83]}"(kb[1]), "={v[84:87]}"(kb[2]), "={v[88:91]}"(kb[3]), [c] "=&s"(cnt)
                     : [o] "{v31}"(o_k), [rs] "s"(rs), [s0] "s"(WS_R), [s1] "s"(WS_R + 1024), [s2] "s"(WS_R + 2048), [s3] "s"(WS_R + 3072), [tag] "s"(tag),
                       [n] "n"(SPIN), [g] "s"((b >> 4) + ((~b & 8) << 1)), [dk] "n"(D_K), [dg] "n"(D_KG), [d2] "n"(D_K2)
                     : "memory", "scc", "vcc", "v48", "v49", "v50", "v51", "v52", "v55", "v56", "v60", "v61", "v62", "v63", "v64", "v65", "v66", "v67",
                       "v68", "v69", "v70", "v71", "v72", "v73", "v74", "v75");
        STAMP(3);
        const u32 s0 = wave_max(k0);
        if (gwg) asm volatile("s_mov_b32 m0, %2\n\ts_nop 0\n\tglobal_load_lds_dwordx4 %0, %1"
                              :: "v"(o_k), "s"(k0_ + ((~s0 & 2047) << 10)), "n"(O_XR) : "memory");
        const u32 s1 = wave_max(k0 == s0 ? k1 : k0);
        const u32 o_t = (~((lane & 1) ? s1 : s0) & 2047) * 8;
#define TSW "s_mov_b64 exec, 1\n\tds_write_b64 %[f], %[ts] offset:%[ot]\n\tds_or_b32 %[f], %[c1] offset:%[os]\n\ts_mov_b64 exec, -1\n\t"
        if (gwg) {
          asm volatile("s_mov_b32 m0, %[m]\n\ts_movk_i32 %0, %[n]\n\tglobal_load_lds_dwordx4 %[o], %[x]\n\t"
                       "buffer_load_dwordx2 v[48:49], %[t], %[rs], %[ss] offen sc1\n\t" TSW
                       "1:\n\ts_waitcnt vmcnt(0)\n\tv_cmp_ne_u32 vcc, %[tag], v49\n\ts_cbranch_vccz 2f\n\t"
                       "buffer_load_dwordx2 v[48:49], %[t], %[rs], %[ss] offen sc1\n\ts_sub_u32 %0, %0, 1\n\ts_cbranch_scc0 1b\n\ts_waitcnt vmcnt(0)\n"
                       "2:\n\ts_mov_b64 exec, 3\n\tds_write_b32 %[fl], v48 offset:%[st]\n\ts_mov_b64 exec, 1\n\tds_or_b32 %[f], %[c2] offset:%[os]\n\ts_mov_b64 exec, -1"
                       : "=&s"(cnt)
                       : [o] "v"(o_k), [x] "s"(k0_ + ((~s1 & 2047) << 10)), [m] "n"(O_XR + 1088), [n] "n"(SPIN), [t] "v"(o_t), [rs] "s"(rs), [ss] "s"(WS_S),
                         [f] "v"(F), [ts] "v"((v2u){s0, s1}), [c1] "v"(256u), [c2] "v"(512u), [ot] "n"(F_TS), [os] "n"(F_SP), [tag] "s"(tag),
                         [fl] "v"(F + lane * 4), [st] "n"(F_ST),
                         "{v[32:35]}"(kc[0]), "{v[36:39]}"(kc[1]), "{v[40:43]}"(kc[2]), "{v[44:47]}"(kc[3]),
                         "{v[76:79]}"(kb[0]), "{v[80:83]}"(kb[1]), "{v[84:87]}"(kb[2]), "{v[88:91]}"(kb[3])
                       : "memory", "scc", "vcc", "v48", "v49");
        } else {
          asm volatile(TSW :: [f] "v"(F), [ts] "v"((v2u){s0, s1}), [c1] "v"(256u), [ot] "n"(F_TS), [os] "n"(F_SP) : "memory");
          if (b == 0 && lane == 0) *(GLB u32*)ws = tag;
        }
        {
          const u32 own = h ? 0x3Cu : 0xC3u;
          const u64 zm = ((own >> (7 - ((s0 >> 8) & 7))) & 1) | ((own >> (7 - ((s1 >> 8) & 7))) & 1) << 1;
          asm volatile("s_mov_b64 exec, %3\n\tglobal_store_dwordx2 %0, %1, %2 sc1\n\ts_mov_b64 exec, -1"
                       :: "v"(o_t * 128), "v"((v2u){0, 0}), "s"(k4_ + cg8), "s"(zm) : "memory");
        }
        SVL();
        STAMP(1);
        SVW();
        asm volatile("s_mov_b64 exec, %5\n\tbuffer_store_dwordx4 %0, %1, %2, %3 offen sc1\n\ts_mov_b64 exec, 1\n\tds_or_b32 %4, %6 offset:%7\n\ts_mov_b64 exec, -1"
                     :: "v"((v4u){0, tag, 0, tag}), "v"(o_k), "s"(rs), "s"(WS_P + cg * PG + 112 * 32), "v"(F), "s"((u64)(h * 3)), "v"(2u), "n"(F_UP) : "memory");
      }
      asm volatile("" : "+s"(w64));
      if (w64 > 256) {
        u32 dummy;
        asm volatile("" : "={v14}"(dummy));
        if (gwg) {
          const int fs = (b & 7) * 14 + (b >> 4);
          v4u wq[8];
          LDS char* ex = F + F_E + (kg * 2 + (lane & 1)) * 32;
          LDS char* sl = F + F_ST + (lane & 1) * 4;
          if (w64 != 320) {
            const int fw = w & 1;
            LDS char* const wl = L + O_W + fw * 8192;
            *(LDS v2u*)(L + O_XR + 2176 + lane * 8) = (v2u){0x3f803f80u, 0x3f803f80u};
            v8bf fg[8], fu[8];
            const int rho = lane >> 1, kk = 8 * ((rho >> 2) & 3) + 4 * (rho >> 4) + (rho & 3);
            u32 go = (fw * 256 + kk) * 3584 + fs * 32 + (lane & 1) * 16, gu = go, bit = 1u << fw;
            LDS char* xl = L + O_XR + ((lane & 2) ? 2176 : (lane & 1) * 1088 + fw * 512) + kg * 16;
            ex += fw * 256;
            asm volatile("s_sleep %6" : "+v"(go), "+v"(gu), "+v"(xl), "+v"(ex), "+v"(sl), "+v"(bit) : "n"(D_W));
            lds_wait<1>(F + F_PB, 31);
            asm volatile("s_mov_b32 m0, %[m]\n\ts_setprio 3\n\t" ST_(1)
                         WG_ WG_ WG_ WG_ WG_ WG_ WG_ WG_ WU(0) WU(1) WU(2) WU(3) WU(4) WU(5) WU(6) WU(7) ST_(2) "s_waitcnt vmcnt(8)"
                         : "=&v"(wq[0]), "=&v"(wq[1]), "=&v"(wq[2]), "=&v"(wq[3]), "=&v"(wq[4]), "=&v"(wq[5]), "=&v"(wq[6]), "=&v"(wq[7]),
                           [go] "+v"(go), [gu] "+v"(gu), "+{v14}"(dummy)
                         : [m] "s"(O_W + fw * 8192), [wg] "s"(k5_), [wu] "s"(k6_)
                         : "memory", "scc");
#pragma unroll
            for (int ks = 0; ks < 8; ks++) fg[ks] = cat(trd(wl + lane * 8 + ks * 1024), trd(wl + lane * 8 + ks * 1024 + 512));
            asm volatile("s_waitcnt vmcnt(0)"
                         : "+v"(wq[0]), "+v"(wq[1]), "+v"(wq[2]), "+v"(wq[3]), "+v"(wq[4]), "+v"(wq[5]), "+v"(wq[6]), "+v"(wq[7]) :: "memory");
#pragma unroll
            for (int ks = 0; ks < 8; ks++) *(LDS v4u*)(wl + ks * 1024 + lane * 16) = wq[ks];
            MEMB();
#pragma unroll
            for (int ks = 0; ks < 8; ks++) fu[ks] = cat(trd(wl + lane * 8 + ks * 1024), trd(wl + lane * 8 + ks * 1024 + 512));
            SVL();
            STAMP(3);
            lds_wait<1>(F + F_SP, 256);
            u32 sc, cnt;
            asm volatile("s_sleep %[sl]\n\ts_mov_b32 %1, 0x20000\n"
                         "1:\n\tds_read_b32 v72, %[f] offset:%[sp]\n\tds_read_b32 v73, %[s]\n\t" XR(0) XR(1) XR(2) XR(3) XR(4) XR(5) XR(6) XR(7)
                         "s_waitcnt lgkmcnt(9)\n\tv_readfirstlane_b32 %0, v72\n\ts_bitcmp1_b32 %0, 9\n\ts_cbranch_scc1 2f\n\t"
                         "s_waitcnt lgkmcnt(0)\n\ts_sub_u32 %1, %1, 1\n\ts_cbranch_scc0 1b\n2:\n\t"
                         XM(0, 2, 10, "0", "0") "v_lshlrev_b32 v73, 16, v73\n\t" XA(1, 3, 11) "v_xor_b32 v73, 0x80000000, v73\n\t"
                         XA(2, 4, 12) XA(3, 5, 13) XA(4, 6, 14) XA(5, 7, 15) XA(6, 8, 16) XA(7, 9, 17) ST_(4)
                         "s_nop 6\n\t" XN(64) XN(65) XN(66) XN(67) XN(68) XN(69) XN(70) XN(71)
                         "s_mov_b64 exec, %[em]\n\tds_write_b128 %[e], v[64:67]\n\tds_write_b128 %[e], v[68:71] offset:16\n\t"
                         "s_mov_b64 exec, 1\n\tds_or_b32 %[f], %[bit] offset:%[ex]\n\ts_mov_b64 exec, -1"
                         : "=&s"(sc), "=&s"(cnt)
                         : "v"(fg[0]), "v"(fg[1]), "v"(fg[2]), "v"(fg[3]), "v"(fg[4]), "v"(fg[5]), "v"(fg[6]), "v"(fg[7]),
                           "v"(fu[0]), "v"(fu[1]), "v"(fu[2]), "v"(fu[3]), "v"(fu[4]), "v"(fu[5]), "v"(fu[6]), "v"(fu[7]),
                           [f] "v"(F), [s] "v"(sl), [x] "v"(xl), [e] "v"(ex), [bit] "v"(bit), [em] "s"(0x0003000300030003ull),
                           [sp] "n"(F_SP), [ex] "n"(F_EX), [sl] "n"(D_G)
                         : "memory", "scc", "v32", "v33", "v34", "v35", "v36", "v37", "v38", "v39", "v40", "v41", "v42", "v43", "v44", "v45", "v46", "v47",
                           "v48", "v49", "v50", "v51", "v52", "v53", "v54", "v55", "v56", "v57", "v58", "v59", "v60", "v61", "v62", "v63", "v64", "v65",
                           "v66", "v67", "v68", "v69", "v70", "v71", "v72", "v73");
            STAMP(5);
            SVW();
          } else {
            LDS char* const wl = L + O_LN;
            u32 dd = (fs * 16 + (lane >> 2)) * 1024 + (lane & 3) * 16, po = PT ? ((c16 >> 1) * 4 + kg) * PG + (lane & 1) * 16 : (c16 >> 1) * 128 + kg * 32 + (lane & 1) * 16;
            LDS char* dl = wl + (lane >> 2) * 64 + (lane & 3) * 8;
            asm volatile("s_sleep %5" : "+v"(dd), "+v"(po), "+v"(dl), "+v"(ex), "+v"(sl) : "n"(D_W));
            lds_wait<1>(F + F_PB, 31);
            asm volatile("s_mov_b32 m0, %[m]\n\ts_sleep %[sl]\n\t" WD(0) WD(1) WD(2) WD(3) WD(4) WD(5) WD(6) WD(7) "s_waitcnt vmcnt(0)"
                         : "=&v"(wq[0]), "=&v"(wq[1]), "=&v"(wq[2]), "=&v"(wq[3]), "=&v"(wq[4]), "=&v"(wq[5]), "=&v"(wq[6]), "=&v"(wq[7]), "+{v14}"(dummy)
                         : [m] "n"(O_LN), [wd] "s"(k7_), [dd] "v"(dd), [sl] "n"(D_D)
                         : "memory", "scc");
            v8bf wdf[16];
#pragma unroll
            for (int j = 0; j < 8; j++) wdf[j] = cat(trd(dl + j * 1024), trd(dl + j * 1024 + 32));
            MEMB();
#pragma unroll
            for (int ks = 0; ks < 8; ks++) *(LDS v4u*)(wl + ks * 1024 + lane * 16) = wq[ks];
            MEMB();
#pragma unroll
            for (int j = 0; j < 8; j++) wdf[8 + j] = cat(trd(dl + j * 1024), trd(dl + j * 1024 + 32));
            GETTAG();
            SVL();
            u64 hm[8];
#pragma unroll
            for (int j = 0; j < 8; j++) hm[j] = 0x0003000300030003ull << (2 * j);
            u32 so = WS_P + fs * (PT ? 32 : PS), so1 = so + (PT ? 32 * PG : 1024), so2 = so + (PT ? 64 * PG : 2048), so3 = so + (PT ? 96 * PG : 3072);
            v4u pv[2] = {{0, tag, 0, tag}, {0, tag, 0, tag}};
            asm volatile("s_setprio 3" : "+s"(hm[0]), "+s"(hm[1]), "+s"(hm[2]), "+s"(hm[3]), "+s"(hm[4]), "+s"(hm[5]), "+s"(hm[6]), "+s"(hm[7]), "+s"(so),
                         "+s"(so1), "+s"(so2), "+s"(so3));
            asm volatile("" : "+v"(wdf[8]), "+v"(wdf[9]), "+v"(wdf[10]), "+v"(wdf[11]), "+v"(wdf[12]), "+v"(wdf[13]), "+v"(wdf[14]), "+v"(wdf[15]),
                         "+v"(pv[0]), "+v"(pv[1]));
            STAMP(1);
            lds_wait<1>(F + F_SP, 512);
            const float sd = hi2f(*(LDS u32*)sl);
            float c1 = __builtin_amdgcn_rcpf(sd) * -1.44269504089f, c2 = sd * sd;
            SVW();
            v2u hf;
            {
              u32 sc, cnt;
#define X4(m) m(0) m(1) m(2) m(3)
#define SA(i) "v_add_f32 v[76+" #i "], v[76+" #i "], v[84+" #i "]\n\t"
#define SB_(i) "v_mul_f32 v[84+" #i "], %[c1], v[76+" #i "]\n\t"
#define SC(i) "v_exp_f32 v[84+" #i "], v[84+" #i "]\n\t"
#define SD(i) "v_add_f32 v[80+" #i "], v[80+" #i "], v[88+" #i "]\n\t"
#define SE(i) "v_fma_f32 v[84+" #i "], v[84+" #i "], %[c2], %[c2]\n\t"
#define SF(i) "v_rcp_f32 v[84+" #i "], v[84+" #i "]\n\t"
#define SG(i) "v_mul_f32 v[76+" #i "], v[76+" #i "], v[80+" #i "]\n\t"
#define SH(i) "v_mul_f32 v[76+" #i "], v[76+" #i "], v[84+" #i "]\n\t"
              asm volatile("s_mov_b32 %2, 0x20000\n"
                           "1:\n\tds_read_b32 v74, %[f]\n\tds_read_b128 v[76:79], %[e]\n\tds_read_b128 v[84:87], %[e] offset:256\n\t"
                           "ds_read_b128 v[80:83], %[e] offset:16\n\tds_read_b128 v[88:91], %[e] offset:272\n\t"
                           "s_waitcnt lgkmcnt(4)\n\tv_readfirstlane_b32 %1, v74\n\ts_cmp_eq_u32 %1, 3\n\t"
                           "s_cbranch_scc1 2f\n\ts_sub_u32 %2, %2, 1\n\ts_cbranch_scc0 1b\n2:\n\ts_waitcnt lgkmcnt(2)\n\t"
                           X4(SA) X4(SB_) X4(SC) "s_waitcnt lgkmcnt(0)\n\t" ST_(2) X4(SD) X4(SE) X4(SF) X4(SG) X4(SH)
                           "v_cvt_pk_bf16_f32 v74, v76, v77\n\tv_cvt_pk_bf16_f32 v75, v78, v79"
                           : "=&{v[74:75]}"(hf), "=&s"(sc), "=&s"(cnt)
                           : [f] "v"(F + F_EX), [e] "v"(ex), [c1] "v"(c1), [c2] "v"(c2)
                           : "memory", "scc", "v76", "v77", "v78", "v79", "v80", "v81", "v82", "v83", "v84", "v85", "v86", "v87", "v88", "v89", "v90", "v91");
            }
            v4f e[4];
#pragma unroll
            for (int q = 0; q < 4; q++) e[q] = (v4f){0.f, 0.f, 0.f, 0.f};
#pragma unroll
            for (int j = 0; j < 4; j++) {
              const bool m0 = __builtin_amdgcn_inverse_ballot_w64(hm[2 * j]), m1 = __builtin_amdgcn_inverse_ballot_w64(hm[2 * j + 1]);
              const v8bf hb = BF8(((v4u){m0 ? hf[0] : 0, m0 ? hf[1] : 0, m1 ? hf[0] : 0, m1 ? hf[1] : 0}));
#pragma unroll
              for (int q = 0; q < 4; q++) e[q] = MF(wdf[4 * q + j], hb, e[q]);
            }
#pragma unroll
            for (int q = 0; q < 4; q++) {
              pv[q & 1][0] = pk(e[q][0], e[q][1]); pv[q & 1][2] = pk(e[q][2], e[q][3]);
              __builtin_amdgcn_raw_buffer_store_b128(pv[q & 1], rs, po, q == 0 ? so : q == 1 ? so1 : q == 2 ? so2 : so3, 16);
            }
            STAMP(0);
          }
        } else {
          STAMP(1);
          asm volatile("s_setprio 0\n\ts_waitcnt vmcnt(0)" : "+{v14}"(dummy));
          STAMP(2);
          lds_wait<4>(F + F_U, 15); SVL(); SVW();
        }
      }
    }
    GETTAG();
    asm volatile("" : "+{s[6:7]}"(dsk));
    const bool ffn = gwg && w > 5;
    {
      u32 cnt, e;
      asm volatile("s_setprio 0\n\ts_cmpk_eq_u32 %[w], %[wq]\n\ts_cbranch_scc0 3f\n\ts_setprio 3\n3:\n\ts_movk_i32 %1, %[n]\n"
                   "1:\n\tv_bitop3_b32 %2, v121, v123, %[tag] bitop3:0x7e\n\tv_cmp_ne_u32 vcc, 0, %2\n\ts_cbranch_vccz 2f\n\t"
                   "buffer_load_dwordx4 %0, %[o], %[rs], %[ss] offen sc1\n\ts_sub_u32 %1, %1, 1\n\ts_waitcnt vmcnt(0)\n\ts_cbranch_scc0 1b\n2:"
                   : "+{v[120:123]}"(sv), "=&s"(cnt), "=&v"(e)
                   : [o] "v"(t0 * 8), [rs] "s"(rs), [ss] "s"(WS_S), [tag] "s"(tag), [n] "n"(SPIN), [w] "s"(w64 + h), [wq] "n"(W_Q) : "memory", "scc", "vcc");
    }
    lds_wait<1>(F + F_U, 15);
    float base[4][2];
    {
      const float m0 = lo2f(sv[0]), m1 = lo2f(sv[2]), s0 = hi2f(sv[0]), s1 = hi2f(sv[2]);
#pragma unroll
      for (int c = 0; c < 4; c++) {
        u32 uu = *(LDS u32*)(L + O_U + c * UP + 512 + t0 * 2), dd = (u32)(dsk >> ((c >> 1) * 32));
        float dk = ((c & 1) ? hi2f(dd) : lo2f(dd)) * 16.f + 16.f;
        base[c][0] = opq(fmaf(lo2f(uu), fmaf(s0, 16.f, dk), m0)); base[c][1] = opq(fmaf(hi2f(uu), fmaf(s1, 16.f, dk), m1));
      }
    }
    lds_wait<1>(F + F_SP, 256);
    const v2u ts = *(LDS v2u*)(F + F_TS);
    const int ts0 = 2047 - (ts[0] & 2047), ts1 = 2047 - (ts[1] & 2047);
    const int n = (w & 1) * 8 + (lane >> 3);
    u64 m0 = __builtin_amdgcn_ballot_w64(t0 != ts0 && t0 != ts1), m1 = __builtin_amdgcn_ballot_w64(t0 + 1 != ts0 && t0 + 1 != ts1);
    u64 op = k4_ + cg8;
    u32 oo = t0 * 1024;
    asm volatile("" : "+v"(oo), "+s"(op), "+s"(m0), "+s"(m1), "+v"(base[0][0]), "+v"(base[1][0]), "+v"(base[2][0]), "+v"(base[3][0]),
                 "+v"(base[0][1]), "+v"(base[1][1]), "+v"(base[2][1]), "+v"(base[3][1]));
    if (!ffn) STAMP(5);
    const bool gath = w64 == 256 && !h;
    v16f qa;
    float sgm; u32 o0 = lane * 16, oa;
    u32 sp = WS_P + cg * PG, sp1 = sp + 1024, sp2 = sp + 2048, sp3 = sp + 3072, c_lo = 0x3f80u, c_hi = 0x3f800000u;
    asm volatile("" : "+s"(sp), "+s"(sp1), "+s"(sp2), "+s"(sp3), "+s"(c_lo), "+s"(c_hi));
#define QARG [o] "v"(o0), [rs] "s"(rs), [s0] "s"(sp), [s1] "s"(sp1), [s2] "s"(sp2), [s3] "s"(sp3)
    asm volatile("" : "={v[32:47]}"(qa), "=v"(sgm), "=v"(oa));
    if (gath) {
      const u32 kk = (lane & 1) ? ts[1] : ts[0];
      const float r = __uint_as_float((kk ^ 0x80000000u ^ ((~(int)kk >> 31) & 0x7fff0000u)) & 0xffff0000u);
      sgm = rbf(__builtin_amdgcn_rcpf(1.f + __builtin_amdgcn_exp2f(r * -1.44269504089f)));
      oa = (~kk & 2047) * 1024 + (kg >> 1) * 4;
      asm volatile("v_mov_b32 v44, 0\n\tv_mov_b32 v45, %[tag]\n\tv_mov_b32 v46, 0\n\tv_mov_b32 v47, %[tag]\n\ts_sleep %[sq]\n\t" KL1(32, 0) KL1(32, 1) KL1(32, 2) "s_mov_b64 exec, %[m]\n\t" KL1(32, 3)
                   "s_mov_b64 exec, 1\n\tds_or_b32 %[f], %[one] offset:%[up]\n\ts_mov_b64 exec, -1"
                   : "+{v[32:47]}"(qa), "+v"(sgm), "+v"(oa)
                   : QARG, [tag] "s"(tag), [sq] "n"(D_Q), [m] "s"(0x3ffffffffull), [f] "v"(F), [one] "v"(1u), [up] "n"(F_UP) : "memory");
      STAMPQ(3);
    }
    v2f pr[8];
    {
      u32 f, sc, cnt;
      asm volatile("s_mov_b32 %10, 0x20000\n"
                   "1:\n\tds_read_b32 %8, %11\n\ts_waitcnt lgkmcnt(0)\n\tv_readfirstlane_b32 %9, %8\n\ts_and_b32 %9, %13, %9\n\t"
                   "s_bcnt1_i32_b32 %9, %9\n\ts_cmp_ge_u32 %9, %14\n\ts_cbranch_scc1 3f\n\ts_sleep 1\n\ts_sub_u32 %10, %10, 1\n\ts_cbranch_scc0 1b\n"
                   "3:\n\tds_read_b32 %8, %11\n\tds_read_b64 %0, %12\n\tds_read_b64 %1, %12 offset:%15\n\tds_read_b64 %2, %12 offset:%15*2\n\t"
                   "ds_read_b64 %3, %12 offset:%15*3\n\tds_read_b64 %4, %12 offset:4096\n\tds_read_b64 %5, %12 offset:%15+4096\n\t"
                   "ds_read_b64 %6, %12 offset:%15*2+4096\n\tds_read_b64 %7, %12 offset:%15*3+4096\n\t"
                   "s_waitcnt lgkmcnt(8)\n\tv_readfirstlane_b32 %9, %8\n\ts_andn2_b32 %9, %13, %9\n\t"
                   "s_cbranch_scc0 2f\n\ts_waitcnt lgkmcnt(0)\n\ts_sub_u32 %10, %10, 1\n\ts_cbranch_scc0 3b\n2:\n\ts_setprio 3\n\ts_waitcnt lgkmcnt(0)"
                   : "=&v"(pr[0]), "=&v"(pr[1]), "=&v"(pr[2]), "=&v"(pr[3]), "=&v"(pr[4]), "=&v"(pr[5]), "=&v"(pr[6]), "=&v"(pr[7]),
                     "=&v"(f), "=&s"(sc), "=&s"(cnt)
                   : "v"(F + F_PH), "v"(L + si * 1024 + n * 64 + 16 * (((lane >> 1) & 3) ^ ((n >> 1) & 3)) + (lane & 1) * 8),
                     "s"(0xf000fu << (4 * si)), "n"(EB), "n"(BC) : "memory", "scc");
    }
    STAMP(6);
#pragma unroll
    for (int c = 0; c < 4; c++) {
      base[c][0] = opq(base[c][0] + opq(pr[c][0]) + opq(pr[4 + c][0])); base[c][1] = opq(base[c][1] + opq(pr[c][1]) + opq(pr[4 + c][1]));
    }
        if (!h && w >= HW) lds_wait<1>(F + F_UP, 1);
    const v2u r0 = {pk(base[0][0], base[1][0]), pk(base[2][0], base[3][0])}, r1 = {pk(base[0][1], base[1][1]), pk(base[2][1], base[3][1])};
    {
      u32 f, sc, cnt;
      asm volatile("s_and_b64 exec, %[m0], %[m1]\n\ts_cmp_eq_u64 exec, -1\n\ts_cbranch_scc1 4f\n\ts_mov_b32 %2, 0x20000\n"
                   "1:\n\tds_read_b32 %0, %[f] offset:%[up]\n\ts_waitcnt lgkmcnt(0)\n\tv_readfirstlane_b32 %1, %0\n\ts_bitcmp1_b32 %1, 1\n\t"
                   "s_cbranch_scc1 2f\n\ts_sub_u32 %2, %2, 1\n\ts_cbranch_scc0 1b\n"
                   "2:\n\ts_not_b64 exec, %[m0]\n\ts_cbranch_scc0 3f\n\t"
                   "global_atomic_pk_add_bf16 %[o], v56, %[p]\n\tglobal_atomic_pk_add_bf16 %[o], v57, %[p] offset:4\n"
                   "3:\n\ts_not_b64 exec, %[m1]\n\ts_cbranch_scc0 4f\n\t"
                   "global_atomic_pk_add_bf16 %[o], v58, %[p] offset:1024\n\tglobal_atomic_pk_add_bf16 %[o], v59, %[p] offset:1028\n"
                   "4:\n\ts_mov_b64 exec, %[m0]\n\tglobal_store_dwordx2 %[o], v[56:57], %[p]\n\ts_mov_b64 exec, %[m1]\n\t"
                   "global_store_dwordx2 %[o], v[58:59], %[p] offset:1024\n\ts_mov_b64 exec, -1"
                   : "=&v"(f), "=&s"(sc), "=&s"(cnt)
                   : [o] "v"(oo), "{v[56:57]}"(r0), "{v[58:59]}"(r1), [p] "s"(op), [m0] "s"(m0), [m1] "s"(m1), [f] "v"(F + F_PH), [up] "n"(F_UP - F_PH)
                   : "memory", "scc");
    }
    if (gath) {
      u32 cnt;
      asm volatile("s_movk_i32 %[c], %[n]\n\ts_waitcnt vmcnt(2)\n"
                   "1:\n\t" KE(32) "s_cbranch_vccz 2f\n\t" PR(0) PR(1) PR(2) PR(3)
                   "s_sub_u32 %[c], %[c], 1\n\ts_waitcnt vmcnt(0)\n\ts_cbranch_scc0 1b\n2:"
                   : "+{v[32:47]}"(qa), [c] "=&s"(cnt)
                   : QARG, [tag] "s"(tag), [n] "n"(SPIN)
                   : "memory", "scc", "vcc", "v48", "v49", "v50", "v51", "v52");
      STAMPQ(4);
      float y[4] = {0.f, 0.f, 0.f, 0.f};
#pragma unroll
      for (int j = 0; j < 4; j++) {
        const u32 lo = __float_as_uint(qa[4 * j]), hi = __float_as_uint(qa[4 * j + 2]);
        y[0] = dot2(lo, c_lo, y[0]); y[1] = dot2(lo, c_hi, y[1]);
        y[2] = dot2(hi, c_lo, y[2]); y[3] = dot2(hi, c_hi, y[3]);
      }
      float f4 = fold4(y[0], y[2], y[1], y[3]);
      f4 += dppf<0x4E>(f4); f4 += dppf<0x124>(f4); f4 += dppf<0x128>(f4);
      asm volatile("s_mov_b64 exec, %3\n\tglobal_atomic_pk_add_bf16 %0, %1, %2\n\ts_mov_b64 exec, -1"
                   :: "v"(oa), "v"(pk(0.f, rbf(f4) * sgm) >> ((~kg & 1) * 16)), "s"(op), "s"(0x0003000300030003ull) : "memory");
    }
    STAMP(7);
    FLUSH();
    asm volatile("s_endpgm" ::: "memory");
  }
  {
    v4u xr, wv;
    asm volatile("" : "={v[12:15]}"(xr), "={v[16:19]}"(wv));
    asm volatile("" : "={s[6:7]}"(dsk), "={s[22:23]}"(k7_));
    SB();
    char* const ws = (char*)k7_;
    const int ch = w & 3, half = (w >> 2) & 1;
    LDS char* const Bc = L + ch * BC;
    LDS char* const Ac = L + O_A + ch * AC;
    LDS char* const Uc = L + O_U + ch * UP;
    const u64 tb = (lane & 16) ? 0xfffff7654f32f10full : 0xcbaaa98876543210ull;
    const u32 ar = (u32)(tb >> (c16 * 4)) & 15;
    LDS char* pa = (ar == 15 ? Uc + 208 - ch * 32 : Ac + half * AH + (kg & 1) * A_UP + ar * RP) + (kg >> 1) * 16;
    LDS char* pb = Bc + half * (BC / 2) + (lane & 31) * RP + (kg >> 1) * 16;
    LDS char* px = Bc + 4096 + lane * 16;
    LDS char* pw;
    asm volatile("" : "+v"(pa), "+v"(pb), "+v"(px));
    v4u b35, b6a, b5b, b4c, b34;
    LDS char *p_q, *p_c, *p_a, *p_u; u32 psel;
    if (half) {
      p_q = Ac + A_HI + (kg >> 1) * 256 + (lane & 31) * 2;
      p_c = Ac + 32 + (lane & 31) * 16 + (kg >> 1) * 8;
      p_a = Ac + A_HI + (c16 - 4 * kg - 3) * 4;
      p_u = Uc + 512 - 48 + (h ? 0 : 1024) + c16 * 32 + kg * 16;
      pw = Bc + 4096 + c16 * 64 + 16 * (kg ^ ((c16 >> 1) & 3));
      asm volatile("" : "+v"(p_q), "+v"(p_c), "+v"(p_a), "+v"(p_u), "+v"(pw));
    }
    v8bf fb[8];
    {
      asm volatile("s_waitcnt lgkmcnt(0)" : "+{s[6:7]}"(dsk) :: "memory");
      const float dt = rbf(__builtin_amdgcn_exp2f(lo2f((u32)(dsk >> (ch * 16))) * 1.44269504089f));
      float d1 = dt * 1.44269504089f, d2 = dt * 0.15915494309f, d16 = dt * 16.f;
      asm volatile("s_mov_b32 m0, %5\n\ts_waitcnt vmcnt(0)"
                   : "+{v[12:15]}"(xr), "+{v[16:19]}"(wv), "+v"(d1), "+v"(d2), "+v"(d16) : "s"(ch * BC + half * (BC / 2)) : "memory");
      STAMP(1);
      const float er = d1 * lo2f(xr[0]), ph = d2 * lo2f(xr[1]);
      const v2f z = cpow(er, ph), E = cpow(er * 256.f, ph * 256.f);
      const v2f z2 = cmul(z, z), cb = cmul((v2f){lo2f(wv[0]), lo2f(wv[1])}, (v2f){lo2f(xr[2]), lo2f(xr[3])} * d16);
      const v2f z4 = cmul(z2, z2), z8 = cmul(z4, z4);
      v2f o = {z8.x, -z8.y};
      const v2f m = cmul(o, o);
      v2f e = m;
      put1<0>(0x3f80u); put1<RP>(pk(o.x, o.y)); put1<2 * RP>(pk(e.x, e.y));
      brows<2>(e, o, m);
      v2f x1, y, c0, c1;
      step2<31 * RP, 31 * RP>(x1, cb, E, o, o, m);
      asm volatile("s_mov_b32 m0, %0" :: "s"(20000 + ch * AC + half * AH) : "memory");
#pragma unroll
      for (int k = 0; k < 8; k++) fb[k] = *(LDS v8bf*)(pb + k * 32);
      MEMB();
#define AL(r) (AOFF + (r) * RP)
#define AU(r) (AOFF + A_UP + (r) * RP)
      put1<AL(0)>(pk(cb.x, cb.y)); put1<AL(8)>(pk(x1.x, x1.y));
      step2<AL(1), AL(12)>(c1, cb, z, y, x1, z8);
      step2<AL(2), AU(2)>(c0, cb, z2, y, y, E);
      step2<AL(3), AU(3)>(c1, c1, z2, y, y, E);
      step2<AL(4), AU(4)>(c0, c0, z2, y, y, E);
      step2<AL(5), AU(5)>(c1, c1, z2, y, y, E);
      step2<AL(6), AU(6)>(c0, c0, z2, y, y, E);
      step2<AL(9), AL(10)>(c0, x1, z2, e, x1, z4);
      step2<AL(7), AU(7)>(c1, c1, z2, y, y, E);
      step2<AL(11), AU(0)>(c0, e, z2, e, e, E);
      e = cmul(e, E); put1<AU(1)>(pk(e.x, e.y));
    }
    STAMP(2);
    v16f e;
    v8bf fa[8];
#pragma unroll
    for (int k = 0; k < 8; k++) fa[k] = *(LDS v8bf*)(pa + k * 32);
#define MX(k) e = __builtin_amdgcn_mfma_f32_32x32x16_bf16(fa[k], fb[k], e, 0, 0, 0); SB()
    if (half) {
      v4f q[4]; u32 f, sc, cnt;
      int be = c16 + 16 - 8 * kg;
      asm volatile("s_mov_b32 %6, 0x20000\n"
                   "1:\n\tds_read_b32 %4, %8\n\tds_read_b128 %0, %9\n\tds_read_b128 %1, %9 offset:1024\n\t"
                   "ds_read_b128 %2, %9 offset:2048\n\tds_read_b128 %3, %9 offset:3072\n\t"
                   "s_waitcnt lgkmcnt(4)\n\tv_readfirstlane_b32 %5, %4\n\ts_bitcmp1_b32 %5, %10\n\t"
                   "s_cbranch_scc1 2f\n\ts_sub_u32 %6, %6, 1\n\ts_cbranch_scc0 1b\n2:\n\ts_waitcnt lgkmcnt(0)"
                   : "=&v"(q[0]), "=&v"(q[1]), "=&v"(q[2]), "=&v"(q[3]), "=&v"(f), "=&s"(sc), "=&s"(cnt), "+v"(be)
                   : "v"(F + F_X), "v"(px), "s"(ch) : "memory", "scc");
#pragma unroll
      for (int i = 0; i < 16; i++) e[i] = q[i >> 2][i & 3];
      SB();
      STAMP(6);
      MX(0);
      const v2s d3 = {(short)(be - 3), (short)(be - 4)};
      u32 n[17];
#pragma unroll
      for (int i = 0; i < 5; i += 2) n[i] = __builtin_bit_cast(u32, (v2s)((d3 - (v2s){(short)i, (short)i}) >> 15));
      MX(1);
#pragma unroll
      for (int i = 6; i < 11; i += 2) n[i] = __builtin_bit_cast(u32, (v2s)((d3 - (v2s){(short)i, (short)i}) >> 15));
      MX(2);
#pragma unroll
      for (int i = 12; i < 17; i += 2) n[i] = __builtin_bit_cast(u32, (v2s)((d3 - (v2s){(short)i, (short)i}) >> 15));
      MX(3);
#pragma unroll
      for (int i = 1; i < 13; i += 2) n[i] = __builtin_amdgcn_alignbit(n[i + 1], n[i - 1], 16);
      MX(4);
#pragma unroll
      for (int i = 13; i < 17; i += 2) n[i] = __builtin_amdgcn_alignbit(n[i + 1], n[i - 1], 16);
      b35 = band<3, 5>(n, 0x3f803f80u);
      MX(5);
      b6a = band<6, 10>(n, 0x3f803f80u);
      MX(6);
      b5b = band<5, 11>(n, 0x3f803f80u);
      MX(7);
      b4c = band<4, 12>(n, 0x3f803f80u); b34 = band<3, 4>(n, ~0u);
      asm volatile("" : "+v"(b35), "+v"(b6a), "+v"(b5b), "+v"(b4c), "+v"(b34));
      SB();
    } else {
#pragma unroll
      for (int i = 0; i < 16; i++) e[i] = 0.f;
      SB();
      MX(0); MX(1); MX(2); MX(3); MX(4); MX(5); MX(6); MX(7);
    }
    const u32 umask = 15;
    if (!half) {
#pragma unroll
      for (int i = 0; i < 4; i++) *(LDS v4f*)(px + i * 1024) = (v4f){e[4 * i], e[4 * i + 1], e[4 * i + 2], e[4 * i + 3]};
      lds_sig(F + F_X, 1u << ch);
      STAMP(7);
      int ln = lane;
      asm volatile("s_setprio 0\n\ts_sleep %1" : "+v"(ln) : "n"(D_M) : "memory");
      const int c6 = ln & 15, k4 = ln >> 4;
      pw = Bc + c6 * 64 + 16 * (k4 ^ ((c6 >> 1) & 3));
      p_c = Ac + (k4 >> 1) * (AH + 128) + (k4 & 1) * 1344 + c6 * 4;
      p_q = p_c - (ln & 8) * 4;
      p_a = Ac + ((ln >> 3) & 1) * (AH + 128) + ((ln >> 2) & 1) * 1344 + 56 - k4 * 16 + (ln & 3) * 8;
      p_u = Uc + 32 - 1024 + h * 512 + c6 * 32 + k4 * 16;
      const int i0 = 2 * c6 - k4;
      const u32 sl = i0 > 2 ? 0x0c0cu : i0 == -3 ? 0x0302u : i0 == -2 ? 0x0504u : 0x0706u, sh = i0 > 1 ? 0x0c0cu : i0 == -3 ? 0x0504u : 0x0706u;
      psel = sl | sh << 16;
      asm volatile("" : "+v"(p_q), "+v"(p_c), "+v"(p_a), "+v"(p_u), "+v"(psel), "+v"(pw));
      LDS char* p = Ac + 32 + ln * 16;
      *(LDS u32*)(Ac + (ln & 7) * 4) = 0;
      v4u un; v2u pv;
      {
        u32 f, sc, cnt;
        asm volatile("s_mov_b32 %4, 0x20000\n"
                     "1:\n\ts_sleep 1\n\tds_read_b32 %2, %5\n\ts_waitcnt lgkmcnt(0)\n\tv_readfirstlane_b32 %3, %2\n\ts_bitcmp1_b32 %3, %7\n\t"
                     "s_cbranch_scc1 2f\n\ts_sub_u32 %4, %4, 1\n\ts_cbranch_scc0 1b\n"
                     "2:\n\ts_setprio %11\n\tds_read_b128 %0, %6 offset:8\n\tds_read_b64 %1, %6\n\tds_write_b32 %8, %9\n\ts_waitcnt lgkmcnt(1)"
                     : "=&v"(un), "=&v"(pv), "=&v"(f), "=&s"(sc), "=&s"(cnt)
                     : "v"(F + F_X), "v"(p - 8), "s"(ch + 4), "v"(p_q), "v"(0u), "n"(D_M), "n"(PM) : "memory", "scc");
      }
      *(LDS u32*)(p_c + 1056) = __builtin_amdgcn_perm(__builtin_amdgcn_readlane(un[3], 63), __builtin_amdgcn_readlane(un[2], 63), psel);
      const u32 a0 = __builtin_amdgcn_alignbit(pv[1], pv[0], 16), a1 = __builtin_amdgcn_alignbit(un[0], pv[1], 16),
                a2 = __builtin_amdgcn_alignbit(un[1], un[0], 16), a3 = __builtin_amdgcn_alignbit(un[2], un[1], 16),
                a4 = __builtin_amdgcn_alignbit(un[3], un[2], 16);
      *(LDS v4u*)(p + 1344) = (v4u){a1, a2, a3, a4};
      *(LDS v4u*)(p + AH + 128) = (v4u){pv[1], un[0], un[1], un[2]};
      *(LDS v4u*)(p + AH + 128 + 1344) = (v4u){a0, a1, a2, a3};
      STAMP(3);
      u32 c0, c1, si = 0;
      asm volatile(
        LA8(40, 512) LA8(8, 0) LA(72, 0, 576)
        "v_mov_b32 v4, 0\n\tv_mov_b64 v[112:113], 0\n\tv_mov_b64 v[114:115], 0\n\ts_setprio %[pm]\n\ts_mov_b32 %1, 0x20000\n" ST_(4)
        "1:\n\tds_read_b32 v5, v3\n\t" LB8 "s_waitcnt lgkmcnt(8)\n\tv_readfirstlane_b32 %0, v5\n\ts_andn2_b32 %0, %[msk], %0\n\t"
        "s_cbranch_scc0 2f\n\ts_sleep 1\n\ts_sub_u32 %1, %1, 1\n\ts_cbranch_scc0 1b\n2:\n\t"
        "s_cmp_eq_u32 %[h], 0\n\ts_cbranch_scc1 4f\n\ts_branch 6f\n"
        "3:\n\ts_waitcnt lgkmcnt(8)\n\tv_mfma_f32_16x16x32_bf16 " AX ", v[72:75], v[108:111], " AX "\n"
        "4:\n\t" H0(0, AX) H0(1, AX) H0(2, AX) H0(3, AX) H0(4, AX) H0(5, AX) H0(6, AX) H0(7, AY)
        "v_add_u32 v2, %0, v2\n\ts_movk_i32 %0, 0x400\n\tv_mov_b32 v4, %[bit]\n\ts_lshl_b32 %[bit], %[bit], 4\n\t"
        "s_add_u32 %2, %2, 1\n\ts_cmp_eq_u32 %2, 4\n\ts_cbranch_scc1 7f\n\t"
        "s_cmp_lg_u32 %2, %[jmp]\n\ts_cbranch_scc1 6f\n\t"
        "v_add_u32 v1, 0x800, v1\n\t" LB8
        "6:\n\tv_add_u32 v1, 0x200, v1\n\tds_read_b128 v[108:111], v1 offset:448\n\t"
        H1(0, "0") H1(1, AX) PUB H1(2, AX) H1(3, AX) H1(4, AX) H1(5, AX) H1(6, AX) H1(7, AX)
        "s_cmp_lg_u32 %2, %[one]\n\ts_cbranch_scc1 3b\n\ts_branch 4b\n"
        "7:\n\ts_nop 2\n\t" PUB ST_(6) FLUSH_
        "s_waitcnt lgkmcnt(0)\n\ts_endpgm"
        : "=&s"(c0), "=&s"(c1), "+s"(si)
        : "{v0}"(p_a), "{v1}"(p_u), "{v2}"(pw), "{v3}"(F + F_U), [msk] "s"(umask), [bit] "s"(1u << ch),
          [h] "s"(h), [jmp] "s"(h ? 9 : 2), [one] "s"(h ? 9 : 1), [pm] "n"(PM2)
          , STADDR
        : "memory", "scc", "v127");
    }
    {
      LDS char* const T = Ac + A_HI;
      {
        *(LDS v2u*)p_c = (v2u){pk(e[0], e[1]), pk(e[2], e[3])};
        *(LDS v2u*)(p_c + 512) = (v2u){pk(e[4], e[5]), pk(e[6], e[7])};
        lds_sig(F + F_X, 16u << ch);
        STAMP(7);
#pragma unroll
        for (int i = 0; i < 4; i++)
          asm volatile("ds_write_b16 %0, %1 offset:%2\n\tds_write_b16_d16_hi %0, %1 offset:%2+64"
                       :: "v"(p_q), "v"(pk(e[8 + 2 * i], e[9 + 2 * i])), "n"((i >> 1) * 512 + (i & 1) * 128) : "memory");
      }
      MEMB();
      LDS char* tp = p_a;
      LDS char* pu = p_u;
      v8bf ta[3], tb4[3], t8[2], ua[4], ub[4];
      v2u q[16]; u32 kk;
      STAMP(3);
      {
        u32 f, sc, cnt;
#define UR(a, b, j) "ds_read_b128 %" #a ", %[u] offset:512*" #j "+16\n\tds_read_b128 %" #b ", %[u] offset:512*" #j "\n\t"
        asm volatile("s_setprio %[pr]\n\ts_mov_b32 %22, 0x20000\n\tds_read_b32 %20, %[f]\n\t" UR(12, 13, 0) UR(14, 15, 1) UR(16, 17, 2) UR(18, 19, 3)
                     "s_waitcnt lgkmcnt(8)\n\tv_readfirstlane_b32 %21, %20\n\ts_andn2_b32 %21, 15, %21\n\ts_cbranch_scc0 2f\n"
                     "1:\n\ts_sleep 1\n\tds_read_b32 %20, %[f]\n\ts_waitcnt lgkmcnt(0)\n\tv_readfirstlane_b32 %21, %20\n\ts_andn2_b32 %21, 15, %21\n\t"
                     "s_cbranch_scc0 3f\n\ts_sub_u32 %22, %22, 1\n\ts_cbranch_scc0 1b\n3:\n\t"
                     UR(12, 13, 0) UR(14, 15, 1) UR(16, 17, 2) UR(18, 19, 3) "2:\n\t" ST_(4)
                     TB(0, 1, 1) TB(2, 3, 2) TB(4, 5, 3) TB(6, 7, 4) TB(8, 9, 5) TB(10, 11, 6) "s_waitcnt lgkmcnt(" LG12 ")"
                     : "=&v"(q[0]), "=&v"(q[1]), "=&v"(q[2]), "=&v"(q[3]), "=&v"(q[4]), "=&v"(q[5]), "=&v"(q[6]), "=&v"(q[7]),
                       "=&v"(q[8]), "=&v"(q[9]), "=&v"(q[10]), "=&v"(q[11]), "=&v"(ua[0]), "=&v"(ub[0]), "=&v"(ua[1]), "=&v"(ub[1]),
                       "=&v"(ua[2]), "=&v"(ub[2]), "=&v"(ua[3]), "=&v"(ub[3]), "=&v"(f), "=&s"(sc), "=&s"(cnt)
                     : [p] "v"(tp), [u] "v"(pu), [f] "v"(F + F_U), [pr] "n"(PF) : "memory", "scc");
      }
      const v4f zz = {0.f, 0.f, 0.f, 0.f};
      v4f r0 = zz, r1 = zz, r2 = zz, r3 = zz;
      v8bf wa0 = wop(ua[0], ub[0], b35, b35), wb0 = wop(ua[0], ub[0], b6a, b6a);
      asm volatile(TB(12, 13, 7) TB(14, 15, 8) "ds_read_b32 %16, %[t] offset:380\n\ts_waitcnt lgkmcnt(5)"
                   : "+v"(q[0]), "+v"(q[1]), "+v"(q[2]), "+v"(q[3]), "+v"(q[4]), "+v"(q[5]), "+v"(q[6]), "+v"(q[7]),
                     "+v"(q[8]), "+v"(q[9]), "+v"(q[10]), "+v"(q[11]), "=&v"(q[12]), "=&v"(q[13]), "=&v"(q[14]), "=&v"(q[15]), "=&v"(kk), "+v"(wa0)
                   : [p] "v"(tp), [t] "v"(T) : "memory");
#pragma unroll
      for (int i = 0; i < 3; i++) { ta[i] = cat2(q[2 * i], q[2 * i + 1]); tb4[i] = cat2(q[6 + 2 * i], q[7 + 2 * i]); }
      v8bf wa1 = wop(ua[1], ub[1], b35, b35), wb1 = wop(ua[1], ub[1], b6a, b6a);
      asm volatile("s_waitcnt lgkmcnt(0)" : "+v"(q[12]), "+v"(q[13]), "+v"(q[14]), "+v"(q[15]), "+v"(kk), "+v"(wa1) :: "memory");
      t8[0] = cat2(q[12], q[13]); t8[1] = cat2(q[14], q[15]);
      kk = __builtin_amdgcn_perm(kk, kk, 0x03020302u);
#pragma unroll
      for (int j = 0; j < 4; j++) b34[j] &= kk;
      if (h) {
        r0 = MF(wa0, ta[0], r0); r1 = MF(wa0, ta[1], r1); r0 = MF(wb0, tb4[0], r0); r1 = MF(wb0, tb4[1], r1);
        r1 = MF(wa1, ta[0], r1); r1 = MF(wb1, tb4[0], r1);
      }
      r2 = MF(wa0, ta[2], r2); r3 = MF(wa1, ta[2], r3); r2 = MF(wb0, tb4[2], r2); r3 = MF(wb1, tb4[2], r3);
      *(LDS v4f*)pw = r0;
      *(LDS v4f*)(pw + 1024) = r1;
      lds_sig(F + F_PH, 0x110000u << ch);
      r2 = MF(wa1, ta[1], r2); r2 = MF(wb1, tb4[1], r2);
      {
        v8bf wa2 = wop(ua[2], ub[2], b35, b35), wb2 = wop(ua[2], ub[2], b6a, b6a), w80 = wop(ua[0], ub[0], b5b, b4c);
        r2 = MF(BF8(b34), ua[0], r2); r3 = MF(BF8(b34), ua[1], r3);
        r2 = MF(wa2, ta[0], r2); r3 = MF(wa2, ta[1], r3); r2 = MF(wb2, tb4[0], r2); r3 = MF(wb2, tb4[1], r3);
        r2 = MF(w80, t8[0], r2); r3 = MF(w80, t8[1], r3);
      }
      if (!h) {
        v8bf va0 = *(LDS v8bf*)(pu - 1024 + 16), vb0 = *(LDS v8bf*)(pu - 1024), va1 = *(LDS v8bf*)(pu - 512 + 16), vb1 = *(LDS v8bf*)(pu - 512);
        asm volatile(TB(0, 1, 9) TB(2, 3, 10) "s_waitcnt lgkmcnt(0)" : "=&v"(q[0]), "=&v"(q[1]), "=&v"(q[2]), "=&v"(q[3]) : [p] "v"(tp) : "memory");
        const v8bf t2 = cat2(q[0], q[1]), t3 = cat2(q[2], q[3]);
        v8bf x0 = wop(va0, vb0, b5b, b4c), x1 = wop(va1, vb1, b5b, b4c);
        r2 = MF(x0, t2, r2); r3 = MF(x0, t3, r3); r2 = MF(x1, t8[1], r2); r3 = MF(x1, t2, r3);
      }
      {
        v8bf wa3 = wop(ua[3], ub[3], b35, b35), wb3 = wop(ua[3], ub[3], b6a, b6a), w81 = wop(ua[1], ub[1], b5b, b4c);
        *(LDS v4f*)(pw + 2048) = r2;
        lds_sig(F + F_PH, 0x1000000u << ch);
        r3 = MF(wa3, ta[0], r3); r3 = MF(wb3, tb4[0], r3); r3 = MF(w81, t8[0], r3);
      }
      *(LDS v4f*)(pw + 3072) = r3;
      lds_sig(F + F_PH, 0x10000000u << ch);
      STAMP(5);
      FLUSH();
    }
  }
}

__global__ void __launch_bounds__(64) kprobe(char* ws) {
  u64 ts; asm volatile("s_memrealtime %0\n\ts_waitcnt lgkmcnt(0)" : "=s"(ts) :: "memory");
  const int lane = threadIdx.x;
  const u64* st = (const u64*)(ws + WS_STAMP);
  u64 t0 = ~0ull, mn = ~0ull, mx = 0;
  for (int j = lane; j < 256; j += 64) { u64 v = st[(j * 16) * 8]; t0 = v < t0 ? v : t0; }
  for (int j = lane; j < 256 * PW; j += 64) { int b = j / PW; if ((PX >> (b & 7)) & 1) { u64 v = st[(b * 16 + PWO + j % PW) * 8 + PI]; if (v) { mn = v < mn ? v : mn; mx = v > mx ? v : mx; } } }
  for (int o = 32; o; o >>= 1) {
    u64 a = __shfl_xor(t0, o), b = __shfl_xor(mn, o), c = __shfl_xor(mx, o);
    t0 = a < t0 ? a : t0; mn = b < mn ? b : mn; mx = c > mx ? c : mx;
  }
  u64 x = (PMODE == 0 ? t0 : PMODE == 1 ? mn : mx) - t0;
  if (x > 5000) x = 5000;
  u64 t1;
  u64 end = t0 + 3000 + 10 * x;
  for (int i = 0; i < 2000000; i++) {
    asm volatile("s_memrealtime %0\n\ts_waitcnt lgkmcnt(0)" : "=s"(t1) :: "memory");
    if (t1 >= end) break;
  }
}

extern "C" void kernel_launch(void* const* d_in, const int* in_sizes, int n_in, void* d_out, int out_size, void* d_ws,
                              size_t ws_size, hipStream_t stream) {
  kf<<<256, 1024, 0, stream>>>(d_in[0], d_in[10], d_ws, d_in[8], d_out, d_in[11], d_in[12], d_in[13],
                               d_in[1], d_in[2], d_in[3], d_in[4], d_in[5], d_in[6], d_in[7], d_ws);
  kprobe<<<1, 64, 0, stream>>>((char*)d_ws);
}
```

```cpp
#include <hip/hip_runtime.h>

typedef unsigned short u16;
typedef unsigned u32;
typedef unsigned long long u64;
typedef u32 v4u __attribute__((ext_vector_type(4)));
typedef u32 v2u __attribute__((ext_vector_type(2)));
typedef float v4f __attribute__((ext_vector_type(4)));
typedef float v2f __attribute__((ext_vector_type(2)));
typedef __bf16 v8bf __attribute__((ext_vector_type(8)));
typedef __bf16 v2bf __attribute__((ext_vector_type(2)));
typedef float v16f __attribute__((ext_vector_type(16)));
typedef short v2s __attribute__((ext_vector_type(2)));
typedef short v4s __attribute__((ext_vector_type(4)));
#define LDS __attribute__((address_space(3)))
#define GLB __attribute__((address_space(1)))
#define CST __attribute__((address_space(4)))
#define DEVI __device__ __forceinline__
#define SB() __builtin_amdgcn_sched_barrier(0)
#define MEMB() asm volatile("" ::: "memory")

#define WS_R 4096
#define WS_S 8192
#define WS_P 32768
#define WS_X (1u << 20)
#define WS_END (3u << 20)
#define SPIN 4000
#define GS 0
#define D_G 2
#define XL 0
#define D_AG 30
#define D_KG 30
#define D_P 8
#define D_R 6
#define CODE_SZ 15160
#define D_K2 6
#define D_D 10
#define D_QG 0
#define D_A 20
#define D_W 4
#define D_K 30
#define D_Q 0

#define ST_(i)
#define STAMP(i)
#define STAMPD(i, v)
#define FLUSH_
#define FLUSH()

DEVI u32 pk(float a, float b) { return __builtin_bit_cast(u32, __builtin_convertvector((v2f){a, b}, v2bf)); }
DEVI float opq(float a) { asm("" : "+v"(a)); return a; }
DEVI u32 pks(float a, float b) { return pk(opq(a), opq(b)); }
DEVI float rbf(float a) { return __uint_as_float(pk(0.f, a)); }
DEVI float lo2f(u32 v) { return __uint_as_float(v << 16); }
DEVI float hi2f(u32 v) { return __uint_as_float(v & 0xffff0000u); }
DEVI float dot2(u32 a, u32 b, float c) { return __builtin_amdgcn_fdot2_f32_bf16(__builtin_bit_cast(v2bf, a), __builtin_bit_cast(v2bf, b), c, false); }
template <int CTRL> DEVI float dppf(float v) { return __int_as_float(__builtin_amdgcn_update_dpp(0, __float_as_int(v), CTRL, 0xf, 0xf, true)); }
template <int CTRL, int RM> DEVI u32 dppu(u32 v) { return (u32)__builtin_amdgcn_update_dpp(0, (int)v, CTRL, RM, 0xf, false); }
DEVI float rdl(float v, int l) { return __int_as_float(__builtin_amdgcn_readlane(__float_as_int(v), l)); }
DEVI float fold4(float a, float b, float c, float d) {
  v2u p = __builtin_amdgcn_permlane32_swap(__float_as_uint(a), __float_as_uint(b), false, false);
  v2u q = __builtin_amdgcn_permlane32_swap(__float_as_uint(c), __float_as_uint(d), false, false);
  float t = __uint_as_float(p[0]) + __uint_as_float(p[1]), s = __uint_as_float(q[0]) + __uint_as_float(q[1]);
  v2u r = __builtin_amdgcn_permlane16_swap(__float_as_uint(t), __float_as_uint(s), false, false);
  return __uint_as_float(r[0]) + __uint_as_float(r[1]);
}
DEVI u32 wave_max(u32 v) {
  v = max(v, dppu<0xB1, 0xf>(v)); v = max(v, dppu<0x4E, 0xf>(v)); v = max(v, dppu<0x141, 0xf>(v)); v = max(v, dppu<0x140, 0xf>(v));
  v = max(v, dppu<0x142, 0xa>(v)); v = max(v, dppu<0x143, 0xc>(v));
  return (u32)__builtin_amdgcn_readlane((int)v, 63);
}
DEVI v4s trd(LDS char* p) { return __builtin_amdgcn_ds_read_tr16_b64_v4i16((LDS v4s*)p); }
DEVI v8bf cat(v4s a, v4s b) { return __builtin_bit_cast(v8bf, __builtin_shufflevector(a, b, 0, 1, 2, 3, 4, 5, 6, 7)); }
template <int SL> DEVI void lds_wait(LDS char* p, u32 m) {
  u32 v, s, c;
  asm volatile("s_mov_b32 %2, 0x20000\n"
               "1:\n\tds_read_b32 %0, %3\n\ts_waitcnt lgkmcnt(0)\n\tv_readfirstlane_b32 %1, %0\n\ts_andn2_b32 %1, %4, %1\n\t"
               "s_cbranch_scc0 2f\n\ts_sleep %5\n\ts_sub_u32 %2, %2, 1\n\ts_cbranch_scc0 1b\n2:"
               : "=&v"(v), "=&s"(s), "=&s"(c) : "v"(p), "s"(m), "n"(SL) : "memory", "scc");
}
DEVI void lds_sig(LDS char* p, u32 bit) {
  asm volatile("s_mov_b64 exec, 1\n\tds_or_b32 %0, %1\n\ts_mov_b64 exec, -1" :: "v"(p), "v"(bit) : "memory");
}
DEVI void merge2(u32& a0, u32& a1, u32 b0, u32 b1) { u32 m = min(a0, b0); a0 = max(a0, b0); a1 = max(m, max(a1, b1)); }
DEVI v2f cpow(float er, float ph) {
  float e = __builtin_amdgcn_exp2f(er), f = __builtin_amdgcn_fractf(ph);
  return (v2f){__builtin_amdgcn_cosf(f), __builtin_amdgcn_sinf(f)} * e;
}

#define RP 272
#define BC 17408
#define O_A 69632
#define AC 11776
#define AH 5888
#define A_HI 3584
#define A_UP 3616
#define O_U (O_A + 4 * AC)
#define UP 4640
#define O_F (O_U + 4 * UP)
#define O_LN (O_F + 768)
#define O_W (O_LN + 8704)
#define F_KS 0
#define F_U 64
#define F_PH 68
#define F_EX 72
#define F_SP 76
#define F_TS 80
#define F_PB 88
#define F_X 92
#define F_LN 96
#define F_ST 104
#define F_SV 128
#define F_UP 160
#define F_E 256
#define O_XR (O_W + 16384)
#define LDS_SZ (O_XR + 2688)
#define AOFF 49632

#define CM1(t, x, m) "v_pk_mul_f32 " t ", " x ", " m " op_sel_hi:[0,1]\n\t"
#define CM2(r, t, x, m) "v_pk_fma_f32 " r ", " x ", " m ", " t " op_sel:[1,1,0] op_sel_hi:[1,0,1] neg_lo:[0,1,0]\n\t"
DEVI v2f cmul(v2f x, v2f m) {
  v2f t, r;
  asm(CM1("%1", "%2", "%3") CM2("%0", "%1", "%2", "%3") : "=v"(r), "=&v"(t) : "v"(x), "v"(m));
  return r;
}
template <int O1, int O2> DEVI void step2(v2f& r1, v2f x1, v2f m1, v2f& r2, v2f x2, v2f m2) {
  v2f t1, t2;
  asm volatile(CM1("%2", "%4", "%5") CM1("%3", "%6", "%7") CM2("%0", "%2", "%4", "%5") CM2("%1", "%3", "%6", "%7")
               : "=&v"(r1), "=v"(r2), "=&v"(t1), "=&v"(t2) : "v"(x1), "v"(m1), "v"(x2), "v"(m2));
  u32 p1 = pk(r1.x, r1.y), p2 = pk(r2.x, r2.y);
  asm volatile("ds_write_addtid_b32 %0 offset:%2\n\tds_write_addtid_b32 %1 offset:%3" :: "v"(p1), "v"(p2), "n"(O1), "n"(O2) : "memory");
}
template <int O1> DEVI void put1(u32 p) { asm volatile("ds_write_addtid_b32 %0 offset:%1" :: "v"(p), "n"(O1) : "memory"); }
template <int J> DEVI void brows(v2f& e, v2f& o, v2f m) {
  if constexpr (J < 16) { step2<2 * J * RP, (2 * J - 1) * RP>(e, e, m, o, o, m); brows<J + 1>(e, o, m); }
}
#define MF(a, b, c) __builtin_amdgcn_mfma_f32_16x16x32_bf16(a, b, c, 0, 0, 0)
#define BF8(x) __builtin_bit_cast(v8bf, x)
template <int LO, int HI> DEVI v4u band(const u32* n, u32 one) {
  v4u r;
#pragma unroll
  for (int j = 0; j < 4; j++) r[j] = __builtin_amdgcn_bitop3_b32(n[LO - 3 + 2 * j], n[HI - 2 + 2 * j], one, 0x08);
  return r;
}
DEVI v8bf wop(v8bf a, v8bf b, v4u ba, v4u bb) {
  const v4f zz = {0.f, 0.f, 0.f, 0.f};
  v4f p = MF(a, BF8(ba), zz), q = MF(b, BF8(bb), zz);
  return BF8(((v4u){pk(p[0], q[0]), pk(p[1], q[1]), pk(p[2], q[2]), pk(p[3], q[3])}));
}
#define TB(a, b, s) "ds_read2_b32 %" #a ", %[p] offset0:16*" #s "+3 offset1:16*" #s "+2\n\tds_read2_b32 %" #b ", %[p] offset0:16*" #s "+1 offset1:16*" #s "\n\t"
DEVI v8bf cat2(v2u a, v2u b) { return BF8(__builtin_shufflevector(a, b, 0, 1, 2, 3)); }

#define VR(b, j) "v[" #b "+4*" #j ":" #b "+3+4*" #j "]"
#define LA(b, j, o) "ds_read_b64_tr_b16 v[" #b "+4*" #j ":" #b "+1+4*" #j "], v0 offset:" #o "+456-64*" #j "\n\t" \
                    "ds_read_b64_tr_b16 v[" #b "+2+4*" #j ":" #b "+3+4*" #j "], v0 offset:" #o "+448-64*" #j "\n\t"
#define LA8(b, o) LA(b, 0, o) LA(b, 1, o) LA(b, 2, o) LA(b, 3, o) LA(b, 4, o) LA(b, 5, o) LA(b, 6, o) LA(b, 7, o)
#define LB(j) "ds_read_b128 " VR(76, j) ", v1 offset:1024+64*" #j "\n\t"
#define LB8 LB(0) LB(1) LB(2) LB(3) LB(4) LB(5) LB(6) LB(7)
#define AX "v[112:115]"
#define AY "v[116:119]"
#define MA(d, a, j, c) "v_mfma_f32_16x16x32_bf16 " d ", " VR(a, j) ", " VR(76, j) ", " c "\n\t"
#define H0(j, d) "s_waitcnt lgkmcnt(7-" #j ")\n\t" MA(d, 8, j, AX)
#define H1(j, c) "s_waitcnt lgkmcnt(8)\n\t" MA(AX, 40, j, c) LB(j)
#define PUB "ds_write_b128 v2, " AY "\n\ts_mov_b64 exec, 1\n\tds_or_b32 v3, v4 offset:4\n\ts_mov_b64 exec, -1\n\t"
#define WG_ "global_load_lds_dwordx4 %[go], %[wg]\n\ts_add_u32 m0, m0, 0x400\n\tv_add_u32 %[go], 0x1c000, %[go]\n\t"
#define WU(j) "global_load_dwordx4 %" #j ", %[gu], %[wu]\n\tv_add_u32 %[gu], 0x1c000, %[gu]\n\t"
#define WD(j) "global_load_lds_dwordx4 %[dd], %[wd] offset:64*" #j "\n\ts_add_u32 m0, m0, 960\n\tglobal_load_dwordx4 %" #j ", %[dd], %[wd] offset:512+64*" #j "\n\t"
#define XR(j) "ds_read_b128 v[32+4*" #j ":35+4*" #j "], %[x] offset:64*" #j "\n\t"
#define XM(j, a, b, c, d) "s_waitcnt lgkmcnt(7-" #j ")\n\tv_mfma_f32_16x16x32_bf16 v[64:67], %" #a ", v[32+4*" #j ":35+4*" #j "], " c "\n\t" \
                          "v_mfma_f32_16x16x32_bf16 v[68:71], %" #b ", v[32+4*" #j ":35+4*" #j "], " d "\n\t"
#define XA(j, a, b) XM(j, a, b, "v[64:67]", "v[68:71]")
#define XN(r) "v_fmac_f32_dpp v" #r ", v" #r ", v73 quad_perm:[2,2,2,2] row_mask:0xf bank_mask:0xf\n\t"

#define TM 0x40004000u
#define PL(j) "buffer_load_dwordx4 v[32+4*" #j ":35+4*" #j "], %[o], %[rs], %[s" #j "] offen sc1\n\t"
#define PE(j) "v_bitop3_b32 v[48+" #j "], v[33+4*" #j "], v[35+4*" #j "], %[tag] bitop3:0x7e\n\t"
#define PR(j) "v_cmp_ne_u32 vcc, 0, v[48+" #j "]\n\ts_mov_b64 exec, vcc\n\t" PL(j) "s_mov_b64 exec, -1\n\t"
DEVI void poll4(v4u* a, __amdgpu_buffer_rsrc_t rs, u32 o, u32 s0, u32 s1, u32 s2, u32 s3, u32 tag, u64 m3) {
  u32 cnt;
  asm volatile(PL(0) PL(1) PL(2) "s_mov_b64 exec, %[m3]\n\t" PL(3) "s_mov_b64 exec, -1\n\ts_movk_i32 %4, %[n]\n"
               "1:\n\ts_waitcnt vmcnt(0)\n\t" PE(0) PE(1) PE(2) PE(3)
               "v_or3_b32 v52, v48, v49, v50\n\tv_or_b32 v52, v52, v51\n\tv_cmp_ne_u32 vcc, 0, v52\n\ts_cbranch_vccz 2f\n\t"
               PR(0) PR(1) PR(2) PR(3) "s_sub_u32 %4, %4, 1\n\ts_cbranch_scc0 1b\n\ts_waitcnt vmcnt(0)\n2:"
               : "=&{v[32:35]}"(a[0]), "=&{v[36:39]}"(a[1]), "=&{v[40:43]}"(a[2]), "+{v[44:47]}"(a[3]), "=&s"(cnt)
               : [o] "v"(o), [rs] "s"(rs), [s0] "s"(s0), [s1] "s"(s1), [s2] "s"(s2), [s3] "s"(s3), [tag] "s"(tag), [m3] "s"(m3), [n] "n"(SPIN)
               : "memory", "scc", "vcc", "v48", "v49", "v50", "v51", "v52");
}
#define KL1(b, j) "buffer_load_dwordx4 v[" #b "+4*" #j ":" #b "+3+4*" #j "], %[o], %[rs], %[s" #j "] offen sc1\n\t"
#define KL(b) KL1(b, 0) KL1(b, 1) KL1(b, 2) KL1(b, 3)
#define KE1(b, j) "v_bitop3_b32 v[48+" #j "], v[" #b "+1+4*" #j "], v[" #b "+3+4*" #j "], %[tag] bitop3:0x7e\n\t"
#define KE(b) KE1(b, 0) KE1(b, 1) KE1(b, 2) KE1(b, 3) "v_or3_b32 v52, v48, v49, v50\n\tv_or_b32 v52, v52, v51\n\tv_cmp_ne_u32 vcc, 0, v52\n\t"
#define KM(b) "v_min_u32 v48, v[" #b "], v[" #b "+4]\n\tv_max_u32 v49, v[" #b "], v[" #b "+4]\n\tv_min_u32 v50, v[" #b "+8], v[" #b "+12]\n\t" \
              "v_max_u32 v51, v[" #b "+8], v[" #b "+12]\n\tv_max_u32 v53, v49, v51\n\tv_min_u32 v52, v49, v51\n\t" \
              "v_max3_u32 v55, v48, v[" #b "+2], v[" #b "+6]\n\tv_max3_u32 v56, v50, v[" #b "+10], v[" #b "+14]\n\tv_max3_u32 v54, v52, v55, v56\n\t"
#define GX(j, c) "s_waitcnt vmcnt(" #c ")\n\tv_xor_b32 v[48+4*" #j "], v[32+4*" #j "], v28\n\tv_xor_b32 v[49+4*" #j "], v[33+4*" #j "], v29\n\t" \
                 "v_xor_b32 v[50+4*" #j "], v[34+4*" #j "], v30\n\tv_xor_b32 v[51+4*" #j "], v[35+4*" #j "], v31\n\t" \
                 "ds_write_b128 %[u], v[48+4*" #j ":51+4*" #j "] offset:1024*" #j "\n\t" \
                 "v_or3_b32 v[64+" #j "], v[48+4*" #j "], v[49+4*" #j "], v[50+4*" #j "]\n\tv_bitop3_b32 v[64+" #j "], v[64+" #j "], v[51+4*" #j "], %[tm] bitop3:0xa8\n\t"
#define GQ(q) "v_or_b32_dpp v64, v64, v64 " q "\n\tv_or_b32_dpp v65, v65, v65 " q "\n\tv_or_b32_dpp v66, v66, v66 " q "\n\tv_or_b32_dpp v67, v67, v67 " q "\n\t"
#define GR(j) "v_cmp_ne_u32 vcc, 0, v[64+" #j "]\n\ts_mov_b64 exec, vcc\n\t" PL(j) "s_mov_b64 exec, -1\n\t"
#define GETTAG() u32 tag; asm volatile("s_waitcnt lgkmcnt(0)\n\ts_add_u32 %0, %1, 0x11111111" : "=s"(tag) : "{s4}"(tag0) : "scc")

__global__ void __launch_bounds__(1024, 4) kf(const void* p0, const void* p1, void* p2, const void* p3, void* p4, const void* p5, const void* p6,
    const void* p7, const void* p8, const void* p9, const void* p10, const void* p11, const void* p12, const void* p13, const void* p14, void* p15) {
  __shared__ __attribute__((aligned(16))) char smem[LDS_SZ];
  LDS char* L = (LDS char*)smem;
  LDS char* F = L + O_F;
  const CST char* kp = (const CST char*)__builtin_amdgcn_kernarg_segment_ptr();
  const int b = blockIdx.x;
  u64 k0_, k2_, k4_, k5_, k6_, k7_; v4u xa, xb, wa, wb; u32 w64, tag0, cg8; u64 dsk;
  asm volatile(
    "v_readfirstlane_b32 s3, %[tid]\n\ts_lshr_b32 s4, s3, 3\n\ts_and_b32 s4, s4, 64\n\ts_load_dwordx16 s[8:23], %[kp], s4\n\t"
    "v_lshlrev_b32 v1, 4, %[tid]\n\ts_cmpk_lt_u32 s3, 0x100\n\ts_cbranch_scc0 1f\n\t"
    "v_and_b32 v2, 0x1f0, v1\n\ts_lshl_b32 s4, %[b], 13\n\tv_sub_u32 v3, v1, v2\n\tv_add3_u32 v3, v3, v1, s4\n\t"
    "s_waitcnt lgkmcnt(0)\n\t"
    "global_load_dwordx4 v[12:15], v3, s[8:9]\n\tglobal_load_dwordx4 v[16:19], v3, s[8:9] offset:512\n\t"
    "global_load_dwordx4 v[20:23], v2, s[10:11]\n\tglobal_load_dwordx4 v[24:27], v2, s[10:11] offset:512\n\ts_setprio 3\n"
    "1:\n\ts_and_b32 s5, %[b], 7\n\ts_lshl_b32 s5, s5, 7\n\ts_lshr_b32 s4, %[b], 4\n\ts_lshl3_add_u32 s5, s4, s5\n\t"
    "s_cmpk_lt_u32 s3, 0x200\n\ts_cbranch_scc1 3f\n\t"
    "s_cmpk_lt_u32 s3, 0x300\n\ts_cbranch_scc1 2f\n\t"
    "s_getpc_b64 s[6:7]\n\tv_lshlrev_b32 v2, 2, v1\n\tv_and_b32 v2, 0x3fc0, v2\n\tv_min_u32 v2, %[cs], v2\n\tglobal_load_dword v18, v2, s[6:7]\n"
    "2:\n\ts_lshl_b32 s4, s5, 7\n\ts_and_b32 s6, s3, 0xc0\n\ts_lshl2_add_u32 s4, s6, s4\n\ts_and_b32 s6, s3, 0x100\n\ts_lshr_b32 s6, s6, 1\n\ts_add_u32 s4, s4, s6\n\t"
    "v_and_b32 v2, 63, %[tid]\n\tv_lshl_add_u32 v2, v2, 1, s4\n\ts_waitcnt lgkmcnt(0)\n\t"
    "global_load_ushort v12, v2, s[8:9]\n\tglobal_load_ushort v13, v2, s[10:11]\n\tglobal_load_ushort v14, v2, s[12:13]\n\t"
    "global_load_ushort v15, v2, s[14:15]\n\tglobal_load_ushort v16, v2, s[16:17]\n\tglobal_load_ushort v17, v2, s[18:19]\n\t"
    "s_load_dwordx2 s[6:7], s[20:21], s5\n\ts_setprio 1\n\ts_branch 5f\n"
    "3:\n\ts_cmpk_lt_u32 s3, 0x100\n\ts_cbranch_scc1 4f\n\t"
    "s_setprio 2\n\ts_bfe_u32 s4, s3, 0x20006\n\ts_mulk_i32 s4, %[up]\n\tv_mov_b64 v[12:13], 0\n\tv_and_b32 v1, 63, %[tid]\n\tv_lshlrev_b32 v2, 3, v1\n\t"
    "v_add_u32 v2, %[ou], v2\n\tv_add_u32 v3, s4, v2\n\tds_write_b64 v3, v[12:13]\n\tds_write_b64 v2, v[12:13] offset:%[up]*4\n\t"
    "s_and_b32 s4, %[b], 7\n\ts_lshr_b32 s6, %[b], 3\n\ts_cmpk_eq_u32 s3, 0x1c0\n\ts_cbranch_scc1 6f\n\t"
    "s_mulk_i32 s4, 448\n\ts_andn2_b32 s4, s4, 127\n\ts_mul_i32 s6, s6, 16*3584\n\ts_add_u32 s4, s4, s6\n\t"
    "v_lshrrev_b32 v2, 2, v1\n\tv_mul_u32_u24 v2, 3584-512, v2\n\tv_lshl_add_u32 v2, v1, 7, v2\n\ts_branch 7f\n"
    "6:\n\ts_lshl_b32 s4, s4, 5\n\ts_add_u32 s4, s4, s6\n\ts_mulk_i32 s4, 7*1024\n\tv_min_u32 v2, 55, v1\n\tv_lshlrev_b32 v2, 7, v2\n"
    "7:\n\tv_add_u32 v2, s4, v2\n\ts_waitcnt lgkmcnt(0)\n\t"
    "s_cmpk_eq_u32 s3, 0x140\n\ts_cselect_b64 s[10:11], s[18:19], s[20:21]\n\t"
    "s_cmpk_eq_u32 s3, 0x1c0\n\ts_cselect_b64 s[10:11], s[22:23], s[10:11]\n"
    "4:\n\ts_load_dword s4, s[12:13], 0\n\ts_load_dwordx2 s[6:7], s[14:15], s5\n"
    "5:\n\ts_barrier\n\ts_sub_u32 s14, s3, 0x140\n\ts_cmpk_lt_u32 s14, 0xc0\n\ts_cbranch_scc0 8f\n\ts_sleep %[dp]\n\tglobal_load_dword v14, v2, s[10:11]\n8:"
    : "={v[12:15]}"(xa), "={v[16:19]}"(xb), "={v[20:23]}"(wa), "={v[24:27]}"(wb)
    : [tid] "{v0}"(threadIdx.x), [kp] "{s[0:1]}"(kp), [b] "{s2}"(b), [cs] "n"(CODE_SZ), [up] "n"(UP), [ou] "n"(O_U), [dp] "n"(D_P)
    : "scc", "memory", "v1", "v2", "v3", "s3", "s4", "s5", "s6", "s7", "s8", "s9", "s10", "s11", "s12", "s13", "s14", "s15", "s16", "s17",
      "s18", "s19", "s20", "s21", "s22", "s23");
  asm volatile("" : "={s[8:9]}"(k0_), "={s[12:13]}"(k2_), "={s[16:17]}"(k4_), "={s[18:19]}"(k5_), "={s[20:21]}"(k6_), "={s[22:23]}"(k7_),
               "={s3}"(w64), "={s4}"(tag0), "={s5}"(cg8), "={s[6:7]}"(dsk));
  STAMP(0);
  const int lane = threadIdx.x & 63, w = w64 >> 6, cg = cg8 >> 3, h = (b >> 3) & 1;
  const int c16 = lane & 15, kg = lane >> 4;
  if (__builtin_expect(w64 < 512, 1)) {
    char* const ws = (char*)k2_;
    const __amdgpu_buffer_rsrc_t rs = __builtin_amdgcn_make_buffer_rsrc(ws, 0, WS_END, 0x00020000);
    const bool gwg = ((b >> 4) + ((~b & 8) << 1)) < 14;
    const int si = w >> 1, s = h ? 2 + si : (si < 2 ? si : si + 4), t0 = s * 256 + (w & 1) * 128 + lane * 2;
    v4u sv;
    asm volatile("" : "={v[120:123]}"(sv));
#define SVL() asm volatile("buffer_load_dwordx4 %0, %1, %2, %3 offen sc1" : "={v[120:123]}"(sv) : "v"(t0 * 8), "s"(rs), "s"(WS_S) : "memory")
#define SVW() asm volatile("s_waitcnt vmcnt(0)" : "+{v[120:123]}"(sv) :: "memory")
    if (__builtin_expect(w64 < 256, 1)) {
      LDS char* a_xn = L + O_LN + (threadIdx.x >> 5) * 1088 + (lane & 31) * 16;
      LDS char* a_ks = F + F_KS + (threadIdx.x >> 5) * 8;
      LDS char* a_f = F;
      LDS char* a_tr = L + O_LN + ((lane >> 2) & 3) * 1088 + w * 256 + kg * 32 + (lane & 3) * 8;
      u32 o_pub = (lane >> 3) * 32768 + (lane & 7) * 16;
      u32 o_g = (lane >> 2) * 128 + (lane & 3) * 16, so_g = WS_X + (cg >> 1) * 32768 + (cg & 1) * 64 + w * 2048;
      u32 rsh = (lane & 3) * 8, bit = 1u << w;
      u32 so_pub = WS_X + w * 16 * 32768 + b * (XL ? 64 : 128), so_pu2 = so_pub + 8 * 32768, kix = 0x80000000u | (u32)(2047 - b * 8 - (threadIdx.x >> 5));
      LDS char* a_u = L + O_U + (lane & 3) * UP + 512 + w * 256 + (lane >> 2) * 16;
      asm volatile("s_waitcnt vmcnt(2)"
                   : "+{v[12:15]}"(xa), "+{v[16:19]}"(xb), "+v"(a_xn), "+v"(a_ks), "+v"(a_f), "+v"(a_tr), "+v"(o_pub), "+v"(rsh), "+v"(bit), "+v"(kix),
                     "+v"(o_g), "+v"(a_u), "+s"(so_pub), "+s"(so_pu2), "+s"(so_g) :: "memory");
      STAMP(1);
      float s1 = 0.f, s2 = 0.f;
#pragma unroll
      for (int j = 0; j < 4; j++) { s1 = dot2(xa[j], 0x3f803f80u, s1); s2 = dot2(xa[j], xa[j], s2); }
#pragma unroll
      for (int j = 0; j < 4; j++) { s1 = dot2(xb[j], 0x3f803f80u, s1); s2 = dot2(xb[j], xb[j], s2); }
      v2u p = __builtin_amdgcn_permlane16_swap(__float_as_uint(s1), __float_as_uint(s2), false, false);
      float f = __uint_as_float(p[0]) + __uint_as_float(p[1]);
      f += dppf<0xB1>(f); f += dppf<0x4E>(f); f += dppf<0x141>(f); f += dppf<0x140>(f);
      f *= 1.f / 512.f;
      p = __builtin_amdgcn_permlane16_swap(__float_as_uint(f), __float_as_uint(f), false, false);
      const float dm = __uint_as_float(p[0]), mb = rbf(dm);
      const float sd = rbf(__builtin_amdgcn_sqrtf(rbf(rbf(fmaxf(__uint_as_float(p[1]) - dm * dm, 0.f)) + 1e-5f)));
      const float r16 = __builtin_amdgcn_rcpf(sd * 16.f), off = -mb * r16;
      v4u na, nb;
#pragma unroll
      for (int j = 0; j < 4; j++) { v2f y = __builtin_elementwise_fma((v2f){lo2f(xa[j]), hi2f(xa[j])}, (v2f){r16, r16}, (v2f){off, off}); na[j] = pk(y.x, y.y); }
      asm volatile("ds_write_b128 %0, %1" :: "v"(a_xn), "v"(na) : "memory");
#pragma unroll
      for (int j = 0; j < 4; j++) { v2f y = __builtin_elementwise_fma((v2f){lo2f(xb[j]), hi2f(xb[j])}, (v2f){r16, r16}, (v2f){off, off}); nb[j] = pk(y.x, y.y); }
      asm volatile("ds_write_b128 %2, %3 offset:512\n\ts_mov_b64 exec, 1\n\tds_or_b32 %4, %5 offset:%6\n\ts_mov_b64 exec, -1\n\ts_waitcnt vmcnt(0)"
                   : "+{v[20:23]}"(wa), "+{v[24:27]}"(wb) : "v"(a_xn), "v"(nb), "v"(a_f), "v"(bit), "n"(F_LN) : "memory");
      float r0 = 0.f, r1 = 0.f;
#pragma unroll
      for (int j = 0; j < 4; j++) { r0 = dot2(na[j], wa[j], r0); r1 = dot2(nb[j], wb[j], r1); }
      r0 += r1;
      u32 fl; v2u a0, a1, a2, a3;
#define TRR "ds_read_b32 %0, %[f] offset:%[o]\n\tds_read_b64_tr_b16 %1, %[t]\n\tds_read_b64_tr_b16 %2, %[t] offset:4352\n\t" \
            "ds_read_b64_tr_b16 %3, %[t] offset:128\n\tds_read_b64_tr_b16 %4, %[t] offset:4480\n\t"
      asm volatile(TRR : "=&v"(fl), "=&v"(a0), "=&v"(a1), "=&v"(a2), "=&v"(a3), "+v"(r0) : [f] "v"(a_f), [t] "v"(a_tr), [o] "n"(F_LN) : "memory");
      r0 += dppf<0xB1>(r0); r0 += dppf<0x4E>(r0); r0 += dppf<0x141>(r0); r0 += dppf<0x140>(r0);
      p = __builtin_amdgcn_permlane16_swap(__float_as_uint(r0), __float_as_uint(r0), false, false);
      const u32 rb = pk(0.f, (__uint_as_float(p[0]) + __uint_as_float(p[1])) * 16.f);
      const u32 key = rb ^ (((int)rb >> 31) & 0x7fff0000u) ^ kix;
      {
        u32 sc, cnt;
        asm volatile("s_mov_b64 exec, %[em]\n\tds_write_b64 %[ka], %[kv]\n\ts_mov_b64 exec, -1\n\ts_mov_b32 %6, 0x20000\n\ts_waitcnt lgkmcnt(0)\n"
                     "1:\n\tv_readfirstlane_b32 %5, %0\n\ts_cmp_eq_u32 %5, 15\n\ts_cbranch_scc1 2f\n\t" TRR
                     "s_waitcnt lgkmcnt(0)\n\ts_sub_u32 %6, %6, 1\n\ts_cbranch_scc0 1b\n2:"
                     : "+v"(fl), "+v"(a0), "+v"(a1), "+v"(a2), "+v"(a3), "=&s"(sc), "=&s"(cnt)
                     : [f] "v"(a_f), [t] "v"(a_tr), [o] "n"(F_LN), [em] "s"(0x100000001ull), [ka] "v"(a_ks),
                       [kv] "v"((v2u){(__float_as_uint(mb) >> 16) | __float_as_uint(sd), key})
                     : "memory", "scc");
      }
      GETTAG();
      u32 pt[4];
      {
        const u32 q = __builtin_amdgcn_ubfe(tag ^ 0x3C5A96C3u, rsh, 8), y = q | q << 15;
#pragma unroll
        for (int j = 0; j < 4; j++) pt[j] = (y << (14 - 2 * j)) & TM;
      }
      __builtin_amdgcn_raw_buffer_store_b128((v4u){(a0[0] & ~TM) | pt[0], (a0[1] & ~TM) | pt[1], (a1[0] & ~TM) | pt[2], (a1[1] & ~TM) | pt[3]}, rs, o_pub, so_pub, 16);
      __builtin_amdgcn_raw_buffer_store_b128((v4u){(a2[0] & ~TM) | pt[0], (a2[1] & ~TM) | pt[1], (a3[0] & ~TM) | pt[2], (a3[1] & ~TM) | pt[3]}, rs, o_pub, so_pu2, 16);
      lds_sig(F + F_PB, bit);
      STAMP(2);
      {
        u32 cnt;
        asm volatile("s_cmp_lt_u32 %[g], 14\n\ts_cbranch_scc0 5f\n\ts_sleep %[sg]\n5:\n\tv_mov_b32 v67, 0\n\ts_movk_i32 %0, %[n]\n\ts_cmp_lg_u32 %[h], 0\n\ts_sleep %[sl]\n\ts_cbranch_scc1 1f\n\t" PL(3)
                     "1:\n\t" PL(0) PL(1) PL(2)
                     "2:\n\ts_cmp_lg_u32 %[h], 0\n\ts_cbranch_scc1 3f\n\t" GX(3, 3)
                     "3:\n\t" GX(0, 2) GX(1, 1) GX(2, 0)
                     "v_or3_b32 v68, v64, v65, v67\n\tv_or_b32 v68, v68, v66\n\tv_cmp_ne_u32 vcc, 0, v68\n\ts_cbranch_vccz 4f\n\t"
                     GQ("quad_perm:[1,0,3,2] row_mask:0xf bank_mask:0xf") GQ("quad_perm:[2,3,0,1] row_mask:0xf bank_mask:0xf")
                     GR(0) GR(1) GR(2) GR(3) "s_waitcnt vmcnt(0)\n\ts_sub_u32 %0, %0, 1\n\ts_cbranch_scc0 2b\n4:"
                     : "=&s"(cnt)
                     : [o] "v"(o_g), [rs] "s"(rs), [s0] "s"(so_g), [s1] "s"(so_g + (8192 >> XL)), [s2] "s"(so_g + (16384 >> XL)), [s3] "s"(so_g + (24576 >> XL)), [u] "v"(a_u),
                       "{v[28:31]}"((v4u){pt[0], pt[1], pt[2], pt[3]}), [tm] "s"(TM), [h] "s"(h), [n] "n"(SPIN), [sl] "n"(D_A), [g] "s"((b >> 4) + ((~b & 8) << 1)), [sg] "n"(D_AG)
                     : "memory", "scc", "vcc", "v32", "v33", "v34", "v35", "v36", "v37", "v38", "v39", "v40", "v41", "v42", "v43", "v44", "v45", "v46", "v47",
                       "v48", "v49", "v50", "v51", "v52", "v53", "v54", "v55", "v56", "v57", "v58", "v59", "v60", "v61", "v62", "v63", "v64", "v65", "v66",
                       "v67", "v68");
      }
      lds_sig(F + F_U, bit);
      SVL();
      STAMP(3);
      SVW();
    }
    asm volatile("" : "+s"(w64));
    {
      if (w64 == 256) {
        u32 o_s = lane * 8, o_k = lane * 16;
        LDS char* a_kr = F + F_KS + (lane & 7) * 8;
        asm volatile("s_setprio 3\n\ts_sleep %3" : "+v"(o_s), "+v"(o_k), "+v"(a_kr) : "n"(D_R) : "memory");
        GETTAG();
        u32 cnt;
#define DQ(q) " " q " row_mask:0xf bank_mask:0xf\n\t"
        asm volatile("v_mov_b32 v33, %[tag]\n\tv_mov_b32 v35, %[tag]\n\tv_mov_b32 v38, 16\n\ts_mov_b32 %0, 0x20000\n"
                     "1:\n\tds_read_b64 v[36:37], %[a]\n\ts_waitcnt lgkmcnt(0)\n\t"
                     "v_cmp_eq_u32 vcc, 0, v36\n\ts_cbranch_vccz 2f\n\ts_sub_u32 %0, %0, 1\n\ts_cbranch_scc0 1b\n"
                     "2:\n\tv_max_u32_dpp v32, v37, v37" DQ("quad_perm:[1,0,3,2]") "v_min_u32_dpp v34, v37, v37" DQ("quad_perm:[1,0,3,2]")
                     "v_mov_b32 v37, %[tag]\n\t"
                     "v_min_u32_dpp v39, v32, v32" DQ("quad_perm:[2,3,0,1]") "v_max_u32_dpp v34, v34, v34" DQ("quad_perm:[2,3,0,1]")
                     "v_max_u32_dpp v32, v32, v32" DQ("quad_perm:[2,3,0,1]") "v_max_u32 v34, v34, v39\n\ts_nop 0\n\t"
                     "v_min_u32_dpp v39, v32, v32" DQ("row_half_mirror") "v_max_u32_dpp v34, v34, v34" DQ("row_half_mirror")
                     "v_max_u32_dpp v32, v32, v32" DQ("row_half_mirror") "v_max_u32 v34, v34, v39\n\ts_mov_b64 exec, 1\n\t"
                     "buffer_store_dwordx4 v[32:35], %[ok], %[rs], %[sk] offen sc1\n\ts_mov_b64 exec, 0xff\n\t"
                     "buffer_store_dwordx2 v[36:37], %[os], %[rs], %[ss] offen sc1\n\ts_mov_b64 exec, 1\n\t"
                     "ds_or_b32 %[a], v38 offset:%[pb]\n\ts_mov_b64 exec, -1"
                     : "=&s"(cnt)
                     : [a] "v"(a_kr), [tag] "s"(tag), [ok] "v"(o_k), [os] "v"(o_s), [rs] "s"(rs), [sk] "s"(WS_R + b * 16), [ss] "s"(WS_S + b * 64),
                       [pb] "n"(F_PB - F_KS)
                     : "memory", "scc", "vcc", "v32", "v33", "v34", "v35", "v36", "v37", "v38", "v39");
        STAMP(2);
        u32 k0, k1; v4u kb[4], kc[4];
        asm volatile("s_cmp_lt_u32 %[g], 14\n\ts_cbranch_scc1 3f\n\ts_sleep %[dk]\n\t" KL(32) "s_branch 4f\n"
                     "3:\n\ts_sleep %[dg]\n\t" KL(60) "s_sleep %[d2]\n\t" KL(76) "s_sleep %[d2]\n\t" KL(32)
                     "s_waitcnt vmcnt(8)\n\t" KE(60) "s_cbranch_vccnz 5f\n\t" KM(60) "s_branch 6f\n"
                     "5:\n\ts_waitcnt vmcnt(4)\n\t" KE(76) "s_cbranch_vccnz 4f\n\t" KM(76) "s_branch 6f\n"
                     "4:\n\ts_movk_i32 %[c], %[n]\n"
                     "1:\n\ts_waitcnt vmcnt(0)\n\t" KE(32) "s_cbranch_vccz 2f\n\t"
                     PR(0) PR(1) PR(2) PR(3) "s_sub_u32 %[c], %[c], 1\n\ts_cbranch_scc0 1b\n\ts_waitcnt vmcnt(0)\n2:\n\t" KM(32) "6:"
                     : "={v53}"(k0), "={v54}"(k1), "={v[32:35]}"(kc[0]), "={v[36:39]}"(kc[1]), "={v[40:43]}"(kc[2]), "={v[44:47]}"(kc[3]),
                       "={v[76:79]}"(kb[0]), "={v[80:83]}"(kb[1]), "={v[84:87]}"(kb[2]), "={v[88:91]}"(kb[3]), [c] "=&s"(cnt)
                     : [o] "{v31}"(o_k), [rs] "s"(rs), [s0] "s"(WS_R), [s1] "s"(WS_R + 1024), [s2] "s"(WS_R + 2048), [s3] "s"(WS_R + 3072), [tag] "s"(tag),
                       [n] "n"(SPIN), [g] "s"((b >> 4) + ((~b & 8) << 1)), [dk] "n"(D_K), [dg] "n"(D_KG), [d2] "n"(D_K2)
                     : "memory", "scc", "vcc", "v48", "v49", "v50", "v51", "v52", "v55", "v56", "v60", "v61", "v62", "v63", "v64", "v65", "v66", "v67",
                       "v68", "v69", "v70", "v71", "v72", "v73", "v74", "v75");
        STAMP(3);
        const u32 s0 = wave_max(k0);
        if (gwg) asm volatile("s_mov_b32 m0, %2\n\ts_nop 0\n\tglobal_load_lds_dwordx4 %0, %1"
                              :: "v"(o_k), "s"(k0_ + ((~s0 & 2047) << 10)), "n"(O_XR) : "memory");
        const u32 s1 = wave_max(k0 == s0 ? k1 : k0);
        const u32 o_t = (~((lane & 1) ? s1 : s0) & 2047) * 8;
#define TSW "s_mov_b64 exec, 1\n\tds_write_b64 %[f], %[ts] offset:%[ot]\n\tds_or_b32 %[f], %[c1] offset:%[os]\n\ts_mov_b64 exec, -1\n\t"
        if (gwg) {
          asm volatile("s_mov_b32 m0, %[m]\n\ts_movk_i32 %0, %[n]\n\tglobal_load_lds_dwordx4 %[o], %[x]\n\t"
                       "buffer_load_dwordx2 v[48:49], %[t], %[rs], %[ss] offen sc1\n\t" TSW ST_(4)
                       "1:\n\ts_waitcnt vmcnt(0)\n\tv_cmp_ne_u32 vcc, %[tag], v49\n\ts_cbranch_vccz 2f\n\t"
                       "buffer_load_dwordx2 v[48:49], %[t], %[rs], %[ss] offen sc1\n\ts_sub_u32 %0, %0, 1\n\ts_cbranch_scc0 1b\n\ts_waitcnt vmcnt(0)\n"
                       "2:\n\ts_mov_b64 exec, 3\n\tds_write_b32 %[fl], v48 offset:%[st]\n\ts_mov_b64 exec, 1\n\tds_or_b32 %[f], %[c2] offset:%[os]\n\ts_mov_b64 exec, -1"
                       : "=&s"(cnt)
                       : [o] "v"(o_k), [x] "s"(k0_ + ((~s1 & 2047) << 10)), [m] "n"(O_XR + 1088), [n] "n"(SPIN), [t] "v"(o_t), [rs] "s"(rs), [ss] "s"(WS_S),
                         [f] "v"(F), [ts] "v"((v2u){s0, s1}), [c1] "v"(256u), [c2] "v"(512u), [ot] "n"(F_TS), [os] "n"(F_SP), [tag] "s"(tag),
                         [fl] "v"(F + lane * 4), [st] "n"(F_ST),
                         "{v[32:35]}"(kc[0]), "{v[36:39]}"(kc[1]), "{v[40:43]}"(kc[2]), "{v[44:47]}"(kc[3]),
                         "{v[76:79]}"(kb[0]), "{v[80:83]}"(kb[1]), "{v[84:87]}"(kb[2]), "{v[88:91]}"(kb[3])
                       : "memory", "scc", "vcc", "v48", "v49");
        } else {
          asm volatile(TSW ST_(4) :: [f] "v"(F), [ts] "v"((v2u){s0, s1}), [c1] "v"(256u), [ot] "n"(F_TS), [os] "n"(F_SP) : "memory");
          if (b == 0 && lane == 0) *(GLB u32*)ws = tag;
        }
        SVL();
        STAMP(1);
        SVW();
      }
      asm volatile("" : "+s"(w64));
      if (w64 > 256) {
        u32 dummy;
        asm volatile("" : "={v14}"(dummy));
        if (gwg) {
          const int fs = (b & 7) * 14 + (b >> 4);
          v4u wq[8];
          LDS char* ex = F + F_E + (kg * 2 + (lane & 1)) * 32;
          LDS char* sl = F + F_ST + (lane & 1) * 4;
          if (w64 != 320) {
            const int fw = w & 1;
            LDS char* const wl = L + O_W + fw * 8192;
            *(LDS v2u*)(L + O_XR + 2176 + lane * 8) = (v2u){0x3f803f80u, 0x3f803f80u};
            v8bf fg[8], fu[8];
            const int rho = lane >> 1, kk = 8 * ((rho >> 2) & 3) + 4 * (rho >> 4) + (rho & 3);
            u32 go = (fw * 256 + kk) * 3584 + fs * 32 + (lane & 1) * 16, gu = go, bit = 1u << fw;
            LDS char* xl = L + O_XR + ((lane & 2) ? 2176 : (lane & 1) * 1088 + fw * 512) + kg * 16;
            ex += fw * 256;
            asm volatile("s_sleep %6" : "+v"(go), "+v"(gu), "+v"(xl), "+v"(ex), "+v"(sl), "+v"(bit) : "n"(D_W));
            lds_wait<1>(F + F_PB, 31);
            asm volatile("s_mov_b32 m0, %[m]\n\ts_setprio 3\n\t" ST_(1)
                         WG_ WG_ WG_ WG_ WG_ WG_ WG_ WG_ WU(0) WU(1) WU(2) WU(3) WU(4) WU(5) WU(6) WU(7) ST_(2) "s_waitcnt vmcnt(8)"
                         : "=&v"(wq[0]), "=&v"(wq[1]), "=&v"(wq[2]), "=&v"(wq[3]), "=&v"(wq[4]), "=&v"(wq[5]), "=&v"(wq[6]), "=&v"(wq[7]),
                           [go] "+v"(go), [gu] "+v"(gu), "+{v14}"(dummy)
                         : [m] "s"(O_W + fw * 8192), [wg] "s"(k5_), [wu] "s"(k6_)
                         : "memory", "scc");
#pragma unroll
            for (int ks = 0; ks < 8; ks++) fg[ks] = cat(trd(wl + lane * 8 + ks * 1024), trd(wl + lane * 8 + ks * 1024 + 512));
            asm volatile("s_waitcnt vmcnt(0)"
                         : "+v"(wq[0]), "+v"(wq[1]), "+v"(wq[2]), "+v"(wq[3]), "+v"(wq[4]), "+v"(wq[5]), "+v"(wq[6]), "+v"(wq[7]) :: "memory");
#pragma unroll
            for (int ks = 0; ks < 8; ks++) *(LDS v4u*)(wl + ks * 1024 + lane * 16) = wq[ks];
            MEMB();
#pragma unroll
            for (int ks = 0; ks < 8; ks++) fu[ks] = cat(trd(wl + lane * 8 + ks * 1024), trd(wl + lane * 8 + ks * 1024 + 512));
            SVL();
            STAMP(3);
            lds_wait<1>(F + F_SP, 256);
            u32 sc, cnt;
            asm volatile("s_sleep %[sl]\n\ts_mov_b32 %1, 0x20000\n"
                         "1:\n\tds_read_b32 v72, %[f] offset:%[sp]\n\tds_read_b32 v73, %[s]\n\t" XR(0) XR(1) XR(2) XR(3) XR(4) XR(5) XR(6) XR(7)
                         "s_waitcnt lgkmcnt(9)\n\tv_readfirstlane_b32 %0, v72\n\ts_bitcmp1_b32 %0, 9\n\ts_cbranch_scc1 2f\n\t"
                         "s_waitcnt lgkmcnt(0)\n\ts_sub_u32 %1, %1, 1\n\ts_cbranch_scc0 1b\n2:\n\t"
                         XM(0, 2, 10, "0", "0") "v_lshlrev_b32 v73, 16, v73\n\t" XA(1, 3, 11) "v_xor_b32 v73, 0x80000000, v73\n\t"
                         XA(2, 4, 12) XA(3, 5, 13) XA(4, 6, 14) XA(5, 7, 15) XA(6, 8, 16) XA(7, 9, 17) ST_(4)
                         "s_nop 6\n\t" XN(64) XN(65) XN(66) XN(67) XN(68) XN(69) XN(70) XN(71)
                         "s_mov_b64 exec, %[em]\n\tds_write_b128 %[e], v[64:67]\n\tds_write_b128 %[e], v[68:71] offset:16\n\t"
                         "s_mov_b64 exec, 1\n\tds_or_b32 %[f], %[bit] offset:%[ex]\n\ts_mov_b64 exec, -1"
                         : "=&s"(sc), "=&s"(cnt)
                         : "v"(fg[0]), "v"(fg[1]), "v"(fg[2]), "v"(fg[3]), "v"(fg[4]), "v"(fg[5]), "v"(fg[6]), "v"(fg[7]),
                           "v"(fu[0]), "v"(fu[1]), "v"(fu[2]), "v"(fu[3]), "v"(fu[4]), "v"(fu[5]), "v"(fu[6]), "v"(fu[7]),
                           [f] "v"(F), [s] "v"(sl), [x] "v"(xl), [e] "v"(ex), [bit] "v"(bit), [em] "s"(0x0003000300030003ull),
                           [sp] "n"(F_SP), [ex] "n"(F_EX), [sl] "n"(D_G)
                         : "memory", "scc", "v32", "v33", "v34", "v35", "v36", "v37", "v38", "v39", "v40", "v41", "v42", "v43", "v44", "v45", "v46", "v47",
                           "v48", "v49", "v50", "v51", "v52", "v53", "v54", "v55", "v56", "v57", "v58", "v59", "v60", "v61", "v62", "v63", "v64", "v65",
                           "v66", "v67", "v68", "v69", "v70", "v71", "v72", "v73");
            STAMP(5);
            SVW();
          } else {
            LDS char* const wl = L + O_LN;
            u32 dd = (fs * 16 + (lane >> 2)) * 1024 + (lane & 3) * 16, po = (c16 >> 1) * 128 + kg * 32 + (lane & 1) * 16;
            LDS char* dl = wl + (lane >> 2) * 64 + (lane & 3) * 8;
            asm volatile("s_sleep %5" : "+v"(dd), "+v"(po), "+v"(dl), "+v"(ex), "+v"(sl) : "n"(D_W));
            lds_wait<1>(F + F_PB, 31);
            asm volatile("s_mov_b32 m0, %[m]\n\ts_sleep %[sl]\n\t" WD(0) WD(1) WD(2) WD(3) WD(4) WD(5) WD(6) WD(7) "s_waitcnt vmcnt(0)"
                         : "=&v"(wq[0]), "=&v"(wq[1]), "=&v"(wq[2]), "=&v"(wq[3]), "=&v"(wq[4]), "=&v"(wq[5]), "=&v"(wq[6]), "=&v"(wq[7]), "+{v14}"(dummy)
                         : [m] "n"(O_LN), [wd] "s"(k7_), [dd] "v"(dd), [sl] "n"(D_D)
                         : "memory", "scc");
            v8bf wdf[16];
#pragma unroll
            for (int j = 0; j < 8; j++) wdf[j] = cat(trd(dl + j * 1024), trd(dl + j * 1024 + 32));
            MEMB();
#pragma unroll
            for (int ks = 0; ks < 8; ks++) *(LDS v4u*)(wl + ks * 1024 + lane * 16) = wq[ks];
            MEMB();
#pragma unroll
            for (int j = 0; j < 8; j++) wdf[8 + j] = cat(trd(dl + j * 1024), trd(dl + j * 1024 + 32));
            GETTAG();
            SVL();
            u64 hm[8];
#pragma unroll
            for (int j = 0; j < 8; j++) hm[j] = 0x0003000300030003ull << (2 * j);
            u32 so = WS_P + fs * 4096;
            v4u pv[2] = {{0, tag, 0, tag}, {0, tag, 0, tag}};
            asm volatile("s_setprio 3" : "+s"(hm[0]), "+s"(hm[1]), "+s"(hm[2]), "+s"(hm[3]), "+s"(hm[4]), "+s"(hm[5]), "+s"(hm[6]), "+s"(hm[7]), "+s"(so));
            asm volatile("" : "+v"(wdf[8]), "+v"(wdf[9]), "+v"(wdf[10]), "+v"(wdf[11]), "+v"(wdf[12]), "+v"(wdf[13]), "+v"(wdf[14]), "+v"(wdf[15]),
                         "+v"(pv[0]), "+v"(pv[1]));
            STAMP(1);
            lds_wait<1>(F + F_SP, 512);
            const float sd = hi2f(*(LDS u32*)sl);
            float c1 = __builtin_amdgcn_rcpf(sd) * -1.44269504089f, c2 = sd * sd;
            SVW();
            v2u hf;
            {
              u32 sc, cnt;
#define X4(m) m(0) m(1) m(2) m(3)
#define SA(i) "v_add_f32 v[76+" #i "], v[76+" #i "], v[84+" #i "]\n\t"
#define SB_(i) "v_mul_f32 v[84+" #i "], %[c1], v[76+" #i "]\n\t"
#define SC(i) "v_exp_f32 v[84+" #i "], v[84+" #i "]\n\t"
#define SD(i) "v_add_f32 v[80+" #i "], v[80+" #i "], v[88+" #i "]\n\t"
#define SE(i) "v_fma_f32 v[84+" #i "], v[84+" #i "], %[c2], %[c2]\n\t"
#define SF(i) "v_rcp_f32 v[84+" #i "], v[84+" #i "]\n\t"
#define SG(i) "v_mul_f32 v[76+" #i "], v[76+" #i "], v[80+" #i "]\n\t"
#define SH(i) "v_mul_f32 v[76+" #i "], v[76+" #i "], v[84+" #i "]\n\t"
              asm volatile("s_mov_b32 %2, 0x20000\n"
                           "1:\n\tds_read_b32 v74, %[f]\n\tds_read_b128 v[76:79], %[e]\n\tds_read_b128 v[84:87], %[e] offset:256\n\t"
                           "ds_read_b128 v[80:83], %[e] offset:16\n\tds_read_b128 v[88:91], %[e] offset:272\n\t"
                           "s_waitcnt lgkmcnt(4)\n\tv_readfirstlane_b32 %1, v74\n\ts_cmp_eq_u32 %1, 3\n\t"
                           "s_cbranch_scc1 2f\n\ts_sub_u32 %2, %2, 1\n\ts_cbranch_scc0 1b\n2:\n\ts_waitcnt lgkmcnt(2)\n\t"
                           X4(SA) X4(SB_) X4(SC) "s_waitcnt lgkmcnt(0)\n\t" ST_(2) X4(SD) X4(SE) X4(SF) X4(SG) X4(SH)
                           "v_cvt_pk_bf16_f32 v74, v76, v77\n\tv_cvt_pk_bf16_f32 v75, v78, v79"
                           : "=&{v[74:75]}"(hf), "=&s"(sc), "=&s"(cnt)
                           : [f] "v"(F + F_EX), [e] "v"(ex), [c1] "v"(c1), [c2] "v"(c2)
                           : "memory", "scc", "v76", "v77", "v78", "v79", "v80", "v81", "v82", "v83", "v84", "v85", "v86", "v87", "v88", "v89", "v90", "v91");
            }
            v4f e[4];
#pragma unroll
            for (int q = 0; q < 4; q++) e[q] = (v4f){0.f, 0.f, 0.f, 0.f};
#pragma unroll
            for (int j = 0; j < 4; j++) {
              const bool m0 = __builtin_amdgcn_inverse_ballot_w64(hm[2 * j]), m1 = __builtin_amdgcn_inverse_ballot_w64(hm[2 * j + 1]);
              const v8bf hb = BF8(((v4u){m0 ? hf[0] : 0, m0 ? hf[1] : 0, m1 ? hf[0] : 0, m1 ? hf[1] : 0}));
#pragma unroll
              for (int q = 0; q < 4; q++) e[q] = MF(wdf[4 * q + j], hb, e[q]);
            }
#pragma unroll
            for (int q = 0; q < 4; q++) {
              pv[q & 1][0] = pk(e[q][0], e[q][1]); pv[q & 1][2] = pk(e[q][2], e[q][3]);
              __builtin_amdgcn_raw_buffer_store_b128(pv[q & 1], rs, po + q * 1024, so, 16);
            }
            STAMP(0);
          }
        } else {
          STAMP(1);
          asm volatile("s_setprio 0\n\ts_waitcnt vmcnt(0)" : "+{v14}"(dummy));
          STAMP(2);
          lds_wait<4>(F + F_U, 15); SVL(); SVW();
        }
      }
    }
    GETTAG();
    asm volatile("" : "+{s[6:7]}"(dsk));
    const bool ffn = gwg && w > 5;
    {
      u32 cnt, e;
      asm volatile("s_setprio 0\n\ts_cmpk_eq_u32 %[w], 320\n\ts_cbranch_scc0 3f\n\ts_setprio 3\n3:\n\ts_movk_i32 %1, %[n]\n"
                   "1:\n\tv_bitop3_b32 %2, v121, v123, %[tag] bitop3:0x7e\n\tv_cmp_ne_u32 vcc, 0, %2\n\ts_cbranch_vccz 2f\n\t"
                   "buffer_load_dwordx4 %0, %[o], %[rs], %[ss] offen sc1\n\ts_sub_u32 %1, %1, 1\n\ts_waitcnt vmcnt(0)\n\ts_cbranch_scc0 1b\n2:"
                   : "+{v[120:123]}"(sv), "=&s"(cnt), "=&v"(e)
                   : [o] "v"(t0 * 8), [rs] "s"(rs), [ss] "s"(WS_S), [tag] "s"(tag), [n] "n"(SPIN), [w] "s"(w64) : "memory", "scc", "vcc");
    }
    lds_wait<1>(F + F_U, 15);
    float base[4][2];
    {
      const float m0 = lo2f(sv[0]), m1 = lo2f(sv[2]), s0 = hi2f(sv[0]), s1 = hi2f(sv[2]);
#pragma unroll
      for (int c = 0; c < 4; c++) {
        u32 uu = *(LDS u32*)(L + O_U + c * UP + 512 + t0 * 2), dd = (u32)(dsk >> ((c >> 1) * 32));
        float dk = ((c & 1) ? hi2f(dd) : lo2f(dd)) * 16.f + 16.f;
        base[c][0] = opq(fmaf(lo2f(uu), fmaf(s0, 16.f, dk), m0)); base[c][1] = opq(fmaf(hi2f(uu), fmaf(s1, 16.f, dk), m1));
      }
    }
    lds_wait<1>(F + F_SP, 256);
    const v2u ts = *(LDS v2u*)(F + F_TS);
    const int ts0 = 2047 - (ts[0] & 2047), ts1 = 2047 - (ts[1] & 2047);
    const int n = (w & 1) * 8 + (lane >> 3);
    u64 m0 = __builtin_amdgcn_ballot_w64(t0 != ts0 && t0 != ts1), m1 = __builtin_amdgcn_ballot_w64(t0 + 1 != ts0 && t0 + 1 != ts1);
    u64 op = k4_ + cg8;
    u32 oo = t0 * 1024;
    asm volatile("" : "+v"(oo), "+s"(op), "+s"(m0), "+s"(m1), "+v"(base[0][0]), "+v"(base[1][0]), "+v"(base[2][0]), "+v"(base[3][0]),
                 "+v"(base[0][1]), "+v"(base[1][1]), "+v"(base[2][1]), "+v"(base[3][1]));
    if (!ffn) STAMP(5);
    if (w64 == 320) {
      const u32 s0 = __builtin_amdgcn_readfirstlane(ts[0]), s1 = __builtin_amdgcn_readfirstlane(ts[1]);
      const u32 own = h ? 0x3Cu : 0xC3u;
      if ((own >> (7 - ((s0 >> 8) & 7)) | own >> (7 - ((s1 >> 8) & 7))) & 1) {
        const u32 kk = (lane & 1) ? s1 : s0;
        const float r = __uint_as_float((kk ^ 0x80000000u ^ ((~(int)kk >> 31) & 0x7fff0000u)) & 0xffff0000u);
        float sgm = rbf(__builtin_amdgcn_rcpf(1.f + __builtin_amdgcn_exp2f(r * -1.44269504089f)));
        u32 c_lo = 0x3f80u, c_hi = 0x3f800000u, o0 = (lane >> 1) * 4096 + (lane & 1) * 16;
        LDS char* a_sv = F + F_SV + c16 * 16 + kg * 4;
        v4u a[4];
        a[3] = (v4u){0, tag, 0, tag};
        asm volatile("" : "+s"(c_lo), "+s"(c_hi));
        asm volatile("s_cmp_lt_u32 %[g], 14\n\ts_cbranch_scc0 5f\n\ts_sleep %[sg]\n5:\n\ts_sleep %[sq]"
                     : "+v"(sgm), "+v"(o0), "+v"(a_sv), "+{v[44:47]}"(a[3]) : [g] "s"((b >> 4) + ((~b & 8) << 1)), [sg] "n"(D_QG), [sq] "n"(D_Q) : "scc");
        STAMP(3);
        poll4(a, rs, o0, WS_P + cg * 32, WS_P + cg * 32 + 32 * 4096, WS_P + cg * 32 + 64 * 4096, WS_P + cg * 32 + 96 * 4096, tag, 0xffffffffull);
        STAMP(4);
        float y[4] = {0.f, 0.f, 0.f, 0.f};
#pragma unroll
        for (int j = 0; j < 4; j++) {
          y[0] = dot2(a[j][0], c_lo, y[0]); y[1] = dot2(a[j][0], c_hi, y[1]);
          y[2] = dot2(a[j][2], c_lo, y[2]); y[3] = dot2(a[j][2], c_hi, y[3]);
        }
        float f4 = fold4(y[0], y[2], y[1], y[3]);
        f4 += dppf<0x4E>(f4); f4 += dppf<0x124>(f4); f4 += dppf<0x128>(f4);
        asm volatile("s_mov_b64 exec, %3\n\tds_write_b32 %0, %1\n\ts_mov_b64 exec, 1\n\tds_or_b32 %0, %2 offset:%4\n\ts_mov_b64 exec, -1"
                     :: "v"(a_sv), "v"(rbf(f4) * sgm), "v"(1024u), "s"(0x0003000300030003ull), "n"(F_UP - F_SV) : "memory");
      }
    }
    v2f pr[8];
    {
      u32 f, sc, cnt;
      asm volatile("s_mov_b32 %10, 0x20000\n"
                   "1:\n\tds_read_b32 %8, %11\n\ts_waitcnt lgkmcnt(0)\n\tv_readfirstlane_b32 %9, %8\n\ts_and_b32 %9, %13, %9\n\t"
                   "s_cmp_gt_u32 %9, %14\n\ts_cbranch_scc1 3f\n\ts_sleep 1\n\ts_sub_u32 %10, %10, 1\n\ts_cbranch_scc0 1b\n"
                   "3:\n\tds_read_b32 %8, %11\n\tds_read_b64 %0, %12\n\tds_read_b64 %1, %12 offset:%15\n\tds_read_b64 %2, %12 offset:%15*2\n\t"
                   "ds_read_b64 %3, %12 offset:%15*3\n\tds_read_b64 %4, %12 offset:4096\n\tds_read_b64 %5, %12 offset:%15+4096\n\t"
                   "ds_read_b64 %6, %12 offset:%15*2+4096\n\tds_read_b64 %7, %12 offset:%15*3+4096\n\t"
                   "s_waitcnt lgkmcnt(8)\n\tv_readfirstlane_b32 %9, %8\n\ts_andn2_b32 %9, %13, %9\n\t"
                   "s_cbranch_scc0 2f\n\ts_waitcnt lgkmcnt(0)\n\ts_sub_u32 %10, %10, 1\n\ts_cbranch_scc0 3b\n2:\n\ts_setprio 3\n\ts_waitcnt lgkmcnt(0)"
                   : "=&v"(pr[0]), "=&v"(pr[1]), "=&v"(pr[2]), "=&v"(pr[3]), "=&v"(pr[4]), "=&v"(pr[5]), "=&v"(pr[6]), "=&v"(pr[7]),
                     "=&v"(f), "=&s"(sc), "=&s"(cnt)
                   : "v"(F + F_PH), "v"(L + si * 1024 + n * 64 + 16 * (((lane >> 1) & 3) ^ ((n >> 1) & 3)) + (lane & 1) * 8),
                     "s"(0xf000fu << (4 * si)), "s"(0xf0000u << (4 * si)), "n"(BC) : "memory", "scc");
    }
    STAMP(6);
#pragma unroll
    for (int c = 0; c < 4; c++) {
      base[c][0] = opq(base[c][0] + opq(pr[c][0]) + opq(pr[4 + c][0])); base[c][1] = opq(base[c][1] + opq(pr[c][1]) + opq(pr[4 + c][1]));
    }
    asm volatile("s_mov_b64 exec, %4\n\tglobal_store_dwordx2 %0, %1, %3\n\ts_mov_b64 exec, %5\n\tglobal_store_dwordx2 %0, %2, %3 offset:1024\n\ts_mov_b64 exec, -1"
                 :: "v"(oo), "v"((v2u){pk(base[0][0], base[1][0]), pk(base[2][0], base[3][0])}),
                    "v"((v2u){pk(base[0][1], base[1][1]), pk(base[2][1], base[3][1])}), "s"(op), "s"(m0), "s"(m1) : "memory");
    if (__builtin_expect((m0 & m1) != ~0ull, 0)) {
      lds_wait<0>(F + F_UP, 1024);
#pragma unroll
      for (int i = 0; i < 2; i++) {
        if (t0 + i == ts0 || t0 + i == ts1) {
          const v4f up = *(LDS v4f*)(F + F_SV + (t0 + i == ts0 ? 0 : 16));
          *(GLB v2u*)(op + oo + i * 1024) = (v2u){pks(base[0][i] + up[0], base[1][i] + up[1]), pks(base[2][i] + up[2], base[3][i] + up[3])};
        }
      }
    }
    STAMP(7);
    FLUSH();
    asm volatile("s_endpgm" ::: "memory");
  }
  {
    v4u xr, wv;
    asm volatile("" : "={v[12:15]}"(xr), "={v[16:19]}"(wv));
    asm volatile("" : "={s[6:7]}"(dsk), "={s[22:23]}"(k7_));
    SB();
    char* const ws = (char*)k7_;
    const int ch = w & 3, half = (w >> 2) & 1;
    LDS char* const Bc = L + ch * BC;
    LDS char* const Ac = L + O_A + ch * AC;
    LDS char* const Uc = L + O_U + ch * UP;
    const u64 tb = (lane & 16) ? 0xfffff7654f32f10full : 0xcbaaa98876543210ull;
    const u32 ar = (u32)(tb >> (c16 * 4)) & 15;
    LDS char* pa = (ar == 15 ? Uc + 208 - ch * 32 : Ac + half * AH + (kg & 1) * A_UP + ar * RP) + (kg >> 1) * 16;
    LDS char* pb = Bc + half * (BC / 2) + (lane & 31) * RP + (kg >> 1) * 16;
    LDS char* px = Bc + 4096 + lane * 16;
    LDS char* pw = Bc + half * 4096 + c16 * 64 + 16 * (kg ^ ((c16 >> 1) & 3));
    asm volatile("" : "+v"(pa), "+v"(pb), "+v"(pw), "+v"(px));
    v4u b35, b6a, b5b, b4c, b34;
    LDS char *p_q, *p_c, *p_a, *p_u; u32 psel;
    if (half) {
      const int be = c16 + 16 - 8 * kg;
      const v2s d3 = {(short)(be - 3), (short)(be - 4)};
      u32 n[17];
#pragma unroll
      for (int i = 0; i < 17; i += 2) n[i] = __builtin_bit_cast(u32, (v2s)((d3 - (v2s){(short)i, (short)i}) >> 15));
#pragma unroll
      for (int i = 1; i < 17; i += 2) n[i] = __builtin_amdgcn_alignbit(n[i + 1], n[i - 1], 16);
      b35 = band<3, 5>(n, 0x3f803f80u); b6a = band<6, 10>(n, 0x3f803f80u); b5b = band<5, 11>(n, 0x3f803f80u); b4c = band<4, 12>(n, 0x3f803f80u);
      b34 = band<3, 4>(n, ~0u);
      p_q = Ac + A_HI + (kg >> 1) * 256 + (lane & 31) * 2;
      p_c = Ac + 32 + (lane & 31) * 16 + (kg >> 1) * 8;
      p_a = Ac + A_HI + (c16 - 4 * kg - 3) * 4;
      p_u = Uc + 512 - 48 + (h ? 0 : 1024) + c16 * 32 + kg * 16;
      asm volatile("" : "+v"(b35), "+v"(b6a), "+v"(b5b), "+v"(b4c), "+v"(b34), "+v"(p_q), "+v"(p_c), "+v"(p_a), "+v"(p_u));
    } else {
      p_c = Ac + (kg >> 1) * (AH + 128) + (kg & 1) * 1344 + c16 * 4;
      p_q = p_c - (lane & 8) * 4;
      p_a = Ac + ((lane >> 3) & 1) * (AH + 128) + ((lane >> 2) & 1) * 1344 + 56 - kg * 16 + (lane & 3) * 8;
      p_u = Uc + 32 - 1024 + h * 512 + c16 * 32 + kg * 16;
      const int i0 = 2 * c16 - kg;
      const u32 sl = i0 > 2 ? 0x0c0cu : i0 == -3 ? 0x0302u : i0 == -2 ? 0x0504u : 0x0706u, sh = i0 > 1 ? 0x0c0cu : i0 == -3 ? 0x0504u : 0x0706u;
      psel = sl | sh << 16;
      asm volatile("" : "+v"(p_q), "+v"(p_c), "+v"(p_a), "+v"(p_u), "+v"(psel));
    }
    v8bf fb[8];
    {
      asm volatile("s_waitcnt lgkmcnt(0)" : "+{s[6:7]}"(dsk) :: "memory");
      const float dt = rbf(__builtin_amdgcn_exp2f(lo2f((u32)(dsk >> (ch * 16))) * 1.44269504089f));
      float d1 = dt * 1.44269504089f, d2 = dt * 0.15915494309f, d16 = dt * 16.f;
      asm volatile("s_mov_b32 m0, %5\n\ts_waitcnt vmcnt(0)"
                   : "+{v[12:15]}"(xr), "+{v[16:19]}"(wv), "+v"(d1), "+v"(d2), "+v"(d16) : "s"(ch * BC + half * (BC / 2)) : "memory");
      STAMP(1);
      const float er = d1 * lo2f(xr[0]), ph = d2 * lo2f(xr[1]);
      const v2f z = cpow(er, ph), E = cpow(er * 256.f, ph * 256.f);
      const v2f z2 = cmul(z, z), cb = cmul((v2f){lo2f(wv[0]), lo2f(wv[1])}, (v2f){lo2f(xr[2]), lo2f(xr[3])} * d16);
      const v2f z4 = cmul(z2, z2), z8 = cmul(z4, z4);
      v2f o = {z8.x, -z8.y};
      const v2f m = cmul(o, o);
      v2f e = m;
      put1<0>(0x3f80u); put1<RP>(pk(o.x, o.y)); put1<2 * RP>(pk(e.x, e.y));
      brows<2>(e, o, m);
      v2f x1, y, c0, c1;
      step2<31 * RP, 31 * RP>(x1, cb, E, o, o, m);
      asm volatile("s_mov_b32 m0, %0" :: "s"(20000 + ch * AC + half * AH) : "memory");
#pragma unroll
      for (int k = 0; k < 8; k++) fb[k] = *(LDS v8bf*)(pb + k * 32);
      MEMB();
#define AL(r) (AOFF + (r) * RP)
#define AU(r) (AOFF + A_UP + (r) * RP)
      put1<AL(0)>(pk(cb.x, cb.y)); put1<AL(8)>(pk(x1.x, x1.y));
      step2<AL(1), AL(12)>(c1, cb, z, y, x1, z8);
      step2<AL(2), AU(2)>(c0, cb, z2, y, y, E);
      step2<AL(3), AU(3)>(c1, c1, z2, y, y, E);
      step2<AL(4), AU(4)>(c0, c0, z2, y, y, E);
      step2<AL(5), AU(5)>(c1, c1, z2, y, y, E);
      step2<AL(6), AU(6)>(c0, c0, z2, y, y, E);
      step2<AL(9), AL(10)>(c0, x1, z2, e, x1, z4);
      step2<AL(7), AU(7)>(c1, c1, z2, y, y, E);
      step2<AL(11), AU(0)>(c0, e, z2, e, e, E);
      e = cmul(e, E); put1<AU(1)>(pk(e.x, e.y));
    }
    STAMP(2);
    v16f e;
    if (half) {
      v4f q[4]; u32 f, sc, cnt;
      asm volatile("s_mov_b32 %6, 0x20000\n"
                   "1:\n\tds_read_b32 %4, %7\n\tds_read_b128 %0, %8\n\tds_read_b128 %1, %8 offset:1024\n\t"
                   "ds_read_b128 %2, %8 offset:2048\n\tds_read_b128 %3, %8 offset:3072\n\t"
                   "s_waitcnt lgkmcnt(4)\n\tv_readfirstlane_b32 %5, %4\n\ts_bitcmp1_b32 %5, %9\n\t"
                   "s_cbranch_scc1 2f\n\ts_sub_u32 %6, %6, 1\n\ts_cbranch_scc0 1b\n2:"
                   : "=&v"(q[0]), "=&v"(q[1]), "=&v"(q[2]), "=&v"(q[3]), "=&v"(f), "=&s"(sc), "=&s"(cnt)
                   : "v"(F + F_X), "v"(px), "s"(ch) : "memory", "scc");
#pragma unroll
      for (int i = 0; i < 16; i++) e[i] = q[i >> 2][i & 3];
    } else {
#pragma unroll
      for (int i = 0; i < 16; i++) e[i] = 0.f;
    }
    {
      v8bf fa[8];
#pragma unroll
      for (int k = 0; k < 8; k++) fa[k] = *(LDS v8bf*)(pa + k * 32);
      SB();
#pragma unroll
      for (int k = 0; k < 8; k++) e = __builtin_amdgcn_mfma_f32_32x32x16_bf16(fa[k], fb[k], e, 0, 0, 0);
    }
    const u32 umask = 15;
    if (!half) {
#pragma unroll
      for (int i = 0; i < 4; i++) *(LDS v4f*)(px + i * 1024) = (v4f){e[4 * i], e[4 * i + 1], e[4 * i + 2], e[4 * i + 3]};
      lds_sig(F + F_X, 1u << ch);
      LDS char* p = Ac + 32 + lane * 16;
      *(LDS u32*)(Ac + (lane & 7) * 4) = 0;
      v4u un; v2u pv;
      {
        u32 f, sc, cnt;
        asm volatile("s_mov_b32 %4, 0x20000\n"
                     "1:\n\tds_read_b32 %2, %5\n\tds_read_b128 %0, %6 offset:8\n\tds_read_b64 %1, %6\n\t"
                     "s_waitcnt lgkmcnt(2)\n\tv_readfirstlane_b32 %3, %2\n\ts_bitcmp1_b32 %3, %7\n\t"
                     "s_cbranch_scc1 2f\n\ts_sub_u32 %4, %4, 1\n\ts_cbranch_scc0 1b\n2:\n\tds_write_b32 %8, %9\n\ts_waitcnt lgkmcnt(1)"
                     : "=&v"(un), "=&v"(pv), "=&v"(f), "=&s"(sc), "=&s"(cnt)
                     : "v"(F + F_X), "v"(p - 8), "s"(ch + 4), "v"(p_q), "v"(0u) : "memory", "scc");
      }
      *(LDS u32*)(p_c + 1056) = __builtin_amdgcn_perm(__builtin_amdgcn_readlane(un[3], 63), __builtin_amdgcn_readlane(un[2], 63), psel);
      const u32 a0 = __builtin_amdgcn_alignbit(pv[1], pv[0], 16), a1 = __builtin_amdgcn_alignbit(un[0], pv[1], 16),
                a2 = __builtin_amdgcn_alignbit(un[1], un[0], 16), a3 = __builtin_amdgcn_alignbit(un[2], un[1], 16),
                a4 = __builtin_amdgcn_alignbit(un[3], un[2], 16);
      *(LDS v4u*)(p + 1344) = (v4u){a1, a2, a3, a4};
      *(LDS v4u*)(p + AH + 128) = (v4u){pv[1], un[0], un[1], un[2]};
      *(LDS v4u*)(p + AH + 128 + 1344) = (v4u){a0, a1, a2, a3};
      STAMP(3);
      u32 c0, c1, si = 0;
      asm volatile(
        LA8(40, 512) LA8(8, 0) LA(72, 0, 576)
        "v_mov_b32 v4, 0\n\tv_mov_b64 v[112:113], 0\n\tv_mov_b64 v[114:115], 0\n\ts_setprio 2\n\ts_mov_b32 %1, 0x20000\n" ST_(4)
        "1:\n\tds_read_b32 v5, v3\n\ts_waitcnt lgkmcnt(0)\n\tv_readfirstlane_b32 %0, v5\n\ts_andn2_b32 %0, %[msk], %0\n\t"
        "s_cbranch_scc0 2f\n\ts_sub_u32 %1, %1, 1\n\ts_cbranch_scc0 1b\n2:\n\t" ST_(5)
        LB8
        "s_cmp_eq_u32 %[h], 0\n\ts_cbranch_scc1 4f\n\ts_branch 6f\n"
        "3:\n\ts_waitcnt lgkmcnt(8)\n\tv_mfma_f32_16x16x32_bf16 " AX ", v[72:75], v[108:111], " AX "\n"
        "4:\n\t" H0(0, AX) H0(1, AX) H0(2, AX) H0(3, AX) H0(4, AX) H0(5, AX) H0(6, AX) H0(7, AY)
        "v_add_u32 v2, %0, v2\n\ts_movk_i32 %0, 0x400\n\tv_mov_b32 v4, %[bit]\n\ts_lshl_b32 %[bit], %[bit], 4\n\t"
        "s_add_u32 %2, %2, 1\n\ts_cmp_eq_u32 %2, 4\n\ts_cbranch_scc1 7f\n\t"
        "s_cmp_lg_u32 %2, %[jmp]\n\ts_cbranch_scc1 6f\n\t"
        "v_add_u32 v1, 0x800, v1\n\t" LB8
        "6:\n\tv_add_u32 v1, 0x200, v1\n\tds_read_b128 v[108:111], v1 offset:448\n\t"
        H1(0, "0") H1(1, AX) PUB H1(2, AX) H1(3, AX) H1(4, AX) H1(5, AX) H1(6, AX) H1(7, AX)
        "s_cmp_lg_u32 %2, %[one]\n\ts_cbranch_scc1 3b\n\ts_branch 4b\n"
        "7:\n\ts_nop 2\n\t" PUB ST_(6) FLUSH_
        "s_waitcnt lgkmcnt(0)\n\ts_endpgm"
        : "=&s"(c0), "=&s"(c1), "+s"(si)
        : "{v0}"(p_a), "{v1}"(p_u), "{v2}"(pw), "{v3}"(F + F_U), [msk] "s"(umask), [bit] "s"(1u << ch),
          [h] "s"(h), [jmp] "s"(h ? 9 : 2), [one] "s"(h ? 9 : 1)
        : "memory", "scc", "v127");
    }
    {
      LDS char* const T = Ac + A_HI;
      {
        *(LDS v2u*)p_c = (v2u){pk(e[0], e[1]), pk(e[2], e[3])};
        *(LDS v2u*)(p_c + 512) = (v2u){pk(e[4], e[5]), pk(e[6], e[7])};
        lds_sig(F + F_X, 16u << ch);
#pragma unroll
        for (int i = 0; i < 4; i++)
          asm volatile("ds_write_b16 %0, %1 offset:%2\n\tds_write_b16_d16_hi %0, %1 offset:%2+64"
                       :: "v"(p_q), "v"(pk(e[8 + 2 * i], e[9 + 2 * i])), "n"((i >> 1) * 512 + (i & 1) * 128) : "memory");
      }
      MEMB();
      LDS char* tp = p_a;
      v8bf ta[3], tb4[3], t8[2];
      {
        v2u q[16]; u32 kk;
        asm volatile(TB(0, 1, 1) TB(2, 3, 2) TB(4, 5, 3) TB(6, 7, 4) TB(8, 9, 5) TB(10, 11, 6) TB(12, 13, 7) TB(14, 15, 8)
                     "ds_read_b32 %16, %[t] offset:380\n\ts_waitcnt lgkmcnt(0)"
                     : "=&v"(q[0]), "=&v"(q[1]), "=&v"(q[2]), "=&v"(q[3]), "=&v"(q[4]), "=&v"(q[5]), "=&v"(q[6]), "=&v"(q[7]),
                       "=&v"(q[8]), "=&v"(q[9]), "=&v"(q[10]), "=&v"(q[11]), "=&v"(q[12]), "=&v"(q[13]), "=&v"(q[14]), "=&v"(q[15]), "=&v"(kk)
                     : [p] "v"(tp), [t] "v"(T) : "memory");
#pragma unroll
        for (int i = 0; i < 3; i++) { ta[i] = cat2(q[2 * i], q[2 * i + 1]); tb4[i] = cat2(q[6 + 2 * i], q[7 + 2 * i]); }
        t8[0] = cat2(q[12], q[13]); t8[1] = cat2(q[14], q[15]);
        kk = __builtin_amdgcn_perm(kk, kk, 0x03020302u);
#pragma unroll
        for (int j = 0; j < 4; j++) b34[j] &= kk;
      }
      asm volatile("" : "+v"(ta[0]), "+v"(ta[1]), "+v"(ta[2]), "+v"(tb4[0]), "+v"(tb4[1]), "+v"(tb4[2]), "+v"(t8[0]), "+v"(t8[1]), "+v"(b34));
      LDS char* pu = p_u;
      __builtin_amdgcn_s_setprio(2);
      STAMP(3);
      lds_wait<0>(F + F_U, umask);
      STAMP(4);
      const v4f zz = {0.f, 0.f, 0.f, 0.f};
      v8bf ua[4], ub[4];
#pragma unroll
      for (int a = 0; a < 4; a++) { ua[a] = *(LDS v8bf*)(pu + a * 512 + 16); ub[a] = *(LDS v8bf*)(pu + a * 512); }
      v4f r0 = zz, r1 = zz, r2 = zz, r3 = zz;
      v8bf wa0 = wop(ua[0], ub[0], b35, b35), wb0 = wop(ua[0], ub[0], b6a, b6a);
      v8bf wa1 = wop(ua[1], ub[1], b35, b35), wb1 = wop(ua[1], ub[1], b6a, b6a);
      if (h) {
        r0 = MF(wa0, ta[0], r0); r1 = MF(wa0, ta[1], r1); r0 = MF(wb0, tb4[0], r0); r1 = MF(wb0, tb4[1], r1);
        r1 = MF(wa1, ta[0], r1); r1 = MF(wb1, tb4[0], r1);
      }
      r2 = MF(wa0, ta[2], r2); r3 = MF(wa1, ta[2], r3); r2 = MF(wb0, tb4[2], r2); r3 = MF(wb1, tb4[2], r3);
      *(LDS v4f*)pw = r0;
      *(LDS v4f*)(pw + 1024) = r1;
      lds_sig(F + F_PH, 0x110000u << ch);
      r2 = MF(wa1, ta[1], r2); r2 = MF(wb1, tb4[1], r2);
      {
        v8bf wa2 = wop(ua[2], ub[2], b35, b35), wb2 = wop(ua[2], ub[2], b6a, b6a), w80 = wop(ua[0], ub[0], b5b, b4c);
        r2 = MF(BF8(b34), ua[0], r2); r3 = MF(BF8(b34), ua[1], r3);
        r2 = MF(wa2, ta[0], r2); r3 = MF(wa2, ta[1], r3); r2 = MF(wb2, tb4[0], r2); r3 = MF(wb2, tb4[1], r3);
        r2 = MF(w80, t8[0], r2); r3 = MF(w80, t8[1], r3);
      }
      if (!h) {
        v8bf va0 = *(LDS v8bf*)(pu - 1024 + 16), vb0 = *(LDS v8bf*)(pu - 1024), va1 = *(LDS v8bf*)(pu - 512 + 16), vb1 = *(LDS v8bf*)(pu - 512);
        v2u q[4];
        asm volatile(TB(0, 1, 9) TB(2, 3, 10) "s_waitcnt lgkmcnt(0)" : "=&v"(q[0]), "=&v"(q[1]), "=&v"(q[2]), "=&v"(q[3]) : [p] "v"(tp) : "memory");
        const v8bf t2 = cat2(q[0], q[1]), t3 = cat2(q[2], q[3]);
        v8bf x0 = wop(va0, vb0, b5b, b4c), x1 = wop(va1, vb1, b5b, b4c);
        r2 = MF(x0, t2, r2); r3 = MF(x0, t3, r3); r2 = MF(x1, t8[1], r2); r3 = MF(x1, t2, r3);
      }
      {
        v8bf wa3 = wop(ua[3], ub[3], b35, b35), wb3 = wop(ua[3], ub[3], b6a, b6a), w81 = wop(ua[1], ub[1], b5b, b4c);
        *(LDS v4f*)(pw + 2048) = r2;
        lds_sig(F + F_PH, 0x1000000u << ch);
        r3 = MF(wa3, ta[0], r3); r3 = MF(wb3, tb4[0], r3); r3 = MF(w81, t8[0], r3);
      }
      *(LDS v4f*)(pw + 3072) = r3;
      lds_sig(F + F_PH, 0x10000000u << ch);
      STAMP(5);
      STAMP(7);
      FLUSH();
    }
  }
}

extern "C" void kernel_launch(void* const* d_in, const int* in_sizes, int n_in, void* d_out, int out_size, void* d_ws,
                              size_t ws_size, hipStream_t stream) {
  kf<<<256, 1024, 0, stream>>>(d_in[0], d_in[10], d_ws, d_in[8], d_out, d_in[11], d_in[12], d_in[13],
                               d_in[1], d_in[2], d_in[3], d_in[4], d_in[5], d_in[6], d_in[7], d_ws);
}
```
